```python
import torch

def _softmax(x, axis=-1):
    return torch.softmax(x, dim=axis)
def _gelu(x):
    return torch.nn.functional.gelu(x, approximate="tanh")
def _silu(x):
    return torch.nn.functional.silu(x)
def _sigmoid(x):
    return torch.sigmoid(x)
def _layernorm(x, axis=-1, eps=1e-5):
    m = x.mean(dim=axis, keepdim=True)
    v = x.var(dim=axis, keepdim=True, unbiased=False)
    return (x - m) / torch.sqrt(v + eps)
def _scatter_add(tgt, idx, src, axis=0):
    return tgt.clone().index_add_(axis, idx, src)
def _segment_sum(src, seg, n):
    out = torch.zeros((n,) + src.shape[1:], dtype=src.dtype)
    return out.index_add_(0, seg, src)
def _pad(x, pw):
    flat = tuple(v for pair in reversed(pw) for v in pair)
    return torch.nn.functional.pad(x, flat)

kernel_name = 'parallel_sum__linear_kernel_hedgehog__mixer_token_mlp__prompt'

def setup_inputs():

    _w = (lambda a: (lambda t: t.to(torch.bfloat16) if t.is_floating_point() else t)(torch.as_tensor(a)))
    def _A_setup():
        B, S, D, H = 1, 2048, 448, 16
        Dh = D // H
        return {
            "x": _w((lambda *s: torch.randn(*s, dtype=torch.bfloat16) * 0.05)(B, S, D)),
            "wqkv": _w((lambda *s: torch.randn(*s, dtype=torch.bfloat16) * 0.2)(D, 3 * D)),
            "w_phi": _w((lambda *s: torch.randn(*s, dtype=torch.bfloat16) * 0.2)(H, Dh, Dh) * (Dh ** -0.5)),
            "wo": _w((lambda *s: torch.randn(*s, dtype=torch.bfloat16) * 0.05)(D, D)),
        }
    _A_in = _A_setup()

    def _B_setup():
        B, S, D, F = 1, 2048, 448, 1792
        return {
            "x": _w((lambda *s: torch.randn(*s, dtype=torch.bfloat16) * 0.05)(B, S, D)),
            "w1": _w((lambda *s: torch.randn(*s, dtype=torch.bfloat16) * 0.8)(S, F) * (S ** -0.5)),
            "b1": _w((lambda *s: torch.randn(*s, dtype=torch.bfloat16) * 3.2)(F) * 0.02),
            "w2": _w((lambda *s: torch.randn(*s, dtype=torch.bfloat16) * 0.4)(F, S) * (F ** -0.5)),
            "b2": _w((lambda *s: torch.randn(*s, dtype=torch.bfloat16) * 1.6)(S) * 0.02),
        }
    _B_in = _B_setup()

    return {
        "A_x": _A_in["x"],
        "A_wqkv": _A_in["wqkv"],
        "A_w_phi": _A_in["w_phi"],
        "A_wo": _A_in["wo"],
        "B_x": _B_in["x"],
        "B_w1": _B_in["w1"],
        "B_b1": _B_in["b1"],
        "B_w2": _B_in["w2"],
        "B_b2": _B_in["b2"],
    }

def reference(A_x, A_wqkv, A_w_phi, A_wo, B_x, B_w1, B_b1, B_w2, B_b2):
    def _A_ref(x, wqkv, w_phi, wo):
        B, S, D = x.shape
        H = 16; Dh = D // H
        qkv = (lambda x, *d: x.permute(*d))((x @ wqkv).reshape(B, S, 3, H, Dh), 2, 0, 3, 1, 4)
        q, k, v = qkv[0], qkv[1], qkv[2]
        pq = torch.einsum("bhsd,hdf->bhsf", q, w_phi)
        pk = torch.einsum("bhsd,hdf->bhsf", k, w_phi)
        phi_q = torch.concatenate([_softmax(pq, axis=-1), _softmax(-pq, axis=-1)], axis=-1)
        phi_k = torch.concatenate([_softmax(pk, axis=-1), _softmax(-pk, axis=-1)], axis=-1)
        kv = torch.einsum("bhsf,bhsd->bhfd", phi_k, v)
        z  = torch.einsum("bhsf->bhf", phi_k)
        num = torch.einsum("bhqf,bhfd->bhqd", phi_q, kv)
        den = torch.einsum("bhqf,bhf->bhq", phi_q, z)[..., None] + 1e-6
        o = num / den
        return (lambda x, *d: x.permute(*d))(o, 0, 2, 1, 3).reshape(B, S, D) @ wo

    def _B_ref(x, w1, b1, w2, b2):
        B, S, D = x.shape
        h = _layernorm(x, axis=-1)
        u = torch.einsum("bsd,sf->bdf", h, w1) + b1
        u = _gelu(u)
        y = torch.einsum("bdf,fs->bds", u, w2) + b2
        return (lambda x, *d: x.permute(*d))(y, 0, 2, 1)

    x = A_x
    xn = _layernorm(x, axis=-1)
    return x + _A_ref(xn, A_wqkv, A_w_phi, A_wo) + _B_ref(xn, B_w1, B_b1, B_w2, B_b2)
```

```cpp
#include <hip/hip_runtime.h>
#include <hip/hip_bf16.h>

typedef __attribute__((ext_vector_type(8))) __bf16 bf16x8;
typedef __attribute__((ext_vector_type(4))) __bf16 bf16x4;
typedef __attribute__((ext_vector_type(2))) __bf16 bf16x2;
typedef __attribute__((ext_vector_type(16))) float f32x16;
typedef __attribute__((ext_vector_type(4))) float f32x4;
typedef __attribute__((ext_vector_type(2))) float f32x2;
typedef __attribute__((ext_vector_type(4))) unsigned u32x4;
typedef __attribute__((ext_vector_type(2))) unsigned u32x2;
typedef __amdgpu_buffer_rsrc_t srd_t;
#define LAS __attribute__((address_space(3)))
typedef LAS char* lptr;
#define DEV __device__ __forceinline__

constexpr int S_ = 2048, D_ = 448, F_ = 1792;
constexpr unsigned HF_BYTES = 7 * 128 * 2 * 1024, PQ_BYTES = 16 * 64 * 4 * 1024, PART_BYTES = 16 * 16 * 8 * 1024;
constexpr unsigned UF_BYTES = 7 * 112 * 2 * 1024, OF_BYTES = 64 * 16 * 2 * 1024;
constexpr unsigned HF_OFF = 0, PQ_OFF = HF_OFF + HF_BYTES, PART_OFF = PQ_OFF + PQ_BYTES, UF_OFF = PART_OFF + PART_BYTES,
                   OF_OFF = UF_OFF + UF_BYTES;
constexpr float LOG2E = 1.4426950408889634f;

DEV srd_t mksrd(const void* p, unsigned bytes) { return __builtin_amdgcn_make_buffer_rsrc((void*)p, 0, bytes, 0x00020000); }
DEV unsigned ldsaddr(lptr p) { return (unsigned)(size_t)p; }
DEV int rfl(int v) { return __builtin_amdgcn_readfirstlane(v); }

template <int OFF> DEV void glds(unsigned voff, srd_t srd, unsigned soff) {
  asm volatile("buffer_load_dwordx4 %0, %1, %2 offen offset:%3 lds" ::"v"(voff), "s"(srd), "s"(soff), "n"(OFF) : "memory");
}
DEV void setm0(unsigned lds) { asm volatile("s_mov_b32 m0, %0\n\ts_nop 0" ::"s"(lds) : "memory"); }
template <int OFF> DEV void gld16(u32x4& d, unsigned voff, srd_t srd, unsigned soff) {
  asm volatile("buffer_load_dwordx4 %0, %1, %2, %3 offen offset:%4" : "=v"(d) : "v"(voff), "s"(srd), "s"(soff), "n"(OFF) : "memory");
}
template <int OFF> DEV void gld8(u32x2& d, unsigned voff, srd_t srd) {
  asm volatile("buffer_load_dwordx2 %0, %1, %2, 0 offen offset:%3" : "=v"(d) : "v"(voff), "s"(srd), "n"(OFF) : "memory");
}
DEV void gld2(unsigned& d, unsigned voff, srd_t srd) {
  asm volatile("buffer_load_ushort %0, %1, %2, 0 offen" : "=v"(d) : "v"(voff), "s"(srd) : "memory");
}
template <int N> DEV void wait_vm() { asm volatile("s_waitcnt vmcnt(%0)" ::"n"(N) : "memory"); }
template <int N> DEV void wait_bar() { asm volatile("s_waitcnt vmcnt(%0) lgkmcnt(0)\n\ts_barrier" ::"n"(N) : "memory"); }
DEV void lds_bar() { asm volatile("s_waitcnt lgkmcnt(0)\n\ts_barrier" ::: "memory"); }
DEV void lds_wait() { asm volatile("s_waitcnt lgkmcnt(0)" ::: "memory"); }

DEV bf16x8 trd(lptr p, int second) {
  bf16x4 a = __builtin_amdgcn_ds_read_tr16_b64_v4bf16((LAS bf16x4*)p);
  bf16x4 b = __builtin_amdgcn_ds_read_tr16_b64_v4bf16((LAS bf16x4*)(p + second));
  return __builtin_shufflevector(a, b, 0, 1, 2, 3, 4, 5, 6, 7);
}
DEV f32x16 mfma(bf16x8 a, bf16x8 b, f32x16 c) { return __builtin_amdgcn_mfma_f32_32x32x16_bf16(a, b, c, 0, 0, 0); }
DEV unsigned pk2(float a, float b) {
  f32x2 v = {a, b};
  return __builtin_bit_cast(unsigned, __builtin_convertvector(v, bf16x2));
}
DEV bf16x8 asbf(u32x4 v) { return __builtin_bit_cast(bf16x8, v); }
DEV u32x4 pack8(const f32x16& v, int s) {
  u32x4 r = {pk2(v[8 * s], v[8 * s + 1]), pk2(v[8 * s + 2], v[8 * s + 3]), pk2(v[8 * s + 4], v[8 * s + 5]), pk2(v[8 * s + 6], v[8 * s + 7])};
  return r;
}
DEV float bflo(unsigned u) { return __builtin_bit_cast(float, u << 16); }
DEV float bfhi(unsigned u) { return __builtin_bit_cast(float, u & 0xffff0000u); }
DEV float dot2(unsigned a, unsigned b, float c) {
  return __builtin_amdgcn_fdot2_f32_bf16(__builtin_bit_cast(bf16x2, a), __builtin_bit_cast(bf16x2, b), c, false);
}
DEV f32x2 swap32(float a, float b) {
  auto r = __builtin_amdgcn_permlane32_swap(__builtin_bit_cast(unsigned, a), __builtin_bit_cast(unsigned, b), false, false);
  const unsigned r0 = r[0], r1 = r[1];
  f32x2 o = {__builtin_bit_cast(float, r0), __builtin_bit_cast(float, r1)};
  return o;
}
DEV float xsum32(float v) { f32x2 r = swap32(v, v); return r[0] + r[1]; }
template <int CTRL> DEV float dppadd(float v) {
  return v + __builtin_bit_cast(float, __builtin_amdgcn_update_dpp(0, __builtin_bit_cast(int, v), CTRL, 0xf, 0xf, true));
}
DEV float halfsum(float v) {
  v = dppadd<0xB1>(v);
  v = dppadd<0x4E>(v);
  v = dppadd<0x141>(v);
  v = dppadd<0x140>(v);
  auto r = __builtin_amdgcn_permlane16_swap(__builtin_bit_cast(unsigned, v), __builtin_bit_cast(unsigned, v), false, false);
  const unsigned r0 = r[0], r1 = r[1];
  return __builtin_bit_cast(float, r0) + __builtin_bit_cast(float, r1);
}

DEV void dual_softmax(const f32x16& p, bool hi, f32x16& ep, f32x16& en) {
  float t[4];
#pragma unroll
  for (int j = 0; j < 4; j++) t[j] = hi ? p[8 + j] : p[12 + j];
  float mx = fmaxf(fmaxf(t[0], t[1]), fmaxf(t[2], t[3])), mn = fminf(fminf(t[0], t[1]), fminf(t[2], t[3]));
#pragma unroll
  for (int i = 0; i < 12; i += 2) {
    mx = fmaxf(mx, fmaxf(p[i], p[i + 1]));
    mn = fminf(mn, fminf(p[i], p[i + 1]));
  }
  f32x2 a = swap32(mx, mx), b = swap32(mn, mn);
  const float bp = -fmaxf(a[0], a[1]) * LOG2E, bn = fminf(b[0], b[1]) * LOG2E;
  const float bp2 = hi ? -__builtin_inff() : bp, bn2 = hi ? -__builtin_inff() : bn;
  float sp0 = 0.f, sp1 = 0.f, sn0 = 0.f, sn1 = 0.f;
#pragma unroll
  for (int i = 0; i < 16; i++) {
    ep[i] = __builtin_amdgcn_exp2f(__builtin_fmaf(p[i], LOG2E, i < 12 ? bp : bp2));
    en[i] = __builtin_amdgcn_exp2f(__builtin_fmaf(p[i], -LOG2E, i < 12 ? bn : bn2));
    if (i & 1) { sp1 += ep[i]; sn1 += en[i]; } else { sp0 += ep[i]; sn0 += en[i]; }
  }
  const float rp = __builtin_amdgcn_rcpf(xsum32(sp0 + sp1)), rn = __builtin_amdgcn_rcpf(xsum32(sn0 + sn1));
  ep *= rp;
  en *= rn;
}

constexpr int K1_W = 0, K1_X = 86016, K1_PHI = K1_X + 65536, K1_CS = K1_PHI + 2048, K1_R = K1_CS + 1536, K1_LN = K1_R + 512,
              K1_LDS = K1_LN + 8 * 960;
constexpr int K1_KT = 0, K1_RED = 32768;

template <int C> struct K1Wait { static constexpr int v = C == 0 ? 30 : C == 1 ? 29 : C == 2 ? 26 : C == 3 ? 23 : C == 4 ? 8 : C == 5 ? 4 : 0; };

struct K1State {
  f32x16 aq, ak, av;
  float s0, s1, q0, q1, cq, ck, cv;
};

template <int C>
DEV void k1_chunk(K1State& st, const lptr (&xb)[4], const lptr (&wb)[4]) {
  constexpr unsigned ONES = 0x3F803F80u;
#pragma unroll
  for (int i = 0; i < 4; i++) {
    const u32x4 xf = *(LAS u32x4*)(xb[i] + (C & 3) * 4096);
    const bf16x8 wq = trd(wb[i] + C * 12288, 256), wk = trd(wb[i] + C * 12288 + 4096, 256), wv = trd(wb[i] + C * 12288 + 8192, 256);
    st.aq = mfma(wq, asbf(xf), st.aq);
    st.ak = mfma(wk, asbf(xf), st.ak);
    st.av = mfma(asbf(xf), wv, st.av);
    st.s0 = dot2(xf[0], ONES, st.s0); st.s1 = dot2(xf[1], ONES, st.s1);
    st.q0 = dot2(xf[0], xf[0], st.q0); st.q1 = dot2(xf[1], xf[1], st.q1);
    st.s0 = dot2(xf[2], ONES, st.s0); st.s1 = dot2(xf[3], ONES, st.s1);
    st.q0 = dot2(xf[2], xf[2], st.q0); st.q1 = dot2(xf[3], xf[3], st.q1);
    if (i == 0) {
      const u32x4 a = __builtin_bit_cast(u32x4, wq), b = __builtin_bit_cast(u32x4, wk), c = __builtin_bit_cast(u32x4, wv);
#pragma unroll
      for (int j = 0; j < 4; j++) { st.cq = dot2(a[j], ONES, st.cq); st.ck = dot2(b[j], ONES, st.ck); st.cv = dot2(c[j], ONES, st.cv); }
    }
  }
}

DEV u32x4 hilo_w(float v, bool hi) {
  const unsigned h2 = pk2(v, v);
  const float lo = v - bflo(h2);
  u32x4 r = {hi ? 0u : h2, hi ? 0u : pk2(lo, lo), 0u, 0u};
  return r;
}

__global__ __launch_bounds__(256, 1) void k1(const void* __restrict__ x, const void* __restrict__ wqkv, const void* __restrict__ wphi,
                                             char* __restrict__ ws) {
  __shared__ __attribute__((aligned(16))) char smem_[K1_LDS];
  const lptr sm = (lptr)smem_;
  const int tid = threadIdx.x, lane = tid & 63, w = rfl(tid >> 6);
  const int b = blockIdx.x, xcd = b & 7, idx = b >> 3;
  const int rt = (xcd >> 1) * 4 + (idx >> 3), hd = (xcd & 1) * 8 + (idx & 7);
  const int rot = (idx & 7) == 7 ? 0 : (idx & 7);
  const int r = lane & 31, h = lane >> 5;
  const bool hi = h != 0;
  const srd_t sx = mksrd(x, S_ * D_ * 2), sw = mksrd(wqkv, D_ * 3 * D_ * 2), sp = mksrd(wphi, 16 * 28 * 28 * 2);

  u32x4 l0, l1;
  u32x2 ph;
  {
    const unsigned lv = r < 28 ? (unsigned)((rt * 128 + hd * 8 + 2 * w + h) * 896 + r * 16) : 0x80000000u;
    gld16<0>(l0, lv, sx, 0);
    gld16<448>(l1, lv, sx, 0);
    const int pr = tid >> 3, pc = tid & 7;
    gld8<0>(ph, (pr < 28 && pc < 7) ? (unsigned)(hd * 1568 + pr * 56 + pc * 8) : 0x80000000u, sp);
  }
  unsigned xv[4];
#pragma unroll
  for (int i = 0; i < 4; i++) {
    const int row = 8 * i + (lane >> 3);
    xv[i] = (rt * 128 + w * 32 + row) * 896 + (((lane & 7) ^ ((row >> 1) & 7)) << 4) - i * 1024;
  }
  const unsigned wv0 = (16 * w + (lane >> 2)) * 2688 + ((56 * hd) & ~15) + (lane & 3) * 16;
  const unsigned xl = ldsaddr(sm) + K1_X + w * 16384, wl = ldsaddr(sm) + K1_W + w * 1024;
  auto issueX = [&](int c) {
    int cc = c + rot; cc = cc >= 7 ? cc - 7 : cc;
    setm0(xl + (c & 3) * 4096);
    glds<0>(xv[0], sx, cc * 128); glds<1024>(xv[1], sx, cc * 128); glds<2048>(xv[2], sx, cc * 128); glds<3072>(xv[3], sx, cc * 128);
  };
  auto issueW = [&](int c) {
    int cc = c + rot; cc = cc >= 7 ? cc - 7 : cc;
#pragma unroll
    for (int m = 0; m < 3; m++) { setm0(wl + c * 12288 + m * 4096); glds<0>(wv0, sw, cc * (64 * 2688) + m * 896); }
  };
#pragma unroll
  for (int c = 0; c < 7; c++) { if (c < 4) issueX(c); issueW(c); }

  asm volatile("s_waitcnt vmcnt(37)" : "+v"(l0), "+v"(l1), "+v"(ph)::"memory");
  {
    float v[16];
#pragma unroll
    for (int j = 0; j < 4; j++) { v[2 * j] = bflo(l0[j]); v[2 * j + 1] = bfhi(l0[j]); v[8 + 2 * j] = bflo(l1[j]); v[9 + 2 * j] = bfhi(l1[j]); }
    const float valid = r < 28 ? 1.f : 0.f;
    float s = 0.f;
#pragma unroll
    for (int j = 0; j < 16; j++) s += v[j];
    const float m = halfsum(s) * (1.f / D_);
    float q = 0.f;
#pragma unroll
    for (int j = 0; j < 16; j++) { v[j] -= m; q = __builtin_fmaf(v[j], v[j], q); }
    const float rs = valid * __builtin_amdgcn_rsqf(halfsum(q * valid) * (1.f / D_) + 1e-5f);
    float s2 = 0.f, q2 = 0.f;
#pragma unroll
    for (int j = 0; j < 16; j += 2) {
      const unsigned u = pk2(v[j] * rs, v[j + 1] * rs);
      v[j] = bflo(u); v[j + 1] = bfhi(u);
      s2 += v[j] + v[j + 1];
      q2 = dot2(u, u, q2);
    }
    const float m2 = halfsum(s2) * (1.f / D_);
    const float rs2 = __builtin_amdgcn_rsqf(fmaxf(halfsum(q2) * (1.f / D_) - m2 * m2, 0.f) + 1e-5f);
    u32x4 o0, o1;
#pragma unroll
    for (int j = 0; j < 4; j++) {
      o0[j] = pk2((v[2 * j] - m2) * rs2, (v[2 * j + 1] - m2) * rs2);
      o1[j] = pk2((v[8 + 2 * j] - m2) * rs2, (v[9 + 2 * j] - m2) * rs2);
    }
    if (r < 28) {
      *(LAS u32x4*)(sm + K1_LN + (2 * w + h) * 960 + r * 16) = o0;
      *(LAS u32x4*)(sm + K1_LN + (2 * w + h) * 960 + 448 + r * 16) = o1;
    }
    *(LAS u32x2*)(sm + K1_PHI + tid * 8) = ph;
  }

  const int g1 = (lane >> 4) & 1, q4 = (lane >> 2) & 3, p4 = lane & 3;
  lptr xb[4], wb[4];
#pragma unroll
  for (int i = 0; i < 4; i++) {
    const int t = i ^ w;
    xb[i] = sm + K1_X + w * 16384 + r * 128 + (((2 * t + h) ^ ((r >> 1) & 7)) << 4);
    wb[i] = sm + K1_W + t * 1024 + (8 * h + q4) * 64 + min((hd & 1) * 8 + 32 * g1 + 8 * p4, 56);
  }
  K1State st;
#pragma unroll
  for (int i = 0; i < 16; i++) { st.aq[i] = 0.f; st.ak[i] = 0.f; st.av[i] = 0.f; }
  st.s0 = st.s1 = st.q0 = st.q1 = st.cq = st.ck = st.cv = 0.f;

  wait_bar<K1Wait<0>::v>();
  {
    const int ks = rt * 8 + (hd >> 1);
#pragma unroll
    for (int n = 0; n < 2; n++) {
      const int mt = n == 0 ? w : (w < 3 ? w + 4 : w);
      const bf16x8 t = trd(sm + K1_LN + q4 * 960 + mt * 128 + (lane >> 4) * 32 + p4 * 8, 4 * 960);
      *(u32x4*)(ws + HF_OFF + ((mt * 128 + ks) * 2 + h) * 1024 + ((hd & 1) * 32 + r) * 16) = __builtin_bit_cast(u32x4, t);
    }
  }
  k1_chunk<0>(st, xb, wb); lds_wait(); issueX(4);
  wait_bar<K1Wait<1>::v>(); k1_chunk<1>(st, xb, wb); lds_wait(); issueX(5);
  wait_bar<K1Wait<2>::v>(); k1_chunk<2>(st, xb, wb); lds_wait(); issueX(6);
  wait_bar<K1Wait<3>::v>(); k1_chunk<3>(st, xb, wb);
  wait_bar<K1Wait<4>::v>(); k1_chunk<4>(st, xb, wb);
  wait_bar<K1Wait<5>::v>(); k1_chunk<5>(st, xb, wb);
  wait_bar<K1Wait<6>::v>(); k1_chunk<6>(st, xb, wb);

  const float mean = xsum32(st.s0 + st.s1) * (1.f / D_);
  const float rstd = __builtin_amdgcn_rsqf(fmaxf(xsum32(st.q0 + st.q1) * (1.f / D_) - mean * mean, 0.f) + 1e-5f);
  {
    const float cq = xsum32(st.cq), ck = xsum32(st.ck), cv = xsum32(st.cv);
    if (!hi) {
      *(LAS float*)(sm + K1_CS + r * 16 + w * 4) = cq;
      *(LAS float*)(sm + K1_CS + 512 + r * 16 + w * 4) = ck;
      *(LAS float*)(sm + K1_CS + 1024 + r * 16 + w * 4) = cv;
      *(LAS float*)(sm + K1_R + w * 128 + r * 4) = rstd;
    }
  }
  lds_bar();
  {
    const f32x4 a = *(LAS f32x4*)(sm + K1_CS + r * 16), c = *(LAS f32x4*)(sm + K1_CS + 512 + r * 16), d = *(LAS f32x4*)(sm + K1_CS + 1024 + r * 16);
    const unsigned mh = pk2(-mean, 0.f);
    const unsigned mm = pk2(-mean, -mean - bflo(mh));
    const u32x4 xm = {mm, mm, 0u, 0u};
    st.aq = mfma(asbf(hilo_w((a[0] + a[1]) + (a[2] + a[3]), hi)), asbf(xm), st.aq);
    st.ak = mfma(asbf(hilo_w((c[0] + c[1]) + (c[2] + c[3]), hi)), asbf(xm), st.ak);
    st.av = mfma(asbf(xm), asbf(hilo_w((d[0] + d[1]) + (d[2] + d[3]), hi)), st.av);
  }
  const lptr pp = sm + K1_PHI + (4 * h + q4) * 64 + 32 * g1 + 8 * p4;
  const bf16x8 f0 = trd(pp, 512), f1 = trd(pp + 1024, 512);
  f32x16 z16;
#pragma unroll
  for (int i = 0; i < 16; i++) z16[i] = 0.f;
  st.ak *= rstd;
  st.aq *= rstd;
  const f32x16 pk = mfma(f1, asbf(pack8(st.ak, 1)), mfma(f0, asbf(pack8(st.ak, 0)), z16));
  const f32x16 pq = mfma(f1, asbf(pack8(st.aq, 1)), mfma(f0, asbf(pack8(st.aq, 0)), z16));

  f32x16 ep, en;
  dual_softmax(pk, hi, ep, en);
  const lptr kt = sm + K1_KT + w * (32 * 136);
#pragma unroll
  for (int g = 0; g < 4; g++) {
    const u32x2 a = {pk2(ep[4 * g], ep[4 * g + 1]), pk2(ep[4 * g + 2], ep[4 * g + 3])};
    const u32x2 c = {pk2(en[4 * g], en[4 * g + 1]), pk2(en[4 * g + 2], en[4 * g + 3])};
    *(LAS u32x2*)(kt + r * 136 + 16 * g + 8 * h) = a;
    *(LAS u32x2*)(kt + r * 136 + 64 + 16 * g + 8 * h) = c;
  }
#pragma unroll
  for (int g = 0; g < 4; g++) {
    const f32x4 rr = *(LAS f32x4*)(sm + K1_R + w * 128 + 32 * g + 16 * h);
#pragma unroll
    for (int j = 0; j < 4; j++) st.av[4 * g + j] = r == 28 ? 1.f : st.av[4 * g + j] * rr[j];
  }
  const u32x4 v0 = pack8(st.av, 0), v1 = pack8(st.av, 1);
  lds_wait();
  const lptr kr = kt + (4 * h + q4) * 136 + 32 * g1 + 8 * p4;
  const f32x16 kvp = mfma(trd(kr + 16 * 136, 8 * 136), asbf(v1), mfma(trd(kr, 8 * 136), asbf(v0), z16));
  const f32x16 kvn = mfma(trd(kr + 16 * 136 + 64, 8 * 136), asbf(v1), mfma(trd(kr + 64, 8 * 136), asbf(v0), z16));

  dual_softmax(pq, hi, ep, en);
  {
    char* o = ws + PQ_OFF + (hd * 64 + rt * 4 + w) * 4096 + lane * 16;
    *(u32x4*)(o) = pack8(ep, 0);
    *(u32x4*)(o + 1024) = pack8(ep, 1);
    *(u32x4*)(o + 2048) = pack8(en, 0);
    *(u32x4*)(o + 3072) = pack8(en, 1);
  }
  const lptr rd = sm + K1_RED + lane * 16;
#pragma unroll
  for (int g = 0; g < 4; g++) {
    const f32x4 a = {kvp[4 * g], kvp[4 * g + 1], kvp[4 * g + 2], kvp[4 * g + 3]};
    const f32x4 c = {kvn[4 * g], kvn[4 * g + 1], kvn[4 * g + 2], kvn[4 * g + 3]};
    *(LAS f32x4*)(rd + w * 8192 + g * 1024) = a;
    *(LAS f32x4*)(rd + w * 8192 + 4096 + g * 1024) = c;
  }
  lds_bar();
#pragma unroll
  for (int n = 0; n < 2; n++) {
    const lptr p = rd + (2 * w + n) * 1024;
    const f32x4 t = (*(LAS f32x4*)(p) + *(LAS f32x4*)(p + 8192)) + (*(LAS f32x4*)(p + 16384) + *(LAS f32x4*)(p + 24576));
    *(f32x4*)(ws + PART_OFF + ((hd * 16 + rt) * 8 + 2 * w + n) * 1024 + lane * 16) = t;
  }
}

constexpr int DW = 8, DA = 6, G_RING = DW * 4096, G_LDS = 4 * G_RING;

template <int NS> constexpr int vm_wait(int t) {
  int after = 0;
  for (int s = 0; s < (DW > DA ? DW : DA); s++) {
    if (s < DA) after = s == t ? 0 : after + 4;
    if (s < DW) after = s == t ? 0 : after + 4;
  }
  for (int u = 0; u < t; u++) {
    if (u + DA < NS) after = u + DA == t ? 0 : after + 4;
    if (u + DW < NS) after = u + DW == t ? 0 : after + 4;
  }
  return after;
}

struct StepSrc { srd_t a, w; unsigned aoff, woff; };

template <int NS, int NMAIN, bool LDS_IS_A, int SECOND, class Src, int T = 0>
struct GemmLoop {
  static DEV void issueA(int s, u32x4 (&act)[DA][4], const unsigned (&av)[2], const unsigned (&av2)[2], const Src& src) {
    const StepSrc q = src(s);
    u32x4(&d)[4] = act[s % DA];
    if (s < NMAIN) { gld16<0>(d[0], av[0], q.a, q.aoff); gld16<0>(d[1], av[1], q.a, q.aoff); gld16<2048>(d[2], av[0], q.a, q.aoff); gld16<2048>(d[3], av[1], q.a, q.aoff); }
    else { gld16<0>(d[0], av2[0], q.a, q.aoff); gld16<0>(d[1], av2[1], q.a, q.aoff); gld16<1024>(d[2], av2[0], q.a, q.aoff); gld16<1024>(d[3], av2[1], q.a, q.aoff); }
  }
  static DEV void issueW(int s, unsigned ring, const unsigned (&wv)[4], const unsigned (&wv2)[4], const Src& src) {
    const StepSrc q = src(s);
    setm0(ring + (s % DW) * 4096);
    if (s < NMAIN) { glds<0>(wv[0], q.w, q.woff); glds<1024>(wv[1], q.w, q.woff); glds<2048>(wv[2], q.w, q.woff); glds<3072>(wv[3], q.w, q.woff); }
    else { glds<0>(wv2[0], q.w, q.woff); glds<1024>(wv2[1], q.w, q.woff); glds<2048>(wv2[2], q.w, q.woff); glds<3072>(wv2[3], q.w, q.woff); }
  }
  static DEV void run(f32x16 (&acc)[2][2], u32x4 (&act)[DA][4], const lptr (&tr)[2], const lptr (&tr2)[2], unsigned ring,
                      const unsigned (&av)[2], const unsigned (&av2)[2], const unsigned (&wv)[4], const unsigned (&wv2)[4], const Src& src) {
    if constexpr (T == 0) {
#pragma unroll
      for (int s = 0; s < (DW > DA ? DW : DA); s++) {
        if (s < DA) issueA(s, act, av, av2, src);
        if (s < DW) issueW(s, ring, wv, wv2, src);
      }
    }
    if constexpr (T < NS) {
      u32x4(&d)[4] = act[T % DA];
      asm volatile("s_waitcnt vmcnt(%4)" : "+v"(d[0]), "+v"(d[1]), "+v"(d[2]), "+v"(d[3]) : "n"(vm_wait<NS>(T)) : "memory");
      constexpr bool la = (T < NMAIN) ? LDS_IS_A : !LDS_IS_A;
      const lptr t0 = (T < NMAIN ? tr[0] : tr2[0]) + (T % DW) * 4096, t1 = (T < NMAIN ? tr[1] : tr2[1]) + (T % DW) * 4096;
      const bf16x8 m00 = trd(t0, SECOND), m01 = trd(t1, SECOND), m10 = trd(t0 + 2048, SECOND), m11 = trd(t1 + 2048, SECOND);
      if constexpr (la) {
        acc[0][0] = mfma(m00, asbf(d[0]), acc[0][0]); acc[0][1] = mfma(m00, asbf(d[1]), acc[0][1]);
        acc[1][0] = mfma(m01, asbf(d[0]), acc[1][0]); acc[1][1] = mfma(m01, asbf(d[1]), acc[1][1]);
        acc[0][0] = mfma(m10, asbf(d[2]), acc[0][0]); acc[0][1] = mfma(m10, asbf(d[3]), acc[0][1]);
        acc[1][0] = mfma(m11, asbf(d[2]), acc[1][0]); acc[1][1] = mfma(m11, asbf(d[3]), acc[1][1]);
      } else {
        acc[0][0] = mfma(asbf(d[0]), m00, acc[0][0]); acc[0][1] = mfma(asbf(d[0]), m01, acc[0][1]);
        acc[1][0] = mfma(asbf(d[1]), m00, acc[1][0]); acc[1][1] = mfma(asbf(d[1]), m01, acc[1][1]);
        acc[0][0] = mfma(asbf(d[2]), m10, acc[0][0]); acc[0][1] = mfma(asbf(d[2]), m11, acc[0][1]);
        acc[1][0] = mfma(asbf(d[3]), m10, acc[1][0]); acc[1][1] = mfma(asbf(d[3]), m11, acc[1][1]);
      }
      if constexpr (T + DA < NS) issueA(T + DA, act, av, av2, src);
      if constexpr (T + DW < NS) { lds_wait(); issueW(T + DW, ring, wv, wv2, src); }
      GemmLoop<NS, NMAIN, LDS_IS_A, SECOND, Src, T + 1>::run(acc, act, tr, tr2, ring, av, av2, wv, wv2, src);
    }
  }
};

DEV f32x16 wave_reduce(f32x16 (&acc)[2][2], lptr sm, int w, int lane) {
  lds_wait();
  const lptr own = sm + w * G_RING + lane * 16;
#pragma unroll
  for (int x = 1; x < 4; x++)
#pragma unroll
    for (int g = 0; g < 4; g++) {
      const f32x16& a = acc[x >> 1][x & 1];
      const f32x4 v = {a[4 * g], a[4 * g + 1], a[4 * g + 2], a[4 * g + 3]};
      *(LAS f32x4*)(own + x * 4096 + g * 1024) = v;
    }
  lds_bar();
  f32x16 o = acc[0][0];
#pragma unroll
  for (int x = 1; x < 4; x++)
#pragma unroll
    for (int g = 0; g < 4; g++) {
      const f32x4 v = *(LAS f32x4*)(sm + (w ^ x) * G_RING + lane * 16 + x * 4096 + g * 1024);
#pragma unroll
      for (int j = 0; j < 4; j++) o[4 * g + j] += v[j];
    }
  return o;
}

struct Src1 {
  srd_t a, w;
  int k0, rot;
  unsigned abase;
  DEV StepSrc operator()(int s) const {
    const int kk = k0 + ((s + rot) & 15);
    StepSrc q = {a, w, abase + kk * 4096u, kk * (32u * F_ * 2)};
    return q;
  }
};

DEV void attn_readout(const char* __restrict__ wsr, char* __restrict__ ws, lptr sm, int hd, int half, int w, int lane) {
  const bool hi = lane >= 32;
  const char* pq = wsr + PQ_OFF + (hd * 64 + half * 32 + w * 8) * 4096 + lane * 16;
  const char* pp = wsr + PART_OFF + (hd * 128 + 2 * w) * 1024 + lane * 16;
  f32x4 a = *(const f32x4*)pp, c = *(const f32x4*)(pp + 1024);
#pragma unroll
  for (int t = 1; t < 16; t++) { a += *(const f32x4*)(pp + t * 8192); c += *(const f32x4*)(pp + t * 8192 + 1024); }
  u32x4 ph[8][4];
#pragma unroll
  for (int t = 0; t < 8; t++)
#pragma unroll
    for (int s = 0; s < 4; s++) ph[t][s] = *(const u32x4*)(pq + t * 4096 + s * 1024);
  const u32x4 kf = {pk2(a[0], a[1]), pk2(a[2], a[3]), pk2(c[0], c[1]), pk2(c[2], c[3])};
  *(LAS u32x4*)(sm + w * 1024 + lane * 16) = kf;
  lds_bar();
  u32x4 kv[4];
#pragma unroll
  for (int s = 0; s < 4; s++) kv[s] = *(LAS u32x4*)(sm + s * 1024 + lane * 16);
#pragma unroll
  for (int t = 0; t < 8; t++) {
    f32x16 n;
#pragma unroll
    for (int i = 0; i < 16; i++) n[i] = 0.f;
#pragma unroll
    for (int s = 0; s < 4; s++) n = mfma(asbf(kv[s]), asbf(ph[t][s]), n);
    const float inv = __builtin_amdgcn_rcpf(swap32(n[12], n[12])[1] + 1e-6f);
    n *= inv;
#pragma unroll
    for (int i = 12; i < 16; i++) n[i] = hi ? 0.f : n[i];
    char* o = ws + OF_OFF + ((half * 32 + w * 8 + t) * 16 + hd) * 2048 + lane * 16;
    *(u32x4*)o = pack8(n, 0);
    *(u32x4*)(o + 1024) = pack8(n, 1);
  }
}

__global__ __launch_bounds__(256, 1) void k2(const void* __restrict__ w1, const void* __restrict__ b1, char* __restrict__ ws) {
  __shared__ __attribute__((aligned(16))) char smem_[G_LDS];
  const lptr sm = (lptr)smem_;
  const int tid = threadIdx.x, lane = tid & 63, w = rfl(tid >> 6);
  const int b = blockIdx.x, xcd = b & 7, idx = b >> 3;
  const int ntile = 24 + ((xcd & 1) ^ 1);
  if (idx >= ntile) {
    if (idx >= 28) attn_readout(ws, ws, sm, xcd * 2 + ((idx - 28) >> 1), idx & 1, w, lane);
    return;
  }
  const int tile = ((xcd * 49 + 1) >> 1) + idx;
  const int ft = tile / 7, mt = tile - ft * 7;
  const int wi = w >> 1, wj = w & 1, h = lane >> 5;
  const srd_t sb = mksrd(b1, F_ * 2);
  u32x2 bb[4];
  {
    const unsigned bv = (ft * 64 + wi * 32 + 4 * h) * 2;
    gld8<0>(bb[0], bv, sb); gld8<16>(bb[1], bv, sb); gld8<32>(bb[2], bv, sb); gld8<48>(bb[3], bv, sb);
  }
  const Src1 src = {mksrd(ws + HF_OFF, HF_BYTES), mksrd(w1, S_ * F_ * 2), w * 16, (mt * 7) / 3, mt * 128u * 2048u};
  const unsigned av[2] = {lane * 16u + wj * 1024u, lane * 16u + (wj ^ 1) * 1024u};
  unsigned wv[4];
#pragma unroll
  for (int i = 0; i < 4; i++) wv[i] = (8 * i + (lane >> 3)) * (F_ * 2) + ft * 128 + (((lane & 7) ^ (((lane >> 4) & 1) << 2)) << 4) - i * 1024;
  const int q4 = (lane >> 2) & 3;
  const int cb = (wi * 64 + ((lane >> 4) & 1) * 32 + (lane & 3) * 8) ^ ((q4 >> 1) << 6);
  const lptr tb = sm + w * G_RING + (8 * h + q4) * 128;
  const lptr tr[2] = {tb + cb, tb + (cb ^ 64)};
  f32x16 acc[2][2];
#pragma unroll
  for (int i = 0; i < 16; i++) acc[0][0][i] = acc[0][1][i] = acc[1][0][i] = acc[1][1][i] = 0.f;
  u32x4 act[DA][4];
  GemmLoop<16, 16, true, 512, Src1>::run(acc, act, tr, tr, ldsaddr(sm) + w * G_RING, av, av, wv, wv, src);
  asm volatile("" : "+v"(bb[0]), "+v"(bb[1]), "+v"(bb[2]), "+v"(bb[3]));
  f32x16 u = wave_reduce(acc, sm, w, lane);
#pragma unroll
  for (int i = 0; i < 16; i++) {
    const unsigned bw = bb[i >> 2][(i >> 1) & 1];
    const float y = u[i] + ((i & 1) ? bfhi(bw) : bflo(bw));
    const float z = y * __builtin_fmaf(y * y, -2.f * 0.7978845608028654f * 0.044715f * LOG2E, -2.f * 0.7978845608028654f * LOG2E);
    u[i] = y * __builtin_amdgcn_rcpf(1.f + __builtin_amdgcn_exp2f(z));
  }
  char* o = ws + UF_OFF + ((mt * 112 + ft * 4 + wi * 2) * 2 + wj) * 1024 + lane * 16;
  *(u32x4*)o = pack8(u, 0);
  *(u32x4*)(o + 2048) = pack8(u, 1);
}

struct Src2 {
  srd_t a, w, a2, w2;
  int wv, rot;
  unsigned abase, a2base;
  DEV StepSrc operator()(int s) const {
    if (s < 14) {
      int k = s + rot;
      k = (k >= 14 ? k - 14 : k) + wv * 14;
      StepSrc q = {a, w, abase + k * 4096u, k * (32u * S_ * 2)};
      return q;
    }
    const int hd = wv * 4 + ((s + rot) & 3);
    StepSrc q = {a2, w2, a2base + hd * 2048u, hd * (28u * D_ * 2)};
    return q;
  }
};

__global__ __launch_bounds__(256, 1) void k3(const void* __restrict__ x, const void* __restrict__ w2, const void* __restrict__ b2,
                                             const void* __restrict__ wo, const char* __restrict__ ws, char* __restrict__ out) {
  __shared__ __attribute__((aligned(16))) char smem_[G_LDS];
  const lptr sm = (lptr)smem_;
  const int tid = threadIdx.x, lane = tid & 63, w = rfl(tid >> 6);
  const int b = blockIdx.x, xcd = b & 7, idx = b >> 3;
  const int stl = idx / 7, mt = idx - stl * 7, st = xcd * 4 + stl;
  const int wi = w >> 1, wj = w & 1, h = lane >> 5, r = lane & 31;
  const unsigned ov = (st * 64 + wj * 32 + r) * 896 + (mt * 64 + wi * 32 + 8 * h) * 2;
  u32x4 xr0, xr1;
  unsigned bs;
  {
    const srd_t sx = mksrd(x, S_ * D_ * 2);
    gld16<0>(xr0, ov, sx, 0); gld16<32>(xr1, ov, sx, 0);
    gld2(bs, (st * 64 + wj * 32 + r) * 2, mksrd(b2, S_ * 2));
  }
  const Src2 src = {mksrd(ws + UF_OFF, UF_BYTES), mksrd(w2, F_ * S_ * 2), mksrd(ws + OF_OFF, OF_BYTES), mksrd(wo, D_ * D_ * 2),
                    w, mt * 2, mt * 112u * 2048u, st * 2u * 32768u};
  const unsigned av[2] = {lane * 16u + wi * 1024u, lane * 16u + (wi ^ 1) * 1024u};
  const unsigned av2[2] = {lane * 16u + wj * 32768u, lane * 16u + (wj ^ 1) * 32768u};
  unsigned wv[4], wv2[4];
#pragma unroll
  for (int i = 0; i < 4; i++) {
    const int sc = ((lane & 7) ^ (((lane >> 4) & 1) << 2)) << 4;
    wv[i] = (8 * i + (lane >> 3)) * (S_ * 2) + st * 128 + sc - i * 1024;
    wv2[i] = (8 * i + ((lane >> 3) & (i == 3 ? 3 : 7))) * (D_ * 2) + mt * 128 + sc - i * 1024;
  }
  const int q4 = (lane >> 2) & 3;
  const int cb = (((lane >> 4) & 1) * 32 + (lane & 3) * 8) ^ ((q4 >> 1) << 6);
  const lptr tb = sm + w * G_RING + (4 * h + q4) * 128;
  const lptr tr[2] = {tb + (cb ^ (wj * 64)), tb + (cb ^ (wj * 64) ^ 64)};
  const lptr tr2[2] = {tb + (cb ^ (wi * 64)), tb + (cb ^ (wi * 64) ^ 64)};
  f32x16 acc[2][2];
#pragma unroll
  for (int i = 0; i < 16; i++) acc[0][0][i] = acc[0][1][i] = acc[1][0][i] = acc[1][1][i] = 0.f;
  u32x4 act[DA][4];
  GemmLoop<18, 14, false, 1024, Src2>::run(acc, act, tr, tr2, ldsaddr(sm) + w * G_RING, av, av2, wv, wv2, src);
  asm volatile("" : "+v"(xr0), "+v"(xr1), "+v"(bs));
  const f32x16 y = wave_reduce(acc, sm, w, lane);
  const float bias = bflo(bs);
#pragma unroll
  for (int G = 0; G < 2; G++) {
    const u32x4 xr = G ? xr1 : xr0;
    float v[8];
#pragma unroll
    for (int j = 0; j < 4; j++) {
      const f32x2 t = swap32(y[8 * G + j], y[8 * G + 4 + j]);
      v[j] = t[0]; v[4 + j] = t[1];
    }
    u32x4 o;
#pragma unroll
    for (int j = 0; j < 4; j++) o[j] = pk2(v[2 * j] + bias + bflo(xr[j]), v[2 * j + 1] + bias + bfhi(xr[j]));
    *(u32x4*)(out + ov + 32 * G) = o;
  }
}

extern "C" void kernel_launch(void* const* d_in, const int* in_sizes, int n_in, void* d_out, int out_size, void* d_ws, size_t ws_size,
                              hipStream_t stream) {
  char* ws = (char*)d_ws;
  k1<<<256, 256, 0, stream>>>(d_in[0], d_in[1], d_in[2], ws);
  k2<<<256, 256, 0, stream>>>(d_in[5], d_in[6], ws);
  k3<<<224, 256, 0, stream>>>(d_in[0], d_in[7], d_in[8], d_in[3], ws, (char*)d_out);
}
```

```cpp
#include <hip/hip_runtime.h>
#include <hip/hip_bf16.h>

typedef __attribute__((ext_vector_type(8))) __bf16 bf16x8;
typedef __attribute__((ext_vector_type(4))) __bf16 bf16x4;
typedef __attribute__((ext_vector_type(2))) __bf16 bf16x2;
typedef __attribute__((ext_vector_type(16))) float f32x16;
typedef __attribute__((ext_vector_type(4))) float f32x4;
typedef __attribute__((ext_vector_type(2))) float f32x2;
typedef __attribute__((ext_vector_type(4))) unsigned u32x4;
typedef __attribute__((ext_vector_type(2))) unsigned u32x2;
typedef __amdgpu_buffer_rsrc_t srd_t;
#define LAS __attribute__((address_space(3)))
typedef LAS char* lptr;
#define DEV __device__ __forceinline__

constexpr int S_ = 2048, D_ = 448, F_ = 1792;
constexpr unsigned HF_BYTES = 7 * 128 * 2 * 1024, PQ_BYTES = 16 * 64 * 4 * 1024, PART_BYTES = 16 * 16 * 8 * 1024;
constexpr unsigned UF_BYTES = 7 * 112 * 2 * 1024, OF_BYTES = 64 * 16 * 2 * 1024;
constexpr unsigned HF_OFF = 0, PQ_OFF = HF_OFF + HF_BYTES, PART_OFF = PQ_OFF + PQ_BYTES, UF_OFF = PART_OFF + PART_BYTES,
                   OF_OFF = UF_OFF + UF_BYTES;
constexpr float LOG2E = 1.4426950408889634f;

#define ST_PARAM
#define ST_ARG
#define ST_DECL
#define ST(i)
#define ST_OUT(kid)

DEV srd_t mksrd(const void* p, unsigned bytes) { return __builtin_amdgcn_make_buffer_rsrc((void*)p, 0, bytes, 0x00020000); }
DEV unsigned ldsaddr(lptr p) { return (unsigned)(size_t)p; }
DEV int rfl(int v) { return __builtin_amdgcn_readfirstlane(v); }

template <int OFF> DEV void glds(unsigned voff, srd_t srd, unsigned soff) {
  asm volatile("buffer_load_dwordx4 %0, %1, %2 offen offset:%3 lds" ::"v"(voff), "s"(srd), "s"(soff), "n"(OFF) : "memory");
}
DEV void setm0(unsigned lds) { asm volatile("s_mov_b32 m0, %0\n\ts_nop 0" ::"s"(lds) : "memory"); }
template <int OFF> DEV void gld16(u32x4& d, unsigned voff, srd_t srd, unsigned soff) {
  asm volatile("buffer_load_dwordx4 %0, %1, %2, %3 offen offset:%4" : "=v"(d) : "v"(voff), "s"(srd), "s"(soff), "n"(OFF) : "memory");
}
template <int OFF> DEV void gld8(u32x2& d, unsigned voff, srd_t srd) {
  asm volatile("buffer_load_dwordx2 %0, %1, %2, 0 offen offset:%3" : "=v"(d) : "v"(voff), "s"(srd), "n"(OFF) : "memory");
}
DEV void gld2(unsigned& d, unsigned voff, srd_t srd) {
  asm volatile("buffer_load_ushort %0, %1, %2, 0 offen" : "=v"(d) : "v"(voff), "s"(srd) : "memory");
}
template <int N> DEV void wait_vm() { asm volatile("s_waitcnt vmcnt(%0)" ::"n"(N) : "memory"); }
template <int N> DEV void wait_bar() { asm volatile("s_waitcnt vmcnt(%0) lgkmcnt(0)\n\ts_barrier" ::"n"(N) : "memory"); }
DEV void lds_bar() { asm volatile("s_waitcnt lgkmcnt(0)\n\ts_barrier" ::: "memory"); }
DEV void lds_wait() { asm volatile("s_waitcnt lgkmcnt(0)" ::: "memory"); }

DEV bf16x8 trd(lptr p, int second) {
  bf16x4 a = __builtin_amdgcn_ds_read_tr16_b64_v4bf16((LAS bf16x4*)p);
  bf16x4 b = __builtin_amdgcn_ds_read_tr16_b64_v4bf16((LAS bf16x4*)(p + second));
  return __builtin_shufflevector(a, b, 0, 1, 2, 3, 4, 5, 6, 7);
}
DEV f32x16 mfma(bf16x8 a, bf16x8 b, f32x16 c) { return __builtin_amdgcn_mfma_f32_32x32x16_bf16(a, b, c, 0, 0, 0); }
DEV unsigned pk2(float a, float b) {
  f32x2 v = {a, b};
  return __builtin_bit_cast(unsigned, __builtin_convertvector(v, bf16x2));
}
DEV bf16x8 asbf(u32x4 v) { return __builtin_bit_cast(bf16x8, v); }
DEV u32x4 pack8(const f32x16& v, int s) {
  u32x4 r = {pk2(v[8 * s], v[8 * s + 1]), pk2(v[8 * s + 2], v[8 * s + 3]), pk2(v[8 * s + 4], v[8 * s + 5]), pk2(v[8 * s + 6], v[8 * s + 7])};
  return r;
}
DEV float bflo(unsigned u) { return __builtin_bit_cast(float, u << 16); }
DEV float bfhi(unsigned u) { return __builtin_bit_cast(float, u & 0xffff0000u); }
DEV float dot2(unsigned a, unsigned b, float c) {
  return __builtin_amdgcn_fdot2_f32_bf16(__builtin_bit_cast(bf16x2, a), __builtin_bit_cast(bf16x2, b), c, false);
}
DEV f32x2 swap32(float a, float b) {
  auto r = __builtin_amdgcn_permlane32_swap(__builtin_bit_cast(unsigned, a), __builtin_bit_cast(unsigned, b), false, false);
  const unsigned r0 = r[0], r1 = r[1];
  f32x2 o = {__builtin_bit_cast(float, r0), __builtin_bit_cast(float, r1)};
  return o;
}
DEV float xsum32(float v) { f32x2 r = swap32(v, v); return r[0] + r[1]; }
template <int CTRL> DEV float dppadd(float v) {
  return v + __builtin_bit_cast(float, __builtin_amdgcn_update_dpp(0, __builtin_bit_cast(int, v), CTRL, 0xf, 0xf, true));
}
DEV float halfsum(float v) {
  v = dppadd<0xB1>(v);
  v = dppadd<0x4E>(v);
  v = dppadd<0x141>(v);
  v = dppadd<0x140>(v);
  auto r = __builtin_amdgcn_permlane16_swap(__builtin_bit_cast(unsigned, v), __builtin_bit_cast(unsigned, v), false, false);
  const unsigned r0 = r[0], r1 = r[1];
  return __builtin_bit_cast(float, r0) + __builtin_bit_cast(float, r1);
}

DEV void dual_softmax(const f32x16& p, bool hi, f32x16& ep, f32x16& en) {
  float t[4];
#pragma unroll
  for (int j = 0; j < 4; j++) t[j] = hi ? p[8 + j] : p[12 + j];
  float mx = fmaxf(fmaxf(t[0], t[1]), fmaxf(t[2], t[3])), mn = fminf(fminf(t[0], t[1]), fminf(t[2], t[3]));
#pragma unroll
  for (int i = 0; i < 12; i += 2) {
    mx = fmaxf(mx, fmaxf(p[i], p[i + 1]));
    mn = fminf(mn, fminf(p[i], p[i + 1]));
  }
  f32x2 a = swap32(mx, mx), b = swap32(mn, mn);
  const float bp = -fmaxf(a[0], a[1]) * LOG2E, bn = fminf(b[0], b[1]) * LOG2E;
  const float bp2 = hi ? -__builtin_inff() : bp, bn2 = hi ? -__builtin_inff() : bn;
  float sp0 = 0.f, sp1 = 0.f, sn0 = 0.f, sn1 = 0.f;
#pragma unroll
  for (int i = 0; i < 16; i++) {
    ep[i] = __builtin_amdgcn_exp2f(__builtin_fmaf(p[i], LOG2E, i < 12 ? bp : bp2));
    en[i] = __builtin_amdgcn_exp2f(__builtin_fmaf(p[i], -LOG2E, i < 12 ? bn : bn2));
    if (i & 1) { sp1 += ep[i]; sn1 += en[i]; } else { sp0 += ep[i]; sn0 += en[i]; }
  }
  const float rp = __builtin_amdgcn_rcpf(xsum32(sp0 + sp1)), rn = __builtin_amdgcn_rcpf(xsum32(sn0 + sn1));
  ep *= rp;
  en *= rn;
}

constexpr int K1_W = 0, K1_X = 86016, K1_PHI = K1_X + 65536, K1_CS = K1_PHI + 2048, K1_R = K1_CS + 1536, K1_LN = K1_R + 512,
              K1_LDS = K1_LN + 8 * 960;
constexpr int K1_KT = 0, K1_RED = 32768;

template <int C> struct K1Wait { static constexpr int v = C == 0 ? 30 : C == 1 ? 29 : C == 2 ? 26 : C == 3 ? 23 : C == 4 ? 8 : C == 5 ? 4 : 0; };

struct K1State {
  f32x16 aq, ak, av;
  float s0, s1, q0, q1, cq, ck, cv;
};

template <int C>
DEV void k1_chunk(K1State& st, const lptr (&xb)[4], const lptr (&wb)[4]) {
  constexpr unsigned ONES = 0x3F803F80u;
#pragma unroll
  for (int i = 0; i < 4; i++) {
    const u32x4 xf = *(LAS u32x4*)(xb[i] + (C & 3) * 4096);
    const bf16x8 wq = trd(wb[i] + C * 12288, 256), wk = trd(wb[i] + C * 12288 + 4096, 256), wv = trd(wb[i] + C * 12288 + 8192, 256);
    st.aq = mfma(wq, asbf(xf), st.aq);
    st.ak = mfma(wk, asbf(xf), st.ak);
    st.av = mfma(asbf(xf), wv, st.av);
    st.s0 = dot2(xf[0], ONES, st.s0); st.s1 = dot2(xf[1], ONES, st.s1);
    st.q0 = dot2(xf[0], xf[0], st.q0); st.q1 = dot2(xf[1], xf[1], st.q1);
    st.s0 = dot2(xf[2], ONES, st.s0); st.s1 = dot2(xf[3], ONES, st.s1);
    st.q0 = dot2(xf[2], xf[2], st.q0); st.q1 = dot2(xf[3], xf[3], st.q1);
    if (i == 0) {
      const u32x4 a = __builtin_bit_cast(u32x4, wq), b = __builtin_bit_cast(u32x4, wk), c = __builtin_bit_cast(u32x4, wv);
#pragma unroll
      for (int j = 0; j < 4; j++) { st.cq = dot2(a[j], ONES, st.cq); st.ck = dot2(b[j], ONES, st.ck); st.cv = dot2(c[j], ONES, st.cv); }
    }
  }
}

DEV u32x4 hilo_w(float v, bool hi) {
  const unsigned h2 = pk2(v, v);
  const float lo = v - bflo(h2);
  u32x4 r = {hi ? 0u : h2, hi ? 0u : pk2(lo, lo), 0u, 0u};
  return r;
}

__global__ __launch_bounds__(256, 1) void k1(const void* __restrict__ x, const void* __restrict__ wqkv, const void* __restrict__ wphi,
                                             char* __restrict__ ws) {
  __shared__ __attribute__((aligned(16))) char smem_[K1_LDS];
  const lptr sm = (lptr)smem_;
  const int tid = threadIdx.x, lane = tid & 63, w = rfl(tid >> 6);
  const int b = blockIdx.x, xcd = b & 7, idx = b >> 3;
  const int rt = (xcd >> 1) * 4 + (idx >> 3), hd = (xcd & 1) * 8 + (idx & 7);
  const int rot = (idx & 7) == 7 ? 0 : (idx & 7);
  ST_DECL;
  ST(0);
  const int r = lane & 31, h = lane >> 5;
  const bool hi = h != 0;
  const srd_t sx = mksrd(x, S_ * D_ * 2), sw = mksrd(wqkv, D_ * 3 * D_ * 2), sp = mksrd(wphi, 16 * 28 * 28 * 2);

  u32x4 l0, l1;
  u32x2 ph;
  {
    const unsigned lv = r < 28 ? (unsigned)((rt * 128 + hd * 8 + 2 * w + h) * 896 + r * 16) : 0x80000000u;
    gld16<0>(l0, lv, sx, 0);
    gld16<448>(l1, lv, sx, 0);
    const int pr = tid >> 3, pc = tid & 7;
    gld8<0>(ph, (pr < 28 && pc < 7) ? (unsigned)(hd * 1568 + pr * 56 + pc * 8) : 0x80000000u, sp);
  }
  unsigned xv[4];
#pragma unroll
  for (int i = 0; i < 4; i++) {
    const int row = 8 * i + (lane >> 3);
    xv[i] = (rt * 128 + w * 32 + row) * 896 + (((lane & 7) ^ ((row >> 1) & 7)) << 4) - i * 1024;
  }
  const unsigned wv0 = (16 * w + (lane >> 2)) * 2688 + ((56 * hd) & ~15) + (lane & 3) * 16;
  const unsigned xl = ldsaddr(sm) + K1_X + w * 16384, wl = ldsaddr(sm) + K1_W + w * 1024;
  auto issueX = [&](int c) {
    int cc = c + rot; cc = cc >= 7 ? cc - 7 : cc;
    setm0(xl + (c & 3) * 4096);
    glds<0>(xv[0], sx, cc * 128); glds<1024>(xv[1], sx, cc * 128); glds<2048>(xv[2], sx, cc * 128); glds<3072>(xv[3], sx, cc * 128);
  };
  auto issueW = [&](int c) {
    int cc = c + rot; cc = cc >= 7 ? cc - 7 : cc;
#pragma unroll
    for (int m = 0; m < 3; m++) { setm0(wl + c * 12288 + m * 4096); glds<0>(wv0, sw, cc * (64 * 2688) + m * 896); }
  };
#pragma unroll
  for (int c = 0; c < 7; c++) { if (c < 4) issueX(c); issueW(c); }

  ST(1);
  asm volatile("s_waitcnt vmcnt(37)" : "+v"(l0), "+v"(l1), "+v"(ph)::"memory");
  ST(2);
  {
    float v[16];
#pragma unroll
    for (int j = 0; j < 4; j++) { v[2 * j] = bflo(l0[j]); v[2 * j + 1] = bfhi(l0[j]); v[8 + 2 * j] = bflo(l1[j]); v[9 + 2 * j] = bfhi(l1[j]); }
    const float valid = r < 28 ? 1.f : 0.f;
    float s = 0.f;
#pragma unroll
    for (int j = 0; j < 16; j++) s += v[j];
    const float m = halfsum(s) * (1.f / D_);
    float q = 0.f;
#pragma unroll
    for (int j = 0; j < 16; j++) { v[j] -= m; q = __builtin_fmaf(v[j], v[j], q); }
    const float rs = valid * __builtin_amdgcn_rsqf(halfsum(q * valid) * (1.f / D_) + 1e-5f);
    float s2 = 0.f, q2 = 0.f;
#pragma unroll
    for (int j = 0; j < 16; j += 2) {
      const unsigned u = pk2(v[j] * rs, v[j + 1] * rs);
      v[j] = bflo(u); v[j + 1] = bfhi(u);
      s2 += v[j] + v[j + 1];
      q2 = dot2(u, u, q2);
    }
    const float m2 = halfsum(s2) * (1.f / D_);
    const float rs2 = __builtin_amdgcn_rsqf(fmaxf(halfsum(q2) * (1.f / D_) - m2 * m2, 0.f) + 1e-5f);
    u32x4 o0, o1;
#pragma unroll
    for (int j = 0; j < 4; j++) {
      o0[j] = pk2((v[2 * j] - m2) * rs2, (v[2 * j + 1] - m2) * rs2);
      o1[j] = pk2((v[8 + 2 * j] - m2) * rs2, (v[9 + 2 * j] - m2) * rs2);
    }
    if (r < 28) {
      *(LAS u32x4*)(sm + K1_LN + (2 * w + h) * 960 + r * 16) = o0;
      *(LAS u32x4*)(sm + K1_LN + (2 * w + h) * 960 + 448 + r * 16) = o1;
    }
    *(LAS u32x2*)(sm + K1_PHI + tid * 8) = ph;
  }

  const int g1 = (lane >> 4) & 1, q4 = (lane >> 2) & 3, p4 = lane & 3;
  lptr xb[4], wb[4];
#pragma unroll
  for (int i = 0; i < 4; i++) {
    const int t = i ^ w;
    xb[i] = sm + K1_X + w * 16384 + r * 128 + (((2 * t + h) ^ ((r >> 1) & 7)) << 4);
    wb[i] = sm + K1_W + t * 1024 + (8 * h + q4) * 64 + min((hd & 1) * 8 + 32 * g1 + 8 * p4, 56);
  }
  K1State st;
#pragma unroll
  for (int i = 0; i < 16; i++) { st.aq[i] = 0.f; st.ak[i] = 0.f; st.av[i] = 0.f; }
  st.s0 = st.s1 = st.q0 = st.q1 = st.cq = st.ck = st.cv = 0.f;

  ST(3);
  wait_bar<K1Wait<0>::v>();
  ST(4);
  {
    const int ks = rt * 8 + (hd >> 1);
#pragma unroll
    for (int n = 0; n < 2; n++) {
      const int mt = n == 0 ? w : (w < 3 ? w + 4 : w);
      const bf16x8 t = trd(sm + K1_LN + q4 * 960 + mt * 128 + (lane >> 4) * 32 + p4 * 8, 4 * 960);
      *(u32x4*)(ws + HF_OFF + ((mt * 128 + ks) * 2 + h) * 1024 + ((hd & 1) * 32 + r) * 16) = __builtin_bit_cast(u32x4, t);
    }
  }
  k1_chunk<0>(st, xb, wb); lds_wait(); issueX(4);
  wait_bar<K1Wait<1>::v>(); k1_chunk<1>(st, xb, wb); lds_wait(); issueX(5);
  wait_bar<K1Wait<2>::v>(); k1_chunk<2>(st, xb, wb); lds_wait(); issueX(6);
  wait_bar<K1Wait<3>::v>(); k1_chunk<3>(st, xb, wb);
  ST(5);
  wait_bar<K1Wait<4>::v>(); ST(6); k1_chunk<4>(st, xb, wb);
  wait_bar<K1Wait<5>::v>(); k1_chunk<5>(st, xb, wb);
  wait_bar<K1Wait<6>::v>(); k1_chunk<6>(st, xb, wb);

  ST(7);
  const float mean = xsum32(st.s0 + st.s1) * (1.f / D_);
  const float rstd = __builtin_amdgcn_rsqf(fmaxf(xsum32(st.q0 + st.q1) * (1.f / D_) - mean * mean, 0.f) + 1e-5f);
  {
    const float cq = xsum32(st.cq), ck = xsum32(st.ck), cv = xsum32(st.cv);
    if (!hi) {
      *(LAS float*)(sm + K1_CS + r * 16 + w * 4) = cq;
      *(LAS float*)(sm + K1_CS + 512 + r * 16 + w * 4) = ck;
      *(LAS float*)(sm + K1_CS + 1024 + r * 16 + w * 4) = cv;
      *(LAS float*)(sm + K1_R + w * 128 + r * 4) = rstd;
    }
  }
  lds_bar();
  {
    const f32x4 a = *(LAS f32x4*)(sm + K1_CS + r * 16), c = *(LAS f32x4*)(sm + K1_CS + 512 + r * 16), d = *(LAS f32x4*)(sm + K1_CS + 1024 + r * 16);
    const unsigned mh = pk2(-mean, 0.f);
    const unsigned mm = pk2(-mean, -mean - bflo(mh));
    const u32x4 xm = {mm, mm, 0u, 0u};
    st.aq = mfma(asbf(hilo_w((a[0] + a[1]) + (a[2] + a[3]), hi)), asbf(xm), st.aq);
    st.ak = mfma(asbf(hilo_w((c[0] + c[1]) + (c[2] + c[3]), hi)), asbf(xm), st.ak);
    st.av = mfma(asbf(xm), asbf(hilo_w((d[0] + d[1]) + (d[2] + d[3]), hi)), st.av);
  }
  const lptr pp = sm + K1_PHI + (4 * h + q4) * 64 + 32 * g1 + 8 * p4;
  const bf16x8 f0 = trd(pp, 512), f1 = trd(pp + 1024, 512);
  f32x16 z16;
#pragma unroll
  for (int i = 0; i < 16; i++) z16[i] = 0.f;
  st.ak *= rstd;
  st.aq *= rstd;
  const f32x16 pk = mfma(f1, asbf(pack8(st.ak, 1)), mfma(f0, asbf(pack8(st.ak, 0)), z16));
  const f32x16 pq = mfma(f1, asbf(pack8(st.aq, 1)), mfma(f0, asbf(pack8(st.aq, 0)), z16));

  f32x16 ep, en;
  ST(8);
  dual_softmax(pk, hi, ep, en);
  const lptr kt = sm + K1_KT + w * (32 * 136);
#pragma unroll
  for (int g = 0; g < 4; g++) {
    const u32x2 a = {pk2(ep[4 * g], ep[4 * g + 1]), pk2(ep[4 * g + 2], ep[4 * g + 3])};
    const u32x2 c = {pk2(en[4 * g], en[4 * g + 1]), pk2(en[4 * g + 2], en[4 * g + 3])};
    *(LAS u32x2*)(kt + r * 136 + 16 * g + 8 * h) = a;
    *(LAS u32x2*)(kt + r * 136 + 64 + 16 * g + 8 * h) = c;
  }
#pragma unroll
  for (int g = 0; g < 4; g++) {
    const f32x4 rr = *(LAS f32x4*)(sm + K1_R + w * 128 + 32 * g + 16 * h);
#pragma unroll
    for (int j = 0; j < 4; j++) st.av[4 * g + j] = r == 28 ? 1.f : st.av[4 * g + j] * rr[j];
  }
  const u32x4 v0 = pack8(st.av, 0), v1 = pack8(st.av, 1);
  lds_wait();
  const lptr kr = kt + (4 * h + q4) * 136 + 32 * g1 + 8 * p4;
  const f32x16 kvp = mfma(trd(kr + 16 * 136, 8 * 136), asbf(v1), mfma(trd(kr, 8 * 136), asbf(v0), z16));
  const f32x16 kvn = mfma(trd(kr + 16 * 136 + 64, 8 * 136), asbf(v1), mfma(trd(kr + 64, 8 * 136), asbf(v0), z16));

  ST(9);
  dual_softmax(pq, hi, ep, en);
  {
    char* o = ws + PQ_OFF + (hd * 64 + rt * 4 + w) * 4096 + lane * 16;
    *(u32x4*)(o) = pack8(ep, 0);
    *(u32x4*)(o + 1024) = pack8(ep, 1);
    *(u32x4*)(o + 2048) = pack8(en, 0);
    *(u32x4*)(o + 3072) = pack8(en, 1);
  }
  const lptr rd = sm + K1_RED + lane * 16;
#pragma unroll
  for (int g = 0; g < 4; g++) {
    const f32x4 a = {kvp[4 * g], kvp[4 * g + 1], kvp[4 * g + 2], kvp[4 * g + 3]};
    const f32x4 c = {kvn[4 * g], kvn[4 * g + 1], kvn[4 * g + 2], kvn[4 * g + 3]};
    *(LAS f32x4*)(rd + w * 8192 + g * 1024) = a;
    *(LAS f32x4*)(rd + w * 8192 + 4096 + g * 1024) = c;
  }
  lds_bar();
#pragma unroll
  for (int n = 0; n < 2; n++) {
    const lptr p = rd + (2 * w + n) * 1024;
    const f32x4 t = (*(LAS f32x4*)(p) + *(LAS f32x4*)(p + 8192)) + (*(LAS f32x4*)(p + 16384) + *(LAS f32x4*)(p + 24576));
    *(f32x4*)(ws + PART_OFF + ((hd * 16 + rt) * 8 + 2 * w + n) * 1024 + lane * 16) = t;
  }
  ST(10);
  ST(11);
  ST_OUT(0);
}

constexpr int DW = 8, DA = 6, G_RING = DW * 4096, G_LDS = 4 * G_RING;

template <int NS> constexpr int vm_wait(int t) {
  int after = 0;
  for (int s = 0; s < (DW > DA ? DW : DA); s++) {
    if (s < DA) after = s == t ? 0 : after + 4;
    if (s < DW) after = s == t ? 0 : after + 4;
  }
  for (int u = 0; u < t; u++) {
    if (u + DA < NS) after = u + DA == t ? 0 : after + 4;
    if (u + DW < NS) after = u + DW == t ? 0 : after + 4;
  }
  return after;
}

struct StepSrc { srd_t a, w; unsigned aoff, woff; };

template <int NS, int NMAIN, bool LDS_IS_A, int SECOND, class Src, int T = 0>
struct GemmLoop {
  static DEV void issueA(int s, u32x4 (&act)[DA][4], const unsigned (&av)[2], const unsigned (&av2)[2], const Src& src) {
    const StepSrc q = src(s);
    u32x4(&d)[4] = act[s % DA];
    if (s < NMAIN) { gld16<0>(d[0], av[0], q.a, q.aoff); gld16<0>(d[1], av[1], q.a, q.aoff); gld16<2048>(d[2], av[0], q.a, q.aoff); gld16<2048>(d[3], av[1], q.a, q.aoff); }
    else { gld16<0>(d[0], av2[0], q.a, q.aoff); gld16<0>(d[1], av2[1], q.a, q.aoff); gld16<1024>(d[2], av2[0], q.a, q.aoff); gld16<1024>(d[3], av2[1], q.a, q.aoff); }
  }
  static DEV void issueW(int s, unsigned ring, const unsigned (&wv)[4], const unsigned (&wv2)[4], const Src& src) {
    const StepSrc q = src(s);
    setm0(ring + (s % DW) * 4096);
    if (s < NMAIN) { glds<0>(wv[0], q.w, q.woff); glds<1024>(wv[1], q.w, q.woff); glds<2048>(wv[2], q.w, q.woff); glds<3072>(wv[3], q.w, q.woff); }
    else { glds<0>(wv2[0], q.w, q.woff); glds<1024>(wv2[1], q.w, q.woff); glds<2048>(wv2[2], q.w, q.woff); glds<3072>(wv2[3], q.w, q.woff); }
  }
  static DEV void run(f32x16 (&acc)[2][2], u32x4 (&act)[DA][4], const lptr (&tr)[2], const lptr (&tr2)[2], unsigned ring,
                      const unsigned (&av)[2], const unsigned (&av2)[2], const unsigned (&wv)[4], const unsigned (&wv2)[4], const Src& src ST_PARAM) {
    if constexpr (T == 0) {
#pragma unroll
      for (int s = 0; s < (DW > DA ? DW : DA); s++) {
        if (s < DA) issueA(s, act, av, av2, src);
        if (s < DW) issueW(s, ring, wv, wv2, src);
      }
      ST(1);
    }
    if constexpr (T < NS) {
      u32x4(&d)[4] = act[T % DA];
      asm volatile("s_waitcnt vmcnt(%4)" : "+v"(d[0]), "+v"(d[1]), "+v"(d[2]), "+v"(d[3]) : "n"(vm_wait<NS>(T)) : "memory");
      if constexpr (T % 4 == 0) ST(2 + T / 4);
      constexpr bool la = (T < NMAIN) ? LDS_IS_A : !LDS_IS_A;
      const lptr t0 = (T < NMAIN ? tr[0] : tr2[0]) + (T % DW) * 4096, t1 = (T < NMAIN ? tr[1] : tr2[1]) + (T % DW) * 4096;
      const bf16x8 m00 = trd(t0, SECOND), m01 = trd(t1, SECOND), m10 = trd(t0 + 2048, SECOND), m11 = trd(t1 + 2048, SECOND);
      if constexpr (la) {
        acc[0][0] = mfma(m00, asbf(d[0]), acc[0][0]); acc[0][1] = mfma(m00, asbf(d[1]), acc[0][1]);
        acc[1][0] = mfma(m01, asbf(d[0]), acc[1][0]); acc[1][1] = mfma(m01, asbf(d[1]), acc[1][1]);
        acc[0][0] = mfma(m10, asbf(d[2]), acc[0][0]); acc[0][1] = mfma(m10, asbf(d[3]), acc[0][1]);
        acc[1][0] = mfma(m11, asbf(d[2]), acc[1][0]); acc[1][1] = mfma(m11, asbf(d[3]), acc[1][1]);
      } else {
        acc[0][0] = mfma(asbf(d[0]), m00, acc[0][0]); acc[0][1] = mfma(asbf(d[0]), m01, acc[0][1]);
        acc[1][0] = mfma(asbf(d[1]), m00, acc[1][0]); acc[1][1] = mfma(asbf(d[1]), m01, acc[1][1]);
        acc[0][0] = mfma(asbf(d[2]), m10, acc[0][0]); acc[0][1] = mfma(asbf(d[2]), m11, acc[0][1]);
        acc[1][0] = mfma(asbf(d[3]), m10, acc[1][0]); acc[1][1] = mfma(asbf(d[3]), m11, acc[1][1]);
      }
      if constexpr (T + DA < NS) issueA(T + DA, act, av, av2, src);
      if constexpr (T + DW < NS) { lds_wait(); issueW(T + DW, ring, wv, wv2, src); }
      GemmLoop<NS, NMAIN, LDS_IS_A, SECOND, Src, T + 1>::run(acc, act, tr, tr2, ring, av, av2, wv, wv2, src ST_ARG);
    }
  }
};

DEV f32x16 wave_reduce(f32x16 (&acc)[2][2], lptr sm, int w, int lane) {
  lds_wait();
  const lptr own = sm + w * G_RING + lane * 16;
#pragma unroll
  for (int x = 1; x < 4; x++)
#pragma unroll
    for (int g = 0; g < 4; g++) {
      const f32x16& a = acc[x >> 1][x & 1];
      const f32x4 v = {a[4 * g], a[4 * g + 1], a[4 * g + 2], a[4 * g + 3]};
      *(LAS f32x4*)(own + x * 4096 + g * 1024) = v;
    }
  lds_bar();
  f32x16 o = acc[0][0];
#pragma unroll
  for (int x = 1; x < 4; x++)
#pragma unroll
    for (int g = 0; g < 4; g++) {
      const f32x4 v = *(LAS f32x4*)(sm + (w ^ x) * G_RING + lane * 16 + x * 4096 + g * 1024);
#pragma unroll
      for (int j = 0; j < 4; j++) o[4 * g + j] += v[j];
    }
  return o;
}

struct Src1 {
  srd_t a, w;
  int k0, rot;
  unsigned abase;
  DEV StepSrc operator()(int s) const {
    const int kk = k0 + ((s + rot) & 15);
    StepSrc q = {a, w, abase + kk * 4096u, kk * (32u * F_ * 2)};
    return q;
  }
};

DEV void attn_readout(const char* __restrict__ wsr, char* __restrict__ ws, lptr sm, int hd, int half, int w, int lane) {
  const bool hi = lane >= 32;
  const char* pq = wsr + PQ_OFF + (hd * 64 + half * 32 + w * 8) * 4096 + lane * 16;
  const char* pp = wsr + PART_OFF + (hd * 128 + 2 * w) * 1024 + lane * 16;
  f32x4 a = *(const f32x4*)pp, c = *(const f32x4*)(pp + 1024);
#pragma unroll
  for (int t = 1; t < 16; t++) { a += *(const f32x4*)(pp + t * 8192); c += *(const f32x4*)(pp + t * 8192 + 1024); }
  u32x4 ph[8][4];
#pragma unroll
  for (int t = 0; t < 8; t++)
#pragma unroll
    for (int s = 0; s < 4; s++) ph[t][s] = *(const u32x4*)(pq + t * 4096 + s * 1024);
  const u32x4 kf = {pk2(a[0], a[1]), pk2(a[2], a[3]), pk2(c[0], c[1]), pk2(c[2], c[3])};
  *(LAS u32x4*)(sm + w * 1024 + lane * 16) = kf;
  lds_bar();
  u32x4 kv[4];
#pragma unroll
  for (int s = 0; s < 4; s++) kv[s] = *(LAS u32x4*)(sm + s * 1024 + lane * 16);
#pragma unroll
  for (int t = 0; t < 8; t++) {
    f32x16 n;
#pragma unroll
    for (int i = 0; i < 16; i++) n[i] = 0.f;
#pragma unroll
    for (int s = 0; s < 4; s++) n = mfma(asbf(kv[s]), asbf(ph[t][s]), n);
    const float inv = __builtin_amdgcn_rcpf(swap32(n[12], n[12])[1] + 1e-6f);
    n *= inv;
#pragma unroll
    for (int i = 12; i < 16; i++) n[i] = hi ? 0.f : n[i];
    char* o = ws + OF_OFF + ((half * 32 + w * 8 + t) * 16 + hd) * 2048 + lane * 16;
    *(u32x4*)o = pack8(n, 0);
    *(u32x4*)(o + 1024) = pack8(n, 1);
  }
}

__global__ __launch_bounds__(256, 1) void k2(const void* __restrict__ w1, const void* __restrict__ b1, char* __restrict__ ws) {
  __shared__ __attribute__((aligned(16))) char smem_[G_LDS];
  const lptr sm = (lptr)smem_;
  const int tid = threadIdx.x, lane = tid & 63, w = rfl(tid >> 6);
  const int b = blockIdx.x, xcd = b & 7, idx = b >> 3;
  const int ntile = 24 + ((xcd & 1) ^ 1);
  ST_DECL;
  ST(0);
  if (idx >= ntile) {
    if (idx >= 28) attn_readout(ws, ws, sm, xcd * 2 + ((idx - 28) >> 1), idx & 1, w, lane);
    ST(10);
    ST(11);
    ST_OUT(1);
    return;
  }
  const int tile = ((xcd * 49 + 1) >> 1) + idx;
  const int ft = tile / 7, mt = tile - ft * 7;
  const int wi = w >> 1, wj = w & 1, h = lane >> 5;
  const srd_t sb = mksrd(b1, F_ * 2);
  u32x2 bb[4];
  {
    const unsigned bv = (ft * 64 + wi * 32 + 4 * h) * 2;
    gld8<0>(bb[0], bv, sb); gld8<16>(bb[1], bv, sb); gld8<32>(bb[2], bv, sb); gld8<48>(bb[3], bv, sb);
  }
  const Src1 src = {mksrd(ws + HF_OFF, HF_BYTES), mksrd(w1, S_ * F_ * 2), w * 16, (mt * 7) / 3, mt * 128u * 2048u};
  const unsigned av[2] = {lane * 16u + wj * 1024u, lane * 16u + (wj ^ 1) * 1024u};
  unsigned wv[4];
#pragma unroll
  for (int i = 0; i < 4; i++) wv[i] = (8 * i + (lane >> 3)) * (F_ * 2) + ft * 128 + (((lane & 7) ^ (((lane >> 4) & 1) << 2)) << 4) - i * 1024;
  const int q4 = (lane >> 2) & 3;
  const int cb = (wi * 64 + ((lane >> 4) & 1) * 32 + (lane & 3) * 8) ^ ((q4 >> 1) << 6);
  const lptr tb = sm + w * G_RING + (8 * h + q4) * 128;
  const lptr tr[2] = {tb + cb, tb + (cb ^ 64)};
  f32x16 acc[2][2];
#pragma unroll
  for (int i = 0; i < 16; i++) acc[0][0][i] = acc[0][1][i] = acc[1][0][i] = acc[1][1][i] = 0.f;
  u32x4 act[DA][4];
  GemmLoop<16, 16, true, 512, Src1>::run(acc, act, tr, tr, ldsaddr(sm) + w * G_RING, av, av, wv, wv, src ST_ARG);
  ST(7);
  asm volatile("" : "+v"(bb[0]), "+v"(bb[1]), "+v"(bb[2]), "+v"(bb[3]));
  f32x16 u = wave_reduce(acc, sm, w, lane);
#pragma unroll
  for (int i = 0; i < 16; i++) {
    const unsigned bw = bb[i >> 2][(i >> 1) & 1];
    const float y = u[i] + ((i & 1) ? bfhi(bw) : bflo(bw));
    const float z = y * __builtin_fmaf(y * y, -2.f * 0.7978845608028654f * 0.044715f * LOG2E, -2.f * 0.7978845608028654f * LOG2E);
    u[i] = y * __builtin_amdgcn_rcpf(1.f + __builtin_amdgcn_exp2f(z));
  }
  char* o = ws + UF_OFF + ((mt * 112 + ft * 4 + wi * 2) * 2 + wj) * 1024 + lane * 16;
  *(u32x4*)o = pack8(u, 0);
  *(u32x4*)(o + 2048) = pack8(u, 1);
  ST(10);
  ST(11);
  ST_OUT(1);
}

struct Src2 {
  srd_t a, w, a2, w2;
  int wv, rot;
  unsigned abase, a2base;
  DEV StepSrc operator()(int s) const {
    if (s < 14) {
      int k = s + rot;
      k = (k >= 14 ? k - 14 : k) + wv * 14;
      StepSrc q = {a, w, abase + k * 4096u, k * (32u * S_ * 2)};
      return q;
    }
    const int hd = wv * 4 + ((s + rot) & 3);
    StepSrc q = {a2, w2, a2base + hd * 2048u, hd * (28u * D_ * 2)};
    return q;
  }
};

__global__ __launch_bounds__(256, 1) void k3(const void* __restrict__ x, const void* __restrict__ w2, const void* __restrict__ b2,
                                             const void* __restrict__ wo, const char* __restrict__ ws, char* __restrict__ out) {
  __shared__ __attribute__((aligned(16))) char smem_[G_LDS];
  const lptr sm = (lptr)smem_;
  const int tid = threadIdx.x, lane = tid & 63, w = rfl(tid >> 6);
  const int b = blockIdx.x, xcd = b & 7, idx = b >> 3;
  const int stl = idx / 7, mt = idx - stl * 7, st = xcd * 4 + stl;
  ST_DECL;
  ST(0);
  const int wi = w >> 1, wj = w & 1, h = lane >> 5, r = lane & 31;
  const unsigned ov = (st * 64 + wj * 32 + r) * 896 + (mt * 64 + wi * 32 + 8 * h) * 2;
  u32x4 xr0, xr1;
  unsigned bs;
  {
    const srd_t sx = mksrd(x, S_ * D_ * 2);
    gld16<0>(xr0, ov, sx, 0); gld16<32>(xr1, ov, sx, 0);
    gld2(bs, (st * 64 + wj * 32 + r) * 2, mksrd(b2, S_ * 2));
  }
  const Src2 src = {mksrd(ws + UF_OFF, UF_BYTES), mksrd(w2, F_ * S_ * 2), mksrd(ws + OF_OFF, OF_BYTES), mksrd(wo, D_ * D_ * 2),
                    w, mt * 2, mt * 112u * 2048u, st * 2u * 32768u};
  const unsigned av[2] = {lane * 16u + wi * 1024u, lane * 16u + (wi ^ 1) * 1024u};
  const unsigned av2[2] = {lane * 16u + wj * 32768u, lane * 16u + (wj ^ 1) * 32768u};
  unsigned wv[4], wv2[4];
#pragma unroll
  for (int i = 0; i < 4; i++) {
    const int sc = ((lane & 7) ^ (((lane >> 4) & 1) << 2)) << 4;
    wv[i] = (8 * i + (lane >> 3)) * (S_ * 2) + st * 128 + sc - i * 1024;
    wv2[i] = (8 * i + ((lane >> 3) & (i == 3 ? 3 : 7))) * (D_ * 2) + mt * 128 + sc - i * 1024;
  }
  const int q4 = (lane >> 2) & 3;
  const int cb = (((lane >> 4) & 1) * 32 + (lane & 3) * 8) ^ ((q4 >> 1) << 6);
  const lptr tb = sm + w * G_RING + (4 * h + q4) * 128;
  const lptr tr[2] = {tb + (cb ^ (wj * 64)), tb + (cb ^ (wj * 64) ^ 64)};
  const lptr tr2[2] = {tb + (cb ^ (wi * 64)), tb + (cb ^ (wi * 64) ^ 64)};
  f32x16 acc[2][2];
#pragma unroll
  for (int i = 0; i < 16; i++) acc[0][0][i] = acc[0][1][i] = acc[1][0][i] = acc[1][1][i] = 0.f;
  u32x4 act[DA][4];
  GemmLoop<18, 14, false, 1024, Src2>::run(acc, act, tr, tr2, ldsaddr(sm) + w * G_RING, av, av2, wv, wv2, src ST_ARG);
  ST(7);
  asm volatile("" : "+v"(xr0), "+v"(xr1), "+v"(bs));
  const f32x16 y = wave_reduce(acc, sm, w, lane);
  const float bias = bflo(bs);
#pragma unroll
  for (int G = 0; G < 2; G++) {
    const u32x4 xr = G ? xr1 : xr0;
    float v[8];
#pragma unroll
    for (int j = 0; j < 4; j++) {
      const f32x2 t = swap32(y[8 * G + j], y[8 * G + 4 + j]);
      v[j] = t[0]; v[4 + j] = t[1];
    }
    u32x4 o;
#pragma unroll
    for (int j = 0; j < 4; j++) o[j] = pk2(v[2 * j] + bias + bflo(xr[j]), v[2 * j + 1] + bias + bfhi(xr[j]));
    *(u32x4*)(out + ov + 32 * G) = o;
  }
  ST(10);
  ST(11);
  ST_OUT(2);
}

extern "C" void kernel_launch(void* const* d_in, const int* in_sizes, int n_in, void* d_out, int out_size, void* d_ws, size_t ws_size,
                              hipStream_t stream) {
  char* ws = (char*)d_ws;
  k1<<<256, 256, 0, stream>>>(d_in[0], d_in[1], d_in[2], ws);
  k2<<<256, 256, 0, stream>>>(d_in[5], d_in[6], ws);
  k3<<<224, 256, 0, stream>>>(d_in[0], d_in[7], d_in[8], d_in[3], ws, (char*)d_out);
}
```

```cpp
#include <hip/hip_runtime.h>

struct Srd { const void* p; unsigned bytes, cfg; };
struct Args { Srd s[10]; };

__global__ __launch_bounds__(256, 1) void fused(Args) {
  __shared__ __attribute__((aligned(128))) char smem[163840];
  asm volatile(
      ".Lk_begin:\n"
      "s_getpc_b64 s[48:49]\n"
      "s_load_dwordx16 s[8:23], %0, 0\n"
      "s_load_dwordx8 s[40:47], %0, 128\n"
      "s_load_dwordx16 s[24:39], %0, 64\n"
      "v_lshrrev_b32 v2, 6, v0\n"
      "v_and_b32 v1, 63, v0\n"
      "v_readfirstlane_b32 s50, v2\n"
      "s_lshr_b32 s51, %1, 3\n"
      "s_lshl_b32 s52, s51, 2\n"
      "s_add_u32 s52, s52, s50\n"
      "s_mul_i32 s52, s52, 512\n"
      "v_and_b32 v4, 3, v0\n"
      "v_lshl_add_u32 v4, v4, 7, s52\n"
      "v_min_u32 v4, 33080, v4\n"
      "global_load_dword v4, v4, s[48:49]\n"
      "s_and_b32 s48, %1, 7\n"
      "s_mul_i32 s52, s48, 56\n"
      "s_lshl_b32 s53, s51, 1\n"
      "s_add_u32 s52, s52, s53\n"
      "s_add_u32 s52, s52, s50\n"
      "s_lshr_b32 s49, s52, 4\n"
      "s_and_b32 s54, s52, 15\n"
      "v_and_b32 v5, 3, v0\n"
      "v_lshrrev_b32 v3, 2, v1\n"
      "v_mul_u32_u24 v8, 2688, v3\n"
      "s_lshl_b32 s52, s50, 4\n"
      "s_max_u32 s52, s52, s51\n"
      "s_cmp_lt_u32 s52, 28\n"
      "s_cselect_b32 s55, 0, 2147483648\n"
      "v_lshl_add_u32 v8, v5, 4, v8\n"
      "v_or_b32 v8, s55, v8\n"
      "s_mul_i32 s52, s54, 56\n"
      "s_and_b32 s52, s52, 0xfff0\n"
      "s_mul_i32 s53, s49, 43008\n"
      "s_add_u32 s4, s52, s53\n"
      "v_add_u32 v7, 128, v8\n"
      "v_add_u32 v6, 256, v8\n"
      "s_mul_i32 s56, s50, 3072\n"
      "s_add_u32 s56, s56, %3\n"
      "s_add_u32 s56, s56, 114688\n"
      "v_mov_b32 v9, 9142272\n"
      "s_add_u32 m0, s56, 24576\n"
      "s_and_b32 s57, s51, 3\n"
      "s_lshr_b32 s58, s51, 2\n"
      "s_min_u32 s59, s48, 6\n"
      "s_lshr_b32 s52, s48, 1\n"
      "s_lshl_b32 s52, s52, 2\n"
      "s_add_u32 s57, s57, s52\n"
      "s_and_b32 s52, s48, 1\n"
      "s_lshl_b32 s52, s52, 3\n"
      "s_add_u32 s58, s58, s52\n"
      "v_and_b32 v10, 31, v0\n"
      "v_bfe_u32 v11, v0, 5, 1\n"
      "v_mov_b32 v12, 2147483648\n"
      "v_cmp_gt_u32 vcc, 28, v10\n"
      "v_mul_u32_u24 v13, 896, v11\n"
      "v_lshl_add_u32 v13, v10, 4, v13\n"
      "s_lshl_b32 s60, s57, 4\n"
      "s_add_u32 s60, s60, s58\n"
      "v_cndmask_b32 v13, v12, v13, vcc\n"
      "s_lshl_b32 s52, s60, 2\n"
      "s_add_u32 s52, s52, s50\n"
      "s_mul_i32 s5, s52, 1792\n"
      "v_cmp_gt_u32 vcc, 14, v1\n"
      "v_lshlrev_b32 v15, 7, v1\n"
      "s_xor_b32 s52, s52, 32\n"
      "s_mul_i32 s6, s52, 1792\n"
      "v_cndmask_b32 v15, v12, v15, vcc\n"
      "v_and_b32 v14, 7, v0\n"
      "v_min_u32 v14, 6, v14\n"
      "v_lshrrev_b32 v2, 3, v0\n"
      "v_mul_u32_u24 v3, 56, v2\n"
      "v_cmp_gt_u32 vcc, 28, v2\n"
      "v_lshl_add_u32 v14, v14, 3, v3\n"
      "s_mul_i32 s7, s58, 1568\n"
      "v_cndmask_b32 v14, v12, v14, vcc\n"
      "s_waitcnt lgkmcnt(0)\n"
      "s_sub_u32 s12, s12, 256\n"
      "s_subb_u32 s13, s13, 0\n"
      "s_add_u32 s14, s14, 256\n"
      "buffer_load_dwordx4 v6, s[12:15], s4 offen offset:0 lds\n"
      "buffer_load_dwordx4 v7, s[12:15], s4 offen offset:1024 lds\n"
      "buffer_load_dwordx4 v8, s[12:15], s4 offen offset:2048 lds\n"
      "buffer_load_dwordx4 v[18:21], v13, s[8:11], s5 offen\n"
      "buffer_load_dwordx4 v[22:25], v13, s[8:11], s5 offen offset:448\n"
      "buffer_load_dwordx2 v[16:17], v9, s[44:47], 0 offen sc1\n"
      "buffer_load_dword v15, v15, s[8:11], s6 offen\n"
      "buffer_load_dwordx2 v[26:27], v14, s[16:19], s7 offen\n"
      "s_lshl2_add_u32 s52, %1, s50\n"
      "s_and_b32 s52, s52, 511\n"
      "s_lshl_b32 s52, s52, 7\n"
      "v_mov_b32 v28, 9076736\n"
      "buffer_load_dword v28, v28, s[44:47], s52 offen sc1\n"
      "s_lshl_b32 s53, s51, 7\n"
      "v_mov_b32 v30, 0\n"
      "buffer_load_dword v31, v30, s[28:31], s53 offen\n"
      "buffer_load_dword v30, v30, s[36:39], s53 offen\n"
      "v_bfe_u32 v6, v0, 3, 3\n"
      "v_bfe_u32 v3, v0, 4, 2\n"
      "v_xor_b32 v3, v3, v0\n"
      "v_and_b32 v3, 7, v3\n"
      "v_mul_u32_u24 v32, 896, v6\n"
      "v_lshl_add_u32 v32, v3, 4, v32\n"
      "s_mul_i32 s61, s50, 28672\n"
      "s_add_u32 s61, s61, %3\n"
      "v_xor_b32 v33, 64, v32\n"
      "v_add_u32 v33, 6144, v33\n"
      "s_lshl_b32 s62, s57, 2\n"
      "s_add_u32 s62, s62, s50\n"
      "s_mul_i32 s62, s62, 28672\n"
      "v_add_u32 v34, 12288, v32\n"
      "v_add_u32 v35, 12288, v33\n"
      "s_lshl_b32 s63, s59, 7\n"
      "s_add_u32 s63, s63, s62\n"
      "s_add_u32 s64, s62, 896\n"
      "s_lshl_b32 s65, s48, 1\n"
      "s_and_b32 s52, s48, 1\n"
      "s_add_u32 s16, s51, s52\n"
      "s_sub_u32 s16, s16, 25\n"
      "s_cselect_b32 s19, 0, -1\n"
      "s_lshl_b32 s16, s16, 2\n"
      "s_add_u32 s16, s16, s50\n"
      "v_bfe_u32 v14, v0, 2, 2\n"
      "v_bfe_u32 v29, v0, 4, 1\n"
      "s_mul_i32 s66, s50, 12288\n"
      "s_add_u32 s66, s66, %3\n"
      "s_add_u32 s66, s66, 114688\n"
      "v_lshlrev_b32 v36, 4, v1\n"
      "v_cmp_eq_u32 vcc, 0, v1\n"
      "v_bfe_u32 v2, v0, 1, 3\n"
      "v_xor_b32 v2, v2, v11\n"
      "v_cndmask_b32 v37, v12, 0, vcc\n"
      "v_lshlrev_b32 v2, 4, v2\n"
      "v_lshl_add_u32 v2, v10, 7, v2\n"
      "s_lshl_b32 s52, s50, 5\n"
      "v_xor_b32 v2, s52, v2\n"
      "v_add_u32 v38, s61, v2\n"
      "v_xor_b32 v39, 32, v38\n"
      "v_xor_b32 v40, 64, v38\n"
      "v_xor_b32 v41, 96, v38\n"
      "s_xor_b32 s52, s50, 0\n"
      "s_mul_i32 s52, s52, 3072\n"
      "s_add_u32 s52, s52, %3\n"
      "s_add_u32 s52, s52, 114688\n"
      "v_add_u32 v42, s52, v36\n"
      "s_xor_b32 s52, s50, 1\n"
      "s_mul_i32 s52, s52, 3072\n"
      "s_add_u32 s52, s52, %3\n"
      "s_add_u32 s52, s52, 114688\n"
      "v_add_u32 v43, s52, v36\n"
      "s_xor_b32 s52, s50, 2\n"
      "s_mul_i32 s52, s52, 3072\n"
      "s_add_u32 s52, s52, %3\n"
      "s_add_u32 s52, s52, 114688\n"
      "v_add_u32 v44, s52, v36\n"
      "s_xor_b32 s52, s50, 3\n"
      "s_mul_i32 s52, s52, 3072\n"
      "s_add_u32 s52, s52, %3\n"
      "s_add_u32 s52, s52, 114688\n"
      "v_add_u32 v45, s52, v36\n"
      "v_lshl_add_u32 v46, v11, 3, v14\n"
      "v_lshlrev_b32 v47, 3, v5\n"
      "v_lshl_add_u32 v47, v29, 5, v47\n"
      "s_and_b32 s52, s54, 1\n"
      "s_lshl_b32 s52, s52, 3\n"
      "v_add_u32 v2, s52, v47\n"
      "v_min_u32 v2, 56, v2\n"
      "v_lshl_add_u32 v2, v46, 6, v2\n"
      "v_add_u32 v48, s56, v2\n"
      "v_or_b32 v49, s55, v36\n"
      "s_mul_i32 s69, s54, 28\n"
      "s_add_u32 s69, s69, s49\n"
      "s_mul_i32 s69, s69, 3072\n"
      "s_add_u32 s69, s69, 7700480\n"
      "s_lshl_b32 s70, s54, 5\n"
      "s_add_u32 s70, s70, s49\n"
      "s_lshl_b32 s70, s70, 3\n"
      "s_add_u32 s70, s70, 9113600\n"
      "s_mul_i32 s67, s58, 28\n"
      "s_add_u32 s67, s67, s50\n"
      "s_mul_i32 s67, s67, 3072\n"
      "s_add_u32 s67, s67, 7700480\n"
      "s_mul_i32 s68, s59, 12288\n"
      "s_add_u32 s68, s68, s67\n"
      "s_mul_i32 s52, s48, 49\n"
      "s_add_u32 s52, s52, 1\n"
      "s_lshr_b32 s52, s52, 1\n"
      "s_add_u32 s52, s52, s51\n"
      "s_mul_i32 s49, s52, 293\n"
      "s_lshr_b32 s49, s49, 11\n"
      "s_mul_i32 s53, s49, 7\n"
      "s_sub_u32 s54, s52, s53\n"
      "s_and_b32 s52, s48, 1\n"
      "s_sub_u32 s52, 25, s52\n"
      "s_mov_b64 s[4:5], s[44:45]\n"
      "s_mov_b32 s7, s47\n"
      "s_cmp_lt_u32 s51, s52\n"
      "s_cselect_b32 s6, s46, 0\n"
      "s_cselect_b32 s26, s26, 0\n"
      "s_lshl_b32 s59, s54, 2\n"
      "s_add_u32 s59, s59, s50\n"
      "s_lshl_b32 s59, s59, 4\n"
      "s_or_b32 s59, s59, s65\n"
      "s_lshl_b32 s59, s59, 12\n"
      "s_and_b32 s52, s50, 1\n"
      "s_lshl_b32 s52, s52, 10\n"
      "v_add_u32 v50, s52, v36\n"
      "v_xor_b32 v51, 1024, v50\n"
      "v_lshlrev_b32 v2, 2, v29\n"
      "v_xor_b32 v2, v2, v0\n"
      "v_and_b32 v2, 7, v2\n"
      "v_lshlrev_b32 v58, 4, v2\n"
      "s_mul_i32 s52, s50, 1835008\n"
      "s_lshl_b32 s53, s49, 7\n"
      "s_add_u32 s52, s52, s53\n"
      "v_mul_u32_u24 v2, 3584, v6\n"
      "v_add3_u32 v52, v2, v58, s52\n"
      "v_add_u32 v53, 27648, v52\n"
      "v_add_u32 v54, 27648, v53\n"
      "v_add_u32 v55, 27648, v54\n"
      "v_bfe_u32 v2, v0, 3, 1\n"
      "v_lshlrev_b32 v2, 6, v2\n"
      "v_xor_b32 v59, v2, v47\n"
      "s_lshr_b32 s52, s50, 1\n"
      "s_lshl_b32 s52, s52, 6\n"
      "v_xor_b32 v2, s52, v59\n"
      "v_lshl_add_u32 v2, v46, 7, v2\n"
      "v_add_u32 v56, s61, v2\n"
      "v_xor_b32 v57, 64, v56\n"
      "s_mul_i32 s52, s50, 1920\n"
      "s_add_u32 s52, s52, %3\n"
      "s_add_u32 s52, s52, 151552\n"
      "v_mul_u32_u24 v2, 960, v11\n"
      "v_lshl_add_u32 v2, v10, 4, v2\n"
      "v_add_u32 v60, s52, v2\n"
      "s_add_u32 s52, %3, 159232\n"
      "v_lshl_add_u32 v61, v0, 3, s52\n"
      "v_mul_u32_u24 v2, 960, v14\n"
      "v_lshl_add_u32 v2, v11, 6, v2\n"
      "v_add_u32 v2, v2, v47\n"
      "s_add_u32 s71, s50, 4\n"
      "s_cmp_lt_u32 s50, 3\n"
      "s_cselect_b32 s71, s71, s50\n"
      "s_lshl_b32 s52, s50, 7\n"
      "s_add_u32 s52, s52, %3\n"
      "s_add_u32 s52, s52, 151552\n"
      "v_add_u32 v62, s52, v2\n"
      "s_lshl_b32 s52, s71, 7\n"
      "s_add_u32 s52, s52, %3\n"
      "s_add_u32 s52, s52, 151552\n"
      "v_add_u32 v63, s52, v2\n"
      "s_lshr_b32 s52, s60, 1\n"
      "s_lshl_b32 s52, s52, 11\n"
      "s_and_b32 s53, s60, 1\n"
      "s_lshl_b32 s53, s53, 9\n"
      "s_add_u32 s52, s52, s53\n"
      "v_cmp_eq_u32 vcc, 0, v10\n"
      "v_lshlrev_b32 v2, 4, v10\n"
      "v_lshl_add_u32 v2, v11, 10, v2\n"
      "v_add_u32 v64, s52, v2\n"
      "v_lshlrev_b32 v65, 3, v11\n"
      "v_cndmask_b32 v65, v12, v65, vcc\n"
      "s_lshl_b32 s72, s58, 8\n"
      "s_add_u32 s72, s72, 9113600\n"
      "v_min_u32 v68, 27, v1\n"
      "v_lshlrev_b32 v68, 3, v68\n"
      "s_lshl_b32 s74, s50, 8\n"
      "s_add_u32 s74, s74, s60\n"
      "s_mul_i32 s74, s74, 8\n"
      "s_add_u32 s74, s74, 9093120\n"
      "s_waitcnt vmcnt(8)\n"
      "ds_read_b64_tr_b16 v[72:73], v48 offset:24576\n"
      "ds_read_b64_tr_b16 v[74:75], v48 offset:24832\n"
      "ds_read_b64_tr_b16 v[76:77], v48 offset:25600\n"
      "ds_read_b64_tr_b16 v[78:79], v48 offset:25856\n"
      "ds_read_b64_tr_b16 v[80:81], v48 offset:26624\n"
      "ds_read_b64_tr_b16 v[82:83], v48 offset:26880\n"
      "s_waitcnt lgkmcnt(4)\n"
      "buffer_store_dwordx4 v[72:75], v49, s[44:47], s69 offen offset:0 sc1\n"
      "s_waitcnt lgkmcnt(2)\n"
      "buffer_store_dwordx4 v[76:79], v49, s[44:47], s69 offen offset:1024 sc1\n"
      "s_waitcnt lgkmcnt(0)\n"
      "buffer_store_dwordx4 v[80:83], v49, s[44:47], s69 offen offset:2048 sc1\n"
      "s_mov_b32 m0, s61\n"
      "v_mov_b32 v48, 0\n"
      "buffer_load_dwordx4 v32, s[8:11], s63 offen offset:0 lds\n"
      "v_mov_b32 v100, 0\n"
      "s_waitcnt vmcnt(9)\n"
      "v_add_u32 v16, 1, v16\n"
      "v_dot2c_f32_bf16 v48, 0x3f803f80, v18\n"
      "v_dot2c_f32_bf16 v100, v18, v18\n"
      "v_dot2c_f32_bf16 v48, 0x3f803f80, v19\n"
      "v_dot2c_f32_bf16 v100, v19, v19\n"
      "buffer_load_dwordx4 v33, s[8:11], s63 offen offset:1024 lds\n"
      "v_dot2c_f32_bf16 v48, 0x3f803f80, v20\n"
      "v_dot2c_f32_bf16 v100, v20, v20\n"
      "v_dot2c_f32_bf16 v48, 0x3f803f80, v21\n"
      "v_dot2c_f32_bf16 v100, v21, v21\n"
      "v_dot2c_f32_bf16 v48, 0x3f803f80, v22\n"
      "v_dot2c_f32_bf16 v100, v22, v22\n"
      "v_dot2c_f32_bf16 v48, 0x3f803f80, v23\n"
      "buffer_load_dwordx4 v34, s[8:11], s63 offen offset:2048 lds\n"
      "v_dot2c_f32_bf16 v100, v23, v23\n"
      "v_dot2c_f32_bf16 v48, 0x3f803f80, v24\n"
      "v_dot2c_f32_bf16 v100, v24, v24\n"
      "v_dot2c_f32_bf16 v48, 0x3f803f80, v25\n"
      "v_dot2c_f32_bf16 v100, v25, v25\n"
      "v_lshlrev_b32 v84, 16, v18\n"
      "v_and_b32 v85, 0xffff0000, v18\n"
      "buffer_load_dwordx4 v35, s[8:11], s63 offen offset:3072 lds\n"
      "s_add_u32 s63, s63, 128\n"
      "s_cmp_eq_u32 s63, s64\n"
      "s_cselect_b32 s63, s62, s63\n"
      "v_lshlrev_b32 v86, 16, v19\n"
      "v_and_b32 v87, 0xffff0000, v19\n"
      "v_add_f32_dpp v48, v48, v48 quad_perm:[1,0,3,2] row_mask:0xf bank_mask:0xf bound_ctrl:1\n"
      "v_add_f32_dpp v100, v100, v100 quad_perm:[1,0,3,2] row_mask:0xf bank_mask:0xf bound_ctrl:1\n"
      "v_lshlrev_b32 v88, 16, v20\n"
      "v_add_f32_dpp v48, v48, v48 quad_perm:[2,3,0,1] row_mask:0xf bank_mask:0xf bound_ctrl:1\n"
      "v_add_f32_dpp v100, v100, v100 quad_perm:[2,3,0,1] row_mask:0xf bank_mask:0xf bound_ctrl:1\n"
      "v_and_b32 v89, 0xffff0000, v20\n"
      "v_add_f32_dpp v48, v48, v48 row_half_mirror row_mask:0xf bank_mask:0xf bound_ctrl:1\n"
      "s_add_u32 m0, s61, 4096\n"
      "v_add_f32_dpp v100, v100, v100 row_half_mirror row_mask:0xf bank_mask:0xf bound_ctrl:1\n"
      "buffer_load_dwordx4 v32, s[8:11], s63 offen offset:0 lds\n"
      "v_lshlrev_b32 v90, 16, v21\n"
      "v_add_f32_dpp v48, v48, v48 row_mirror row_mask:0xf bank_mask:0xf bound_ctrl:1\n"
      "v_add_f32_dpp v100, v100, v100 row_mirror row_mask:0xf bank_mask:0xf bound_ctrl:1\n"
      "v_and_b32 v91, 0xffff0000, v21\n"
      "v_lshlrev_b32 v92, 16, v22\n"
      "v_permlane16_swap_b32 v48, v100\n"
      "v_add_f32 v48, v48, v100\n"
      "v_mov_b32 v100, v48\n"
      "v_and_b32 v93, 0xffff0000, v22\n"
      "v_lshlrev_b32 v94, 16, v23\n"
      "v_permlane16_swap_b32 v48, v100\n"
      "v_mul_f32 v48, 0xbb124925, v48\n"
      "buffer_load_dwordx4 v33, s[8:11], s63 offen offset:1024 lds\n"
      "v_mul_f32 v103, v48, v48\n"
      "v_mul_f32 v100, 0x3b124925, v100\n"
      "v_sub_f32 v100, v100, v103\n"
      "v_max_f32 v100, 0, v100\n"
      "v_add_f32 v100, 0x3727c5ac, v100\n"
      "v_rsq_f32 v102, v100\n"
      "v_and_b32 v95, 0xffff0000, v23\n"
      "v_lshlrev_b32 v96, 16, v24\n"
      "v_and_b32 v97, 0xffff0000, v24\n"
      "v_lshlrev_b32 v98, 16, v25\n"
      "v_and_b32 v99, 0xffff0000, v25\n"
      "v_cmp_gt_u32 vcc, 28, v10\n"
      "buffer_load_dwordx4 v34, s[8:11], s63 offen offset:2048 lds\n"
      "v_mov_b32 v104, v48\n"
      "v_mov_b32 v105, v102\n"
      "v_mul_f32 v103, v48, v102\n"
      "v_mov_b32 v48, 0\n"
      "v_mov_b32 v100, 0\n"
      "v_cndmask_b32 v103, 0, v103, vcc\n"
      "v_pk_fma_f32 v[84:85], v[84:85], v[102:103], v[102:103] op_sel:[0,0,1] op_sel_hi:[1,0,1]\n"
      "v_pk_fma_f32 v[86:87], v[86:87], v[102:103], v[102:103] op_sel:[0,0,1] op_sel_hi:[1,0,1]\n"
      "v_pk_fma_f32 v[88:89], v[88:89], v[102:103], v[102:103] op_sel:[0,0,1] op_sel_hi:[1,0,1]\n"
      "v_pk_fma_f32 v[90:91], v[90:91], v[102:103], v[102:103] op_sel:[0,0,1] op_sel_hi:[1,0,1]\n"
      "v_pk_fma_f32 v[92:93], v[92:93], v[102:103], v[102:103] op_sel:[0,0,1] op_sel_hi:[1,0,1]\n"
      "v_pk_fma_f32 v[94:95], v[94:95], v[102:103], v[102:103] op_sel:[0,0,1] op_sel_hi:[1,0,1]\n"
      "v_pk_fma_f32 v[96:97], v[96:97], v[102:103], v[102:103] op_sel:[0,0,1] op_sel_hi:[1,0,1]\n"
      "buffer_load_dwordx4 v35, s[8:11], s63 offen offset:3072 lds\n"
      "s_add_u32 s63, s63, 128\n"
      "s_cmp_eq_u32 s63, s64\n"
      "s_cselect_b32 s63, s62, s63\n"
      "v_pk_fma_f32 v[98:99], v[98:99], v[102:103], v[102:103] op_sel:[0,0,1] op_sel_hi:[1,0,1]\n"
      "v_cvt_pk_bf16_f32 v18, v84, v85\n"
      "v_cvt_pk_bf16_f32 v19, v86, v87\n"
      "v_cvt_pk_bf16_f32 v20, v88, v89\n"
      "v_cvt_pk_bf16_f32 v21, v90, v91\n"
      "v_cvt_pk_bf16_f32 v22, v92, v93\n"
      "v_cvt_pk_bf16_f32 v23, v94, v95\n"
      "v_cvt_pk_bf16_f32 v24, v96, v97\n"
      "v_cvt_pk_bf16_f32 v25, v98, v99\n"
      "v_or_b32 v49, s55, v37\n"
      "s_waitcnt vmcnt(8)\n"
      "s_add_u32 m0, s61, 8192\n"
      "buffer_store_dwordx2 v[16:17], v49, s[44:47], s70 offen sc1\n"
      "buffer_load_dwordx4 v32, s[8:11], s63 offen offset:0 lds\n"
      "v_dot2c_f32_bf16 v48, 0x3f803f80, v18\n"
      "v_dot2c_f32_bf16 v100, v18, v18\n"
      "v_dot2c_f32_bf16 v48, 0x3f803f80, v19\n"
      "v_dot2c_f32_bf16 v100, v19, v19\n"
      "v_dot2c_f32_bf16 v48, 0x3f803f80, v20\n"
      "buffer_load_dwordx4 v33, s[8:11], s63 offen offset:1024 lds\n"
      "v_dot2c_f32_bf16 v100, v20, v20\n"
      "v_dot2c_f32_bf16 v48, 0x3f803f80, v21\n"
      "v_dot2c_f32_bf16 v100, v21, v21\n"
      "v_dot2c_f32_bf16 v48, 0x3f803f80, v22\n"
      "v_dot2c_f32_bf16 v100, v22, v22\n"
      "v_dot2c_f32_bf16 v48, 0x3f803f80, v23\n"
      "v_dot2c_f32_bf16 v100, v23, v23\n"
      "buffer_load_dwordx4 v34, s[8:11], s63 offen offset:2048 lds\n"
      "v_dot2c_f32_bf16 v48, 0x3f803f80, v24\n"
      "v_dot2c_f32_bf16 v100, v24, v24\n"
      "v_dot2c_f32_bf16 v48, 0x3f803f80, v25\n"
      "v_dot2c_f32_bf16 v100, v25, v25\n"
      "v_lshlrev_b32 v84, 16, v18\n"
      "v_and_b32 v85, 0xffff0000, v18\n"
      "v_lshlrev_b32 v86, 16, v19\n"
      "v_and_b32 v87, 0xffff0000, v19\n"
      "buffer_load_dwordx4 v35, s[8:11], s63 offen offset:3072 lds\n"
      "s_add_u32 s63, s63, 128\n"
      "s_cmp_eq_u32 s63, s64\n"
      "s_cselect_b32 s63, s62, s63\n"
      "v_add_f32_dpp v48, v48, v48 quad_perm:[1,0,3,2] row_mask:0xf bank_mask:0xf bound_ctrl:1\n"
      "v_add_f32_dpp v100, v100, v100 quad_perm:[1,0,3,2] row_mask:0xf bank_mask:0xf bound_ctrl:1\n"
      "v_lshlrev_b32 v88, 16, v20\n"
      "v_add_f32_dpp v48, v48, v48 quad_perm:[2,3,0,1] row_mask:0xf bank_mask:0xf bound_ctrl:1\n"
      "v_add_f32_dpp v100, v100, v100 quad_perm:[2,3,0,1] row_mask:0xf bank_mask:0xf bound_ctrl:1\n"
      "v_and_b32 v89, 0xffff0000, v20\n"
      "v_add_f32_dpp v48, v48, v48 row_half_mirror row_mask:0xf bank_mask:0xf bound_ctrl:1\n"
      "v_add_f32_dpp v100, v100, v100 row_half_mirror row_mask:0xf bank_mask:0xf bound_ctrl:1\n"
      "v_lshlrev_b32 v90, 16, v21\n"
      "s_add_u32 m0, s61, 12288\n"
      "v_add_f32_dpp v48, v48, v48 row_mirror row_mask:0xf bank_mask:0xf bound_ctrl:1\n"
      "buffer_load_dwordx4 v32, s[8:11], s63 offen offset:0 lds\n"
      "v_add_f32_dpp v100, v100, v100 row_mirror row_mask:0xf bank_mask:0xf bound_ctrl:1\n"
      "v_and_b32 v91, 0xffff0000, v21\n"
      "v_lshlrev_b32 v92, 16, v22\n"
      "v_permlane16_swap_b32 v48, v100\n"
      "v_add_f32 v48, v48, v100\n"
      "v_mov_b32 v100, v48\n"
      "v_and_b32 v93, 0xffff0000, v22\n"
      "v_lshlrev_b32 v94, 16, v23\n"
      "v_permlane16_swap_b32 v48, v100\n"
      "v_mul_f32 v48, 0xbb124925, v48\n"
      "v_mul_f32 v103, v48, v48\n"
      "v_mul_f32 v100, 0x3b124925, v100\n"
      "buffer_load_dwordx4 v33, s[8:11], s63 offen offset:1024 lds\n"
      "v_sub_f32 v100, v100, v103\n"
      "v_max_f32 v100, 0, v100\n"
      "v_add_f32 v100, 0x3727c5ac, v100\n"
      "v_rsq_f32 v102, v100\n"
      "v_and_b32 v95, 0xffff0000, v23\n"
      "v_lshlrev_b32 v96, 16, v24\n"
      "v_and_b32 v97, 0xffff0000, v24\n"
      "v_lshlrev_b32 v98, 16, v25\n"
      "v_and_b32 v99, 0xffff0000, v25\n"
      "v_mul_f32 v103, v48, v102\n"
      "v_pk_fma_f32 v[84:85], v[84:85], v[102:103], v[102:103] op_sel:[0,0,1] op_sel_hi:[1,0,1]\n"
      "v_pk_fma_f32 v[86:87], v[86:87], v[102:103], v[102:103] op_sel:[0,0,1] op_sel_hi:[1,0,1]\n"
      "buffer_load_dwordx4 v34, s[8:11], s63 offen offset:2048 lds\n"
      "v_pk_fma_f32 v[88:89], v[88:89], v[102:103], v[102:103] op_sel:[0,0,1] op_sel_hi:[1,0,1]\n"
      "v_pk_fma_f32 v[90:91], v[90:91], v[102:103], v[102:103] op_sel:[0,0,1] op_sel_hi:[1,0,1]\n"
      "v_pk_fma_f32 v[92:93], v[92:93], v[102:103], v[102:103] op_sel:[0,0,1] op_sel_hi:[1,0,1]\n"
      "v_pk_fma_f32 v[94:95], v[94:95], v[102:103], v[102:103] op_sel:[0,0,1] op_sel_hi:[1,0,1]\n"
      "v_pk_fma_f32 v[96:97], v[96:97], v[102:103], v[102:103] op_sel:[0,0,1] op_sel_hi:[1,0,1]\n"
      "v_pk_fma_f32 v[98:99], v[98:99], v[102:103], v[102:103] op_sel:[0,0,1] op_sel_hi:[1,0,1]\n"
      "v_cvt_pk_bf16_f32 v18, v84, v85\n"
      "v_cvt_pk_bf16_f32 v19, v86, v87\n"
      "v_cvt_pk_bf16_f32 v20, v88, v89\n"
      "v_cvt_pk_bf16_f32 v21, v90, v91\n"
      "v_cvt_pk_bf16_f32 v22, v92, v93\n"
      "v_cvt_pk_bf16_f32 v23, v94, v95\n"
      "v_cvt_pk_bf16_f32 v24, v96, v97\n"
      "buffer_load_dwordx4 v35, s[8:11], s63 offen offset:3072 lds\n"
      "s_add_u32 s63, s63, 128\n"
      "s_cmp_eq_u32 s63, s64\n"
      "s_cselect_b32 s63, s62, s63\n"
      "v_cvt_pk_bf16_f32 v25, v98, v99\n"
      "ds_write_b128 v60, v[18:21]\n"
      "ds_write_b128 v60, v[22:25] offset:448\n"
      "ds_write_b64 v61, v[26:27]\n"
      "s_lshl_b32 s52, s60, 2\n"
      "s_add_u32 s52, s52, s50\n"
      "s_lshl_b32 s52, s52, 4\n"
      "s_add_u32 s52, s52, 9076736\n"
      "buffer_store_dwordx2 v[104:105], v65, s[44:47], s52 offen sc1\n"
      "s_waitcnt lgkmcnt(0)\n"
      "s_barrier\n"
      "ds_read_b64_tr_b16 v[18:19], v62\n"
      "ds_read_b64_tr_b16 v[20:21], v62 offset:3840\n"
      "ds_read_b64_tr_b16 v[22:23], v63\n"
      "ds_read_b64_tr_b16 v[24:25], v63 offset:3840\n"
      "s_lshl_b32 s52, s50, 18\n"
      "s_lshl_b32 s53, s71, 18\n"
      "s_add_u32 m0, s61, 16384\n"
      "s_nop 0\n"
      "buffer_load_dwordx4 v32, s[8:11], s63 offen offset:0 lds\n"
      "buffer_load_dwordx4 v33, s[8:11], s63 offen offset:1024 lds\n"
      "s_waitcnt lgkmcnt(2)\n"
      "buffer_store_dwordx4 v[18:21], v64, s[44:47], s52 offen sc1\n"
      "s_waitcnt lgkmcnt(0)\n"
      "buffer_store_dwordx4 v[22:25], v64, s[44:47], s53 offen sc1\n"
      "s_add_u32 s70, s67, 86016\n"
      "buffer_load_dwordx2 v[66:67], v68, s[44:47], s72 offen sc1\n"
      "s_waitcnt vmcnt(19)\n"
      "ds_read_b128 a[0:3], v38 offset:0\n"
      "ds_read_b128 a[4:7], v39 offset:0\n"
      "ds_read_b128 a[8:11], v40 offset:0\n"
      "ds_read_b128 a[12:15], v41 offset:0\n"
      "s_waitcnt vmcnt(15)\n"
      "ds_read_b128 a[16:19], v38 offset:4096\n"
      "ds_read_b128 a[20:23], v39 offset:4096\n"
      "ds_read_b128 a[24:27], v40 offset:4096\n"
      "ds_read_b128 a[28:31], v41 offset:4096\n"
      "s_waitcnt vmcnt(10)\n"
      "ds_read_b128 a[32:35], v38 offset:8192\n"
      "ds_read_b128 a[36:39], v39 offset:8192\n"
      "ds_read_b128 a[40:43], v40 offset:8192\n"
      "ds_read_b128 a[44:47], v41 offset:8192\n"
      "s_waitcnt vmcnt(6)\n"
      "ds_read_b128 a[48:51], v38 offset:12288\n"
      "ds_read_b128 a[52:55], v39 offset:12288\n"
      "ds_read_b128 a[56:59], v40 offset:12288\n"
      "ds_read_b128 a[60:63], v41 offset:12288\n"
      "s_mul_i32 s75, s51, 37\n"
      "s_lshr_b32 s75, s75, 8\n"
      "s_mul_i32 s52, s75, 7\n"
      "s_sub_u32 s76, s51, s52\n"
      "s_lshl_b32 s52, s48, 2\n"
      "s_add_u32 s75, s75, s52\n"
      "s_lshr_b32 s52, s50, 1\n"
      "s_lshl_b32 s52, s52, 10\n"
      "v_add_u32 v26, s52, v36\n"
      "v_xor_b32 v27, 1024, v26\n"
      "s_and_b32 s52, s50, 1\n"
      "s_lshl_b32 s52, s52, 15\n"
      "v_add_u32 v30, s52, v36\n"
      "v_xor_b32 v31, 0x8000, v30\n"
      "s_mul_i32 s52, s50, 1835008\n"
      "s_lshl_b32 s53, s75, 7\n"
      "s_add_u32 s52, s52, s53\n"
      "v_lshl_add_u32 v2, v6, 12, v58\n"
      "v_add_u32 v60, s52, v2\n"
      "v_add_u32 v61, 31744, v60\n"
      "v_add_u32 v62, 31744, v61\n"
      "v_add_u32 v63, 31744, v62\n"
      "s_mul_i32 s52, s50, 100352\n"
      "s_lshl_b32 s53, s76, 7\n"
      "s_add_u32 s52, s52, s53\n"
      "v_mul_u32_u24 v2, 896, v6\n"
      "v_add3_u32 v72, v2, v58, s52\n"
      "v_add_u32 v73, 6144, v72\n"
      "v_add_u32 v74, 12288, v72\n"
      "v_and_b32 v2, 3, v6\n"
      "v_mul_u32_u24 v2, 896, v2\n"
      "v_add3_u32 v2, v2, v58, s52\n"
      "v_add_u32 v75, 18432, v2\n"
      "v_lshl_add_u32 v15, v11, 2, v14\n"
      "v_lshl_add_u32 v2, v15, 7, v59\n"
      "s_and_b32 s52, s50, 1\n"
      "s_lshl_b32 s52, s52, 6\n"
      "v_xor_b32 v3, s52, v2\n"
      "v_add_u32 v76, s61, v3\n"
      "v_add_u32 v78, s66, v3\n"
      "v_xor_b32 v77, 64, v76\n"
      "v_xor_b32 v79, 64, v78\n"
      "s_lshr_b32 s52, s50, 1\n"
      "s_lshl_b32 s52, s52, 6\n"
      "v_xor_b32 v3, s52, v2\n"
      "v_add_u32 v80, s61, v3\n"
      "v_add_u32 v82, s66, v3\n"
      "v_xor_b32 v81, 64, v80\n"
      "v_xor_b32 v83, 64, v82\n"
      "s_waitcnt vmcnt(1)\n"
      "buffer_store_dwordx2 v[16:17], v37, s[44:47], s74 offen sc1\n"
      "buffer_load_dwordx2 v[70:71], v68, s[44:47], s72 offen sc1\n"
      "s_waitcnt vmcnt(2)\n"
      "v_cmp_ne_u64 vcc, v[66:67], v[16:17]\n"
      "s_cbranch_vccz .Lk_ok1\n"
      "s_waitcnt vmcnt(0)\n"
      "v_cmp_ne_u64 vcc, v[70:71], v[16:17]\n"
      "s_cbranch_vccz .Lk_ok1\n"
      "s_mov_b32 s52, 20000\n"
      "s_nop 2\n"
      ".Lk_spin1:\n"
      "s_sleep 2\n"
      "buffer_load_dwordx2 v[66:67], v68, s[44:47], s72 offen sc1\n"
      "s_waitcnt vmcnt(0)\n"
      "v_cmp_ne_u64 vcc, v[66:67], v[16:17]\n"
      "s_cbranch_vccz .Lk_ok1\n"
      "s_sub_u32 s52, s52, 1\n"
      "s_cbranch_scc0 .Lk_spin1\n"
      "s_nop 3\n"
      ".Lk_ok1:\n"
      "s_mov_b32 m0, s56\n"
      "s_nop 0\n"
      "buffer_load_dwordx4 v36, s[44:47], s68 offen offset:0 sc1 lds\n"
      "buffer_load_dwordx4 v36, s[44:47], s68 offen offset:1024 sc1 lds\n"
      "buffer_load_dwordx4 v36, s[44:47], s68 offen offset:2048 sc1 lds\n"
      "s_add_u32 s68, s68, 12288\n"
      "s_cmp_eq_u32 s68, s70\n"
      "s_cselect_b32 s68, s67, s68\n"
      "s_add_u32 s52, %3, 161280\n"
      "s_add_u32 m0, s61, 16384\n"
      "v_lshl_add_u32 v18, v10, 2, s52\n"
      "buffer_load_dwordx4 v34, s[8:11], s63 offen offset:2048 lds\n"
      "s_lshl_b32 s53, s50, 7\n"
      "s_add_u32 s52, s52, s53\n"
      "v_and_b32 v2, 1, v0\n"
      "buffer_load_dwordx4 v35, s[8:11], s63 offen offset:3072 lds\n"
      "s_add_u32 s63, s63, 128\n"
      "s_cmp_eq_u32 s63, s64\n"
      "s_cselect_b32 s63, s62, s63\n"
      "s_add_u32 m0, s61, 20480\n"
      "v_lshl_add_u32 v2, v2, 6, s52\n"
      "buffer_load_dwordx4 v32, s[8:11], s63 offen offset:0 lds\n"
      "v_and_b32 v3, 48, v0\n"
      "v_add_u32 v6, v2, v3\n"
      "s_lshl_b32 s52, s50, 7\n"
      "s_add_u32 s52, s52, %3\n"
      "s_add_u32 s52, s52, 162816\n"
      "v_lshl_add_u32 v19, v10, 2, s52\n"
      "buffer_load_dwordx4 v33, s[8:11], s63 offen offset:1024 lds\n"
      "v_lshl_add_u32 v20, v11, 4, s52\n"
      "s_add_u32 s52, %3, 159232\n"
      "v_lshl_add_u32 v2, v15, 6, v47\n"
      "buffer_load_dwordx4 v34, s[8:11], s63 offen offset:2048 lds\n"
      "v_add_u32 v21, s52, v2\n"
      "s_lshl_b32 s52, s50, 13\n"
      "s_add_u32 s52, s52, %3\n"
      "s_add_u32 s52, s52, 114688\n"
      "v_mul_u32_u24 v2, 136, v10\n"
      "buffer_load_dwordx4 v35, s[8:11], s63 offen offset:3072 lds\n"
      "s_add_u32 s63, s63, 128\n"
      "s_cmp_eq_u32 s63, s64\n"
      "s_cselect_b32 s63, s62, s63\n"
      "v_lshl_add_u32 v2, v11, 3, v2\n"
      "s_add_u32 m0, s61, 24576\n"
      "v_add_u32 v22, s52, v2\n"
      "buffer_load_dwordx4 v32, s[8:11], s63 offen offset:0 lds\n"
      "v_mul_u32_u24 v2, 136, v15\n"
      "v_add3_u32 v23, v2, v47, s52\n"
      "v_add_u32 v24, s52, v36\n"
      "s_lshl_b32 s52, s50, 11\n"
      "s_add_u32 s52, s52, %3\n"
      "s_add_u32 s52, s52, 114688\n"
      "v_add_u32 v25, s52, v36\n"
      "buffer_load_dwordx4 v33, s[8:11], s63 offen offset:1024 lds\n"
      "v_xor_b32 v2, v29, v0\n"
      "v_and_b32 v2, 1, v2\n"
      "buffer_load_dwordx4 v34, s[8:11], s63 offen offset:2048 lds\n"
      "v_cmp_eq_u32 vcc, 0, v2\n"
      "v_mov_b32 v28, 0x3f803f80\n"
      "v_mul_u32_u24 v46, 0x1ff, v11\n"
      "v_lshlrev_b32 v46, 23, v46\n"
      "v_cndmask_b32 v96, 0, v28, vcc\n"
      "buffer_load_dwordx4 v35, s[8:11], s63 offen offset:3072 lds\n"
      "v_cndmask_b32 v97, 0, v28, vcc\n"
      "v_cndmask_b32 v98, 0, v28, vcc\n"
      "v_cndmask_b32 v99, 0, v28, vcc\n"
      "v_mov_b32 v90, 0\n"
      "s_add_u32 m0, s56, 12288\n"
      "v_mov_b32 v91, 0\n"
      "buffer_load_dwordx4 v36, s[44:47], s68 offen offset:0 sc1 lds\n"
      "v_mov_b32 v94, 0\n"
      "v_mov_b32 v95, 0\n"
      "s_min_u32 s60, s48, 6\n"
      "s_mov_b32 s52, 0\n"
      "s_lshl1_add_u32 s60, s60, s52\n"
      "s_sub_u32 s52, s60, 14\n"
      "s_cselect_b32 s60, s60, s52\n"
      "s_lshl_b32 s72, s57, 2\n"
      "s_add_u32 s72, s72, s50\n"
      "s_mul_i32 s72, s72, 32\n"
      "buffer_load_dwordx4 v36, s[44:47], s68 offen offset:1024 sc1 lds\n"
      "s_add_u32 s72, s72, 9093120\n"
      "v_bfe_u32 v2, v0, 2, 2\n"
      "buffer_load_dwordx4 v36, s[44:47], s68 offen offset:2048 sc1 lds\n"
      "s_add_u32 s68, s68, 12288\n"
      "s_cmp_eq_u32 s68, s70\n"
      "s_cselect_b32 s68, s67, s68\n"
      "s_add_u32 m0, s56, 24576\n"
      "v_and_b32 v3, 3, v0\n"
      "buffer_load_dwordx4 v36, s[44:47], s68 offen offset:0 sc1 lds\n"
      "v_lshl_add_u32 v2, v2, 8, v3\n"
      "v_mul_u32_u24 v68, 8, v2\n"
      "buffer_load_dwordx4 v36, s[44:47], s68 offen offset:1024 sc1 lds\n"
      "s_and_b32 s73, s54, 3\n"
      "s_lshl_b32 s73, s73, 2\n"
      "s_add_u32 s73, s73, s50\n"
      "s_mul_i32 s73, s73, 512\n"
      "s_add_u32 s73, s73, 9093120\n"
      "v_mul_u32_u24 v69, 8, v1\n"
      "buffer_load_dwordx4 v36, s[44:47], s68 offen offset:2048 sc1 lds\n"
      "s_add_u32 s68, s68, 12288\n"
      "s_cmp_eq_u32 s68, s70\n"
      "s_cselect_b32 s68, s67, s68\n"
      "s_cmp_eq_u32 s26, 0\n"
      "s_cbranch_scc1 .Lk_help\n"
      "s_waitcnt vmcnt(16) lgkmcnt(0)\n"
      "s_barrier\n"
      "ds_read_b128 v[152:155], v42 offset:0\n"
      "ds_read_b128 v[156:159], v42 offset:1024\n"
      "ds_read_b128 v[160:163], v42 offset:2048\n"
      "s_waitcnt lgkmcnt(2)\n"
      "v_mfma_f32_32x32x16_bf16 v[100:115], v[152:155], a[0:3], 0\n"
      "ds_read_b128 v[168:171], v43 offset:0\n"
      "v_mfma_f32_16x16x32_bf16 v[180:183], v[152:155], v[96:99], 0\n"
      "s_waitcnt lgkmcnt(2)\n"
      "v_mfma_f32_32x32x16_bf16 v[116:131], v[156:159], a[0:3], 0\n"
      "ds_read_b128 v[172:175], v43 offset:1024\n"
      "v_mfma_f32_16x16x32_bf16 v[184:187], v[156:159], v[96:99], 0\n"
      "s_waitcnt lgkmcnt(2)\n"
      "v_mfma_f32_32x32x16_bf16 v[132:147], a[0:3], v[160:163], 0\n"
      "ds_read_b128 v[176:179], v43 offset:2048\n"
      "v_mfma_f32_16x16x32_bf16 v[188:191], v[160:163], v[96:99], 0\n"
      "s_waitcnt lgkmcnt(2)\n"
      "v_mfma_f32_32x32x16_bf16 v[100:115], v[168:171], a[4:7], v[100:115]\n"
      "ds_read_b128 v[152:155], v44 offset:0\n"
      "s_waitcnt lgkmcnt(2)\n"
      "v_mfma_f32_32x32x16_bf16 v[116:131], v[172:175], a[4:7], v[116:131]\n"
      "ds_read_b128 v[156:159], v44 offset:1024\n"
      "s_waitcnt lgkmcnt(2)\n"
      "v_mfma_f32_32x32x16_bf16 v[132:147], a[4:7], v[176:179], v[132:147]\n"
      "ds_read_b128 v[160:163], v44 offset:2048\n"
      "buffer_load_dwordx2 v[66:67], v68, s[44:47], s72 offen sc1\n"
      "buffer_load_dwordx2 v[70:71], v69, s[44:47], s73 offen sc1\n"
      "s_waitcnt lgkmcnt(2)\n"
      "v_mfma_f32_32x32x16_bf16 v[100:115], v[152:155], a[8:11], v[100:115]\n"
      "ds_read_b128 v[168:171], v45 offset:0\n"
      "s_waitcnt lgkmcnt(2)\n"
      "v_mfma_f32_32x32x16_bf16 v[116:131], v[156:159], a[8:11], v[116:131]\n"
      "ds_read_b128 v[172:175], v45 offset:1024\n"
      "s_waitcnt lgkmcnt(2)\n"
      "v_mfma_f32_32x32x16_bf16 v[132:147], a[8:11], v[160:163], v[132:147]\n"
      "ds_read_b128 v[176:179], v45 offset:2048\n"
      "s_waitcnt vmcnt(5) lgkmcnt(0)\n"
      "s_barrier\n"
      "v_mfma_f32_32x32x16_bf16 v[100:115], v[168:171], a[12:15], v[100:115]\n"
      "s_mov_b32 m0, s56\n"
      "ds_read_b128 v[152:155], v42 offset:12288\n"
      "buffer_load_dwordx4 v36, s[44:47], s68 offen offset:0 sc1 lds\n"
      "v_mfma_f32_32x32x16_bf16 v[116:131], v[172:175], a[12:15], v[116:131]\n"
      "ds_read_b128 v[156:159], v42 offset:13312\n"
      "buffer_load_dwordx4 v36, s[44:47], s68 offen offset:1024 sc1 lds\n"
      "v_mfma_f32_32x32x16_bf16 v[132:147], a[12:15], v[176:179], v[132:147]\n"
      "ds_read_b128 v[160:163], v42 offset:14336\n"
      "buffer_load_dwordx4 v36, s[44:47], s68 offen offset:2048 sc1 lds\n"
      "s_add_u32 s68, s68, 12288\n"
      "s_cmp_eq_u32 s68, s70\n"
      "s_cselect_b32 s68, s67, s68\n"
      "ds_read_b128 a[64:67], v38 offset:16384\n"
      "ds_read_b128 a[68:71], v39 offset:16384\n"
      "ds_read_b128 a[72:75], v40 offset:16384\n"
      "ds_read_b128 a[76:79], v41 offset:16384\n"
      "s_waitcnt lgkmcnt(6)\n"
      "v_mfma_f32_32x32x16_bf16 v[100:115], v[152:155], a[16:19], v[100:115]\n"
      "ds_read_b128 v[168:171], v43 offset:12288\n"
      "v_mfma_f32_16x16x32_bf16 v[180:183], v[152:155], v[96:99], v[180:183]\n"
      "s_waitcnt lgkmcnt(6)\n"
      "v_mfma_f32_32x32x16_bf16 v[116:131], v[156:159], a[16:19], v[116:131]\n"
      "ds_read_b128 v[172:175], v43 offset:13312\n"
      "v_mfma_f32_16x16x32_bf16 v[184:187], v[156:159], v[96:99], v[184:187]\n"
      "s_waitcnt lgkmcnt(6)\n"
      "v_mfma_f32_32x32x16_bf16 v[132:147], a[16:19], v[160:163], v[132:147]\n"
      "ds_read_b128 v[176:179], v43 offset:14336\n"
      "v_mfma_f32_16x16x32_bf16 v[188:191], v[160:163], v[96:99], v[188:191]\n"
      "ds_read_b128 a[80:83], v38 offset:20480\n"
      "ds_read_b128 a[84:87], v39 offset:20480\n"
      "ds_read_b128 a[88:91], v40 offset:20480\n"
      "ds_read_b128 a[92:95], v41 offset:20480\n"
      "s_waitcnt vmcnt(4)\n"
      "v_cmp_ne_u64 vcc, v[66:67], v[16:17]\n"
      "s_cbranch_vccz .Lk_ok2\n"
      "s_mov_b32 s52, 20000\n"
      "s_nop 2\n"
      ".Lk_spin2:\n"
      "s_sleep 2\n"
      "buffer_load_dwordx2 v[66:67], v68, s[44:47], s72 offen sc1\n"
      "s_waitcnt vmcnt(0)\n"
      "v_cmp_ne_u64 vcc, v[66:67], v[16:17]\n"
      "s_cbranch_vccz .Lk_ok2\n"
      "s_sub_u32 s52, s52, 1\n"
      "s_cbranch_scc0 .Lk_spin2\n"
      "s_nop 3\n"
      ".Lk_ok2:\n"
      "s_lshl_b32 s52, s57, 2\n"
      "s_add_u32 s52, s52, s50\n"
      "s_lshl_b32 s52, s52, 8\n"
      "s_add_u32 s52, s52, 9076736\n"
      "v_lshlrev_b32 v2, 3, v10\n"
      "buffer_load_dwordx2 v[14:15], v2, s[44:47], s52 offen sc1\n"
      "s_waitcnt vmcnt(4)\n"
      "v_cmp_ne_u64 vcc, v[70:71], v[16:17]\n"
      "s_cbranch_vccz .Lk_ok3\n"
      "s_mov_b32 s52, 20000\n"
      "s_nop 2\n"
      ".Lk_spin3:\n"
      "s_sleep 2\n"
      "buffer_load_dwordx2 v[70:71], v69, s[44:47], s73 offen sc1\n"
      "s_waitcnt vmcnt(0)\n"
      "v_cmp_ne_u64 vcc, v[70:71], v[16:17]\n"
      "s_cbranch_vccz .Lk_ok3\n"
      "s_sub_u32 s52, s52, 1\n"
      "s_cbranch_scc0 .Lk_spin3\n"
      "s_nop 3\n"
      ".Lk_ok3:\n"
      "s_waitcnt lgkmcnt(6)\n"
      "v_mfma_f32_32x32x16_bf16 v[100:115], v[168:171], a[20:23], v[100:115]\n"
      "ds_read_b128 v[152:155], v44 offset:12288\n"
      "s_xor_b32 s69, s59, 0\n"
      "buffer_load_dwordx4 a[112:115], v50, s[4:7], s69 offen offset:0 sc1\n"
      "s_waitcnt lgkmcnt(6)\n"
      "v_mfma_f32_32x32x16_bf16 v[116:131], v[172:175], a[20:23], v[116:131]\n"
      "ds_read_b128 v[156:159], v44 offset:13312\n"
      "buffer_load_dwordx4 a[116:119], v51, s[4:7], s69 offen offset:0 sc1\n"
      "s_waitcnt lgkmcnt(6)\n"
      "v_mfma_f32_32x32x16_bf16 v[132:147], a[20:23], v[176:179], v[132:147]\n"
      "ds_read_b128 v[160:163], v44 offset:14336\n"
      "buffer_load_dwordx4 a[120:123], v50, s[4:7], s69 offen offset:2048 sc1\n"
      "ds_read_b128 a[96:99], v38 offset:24576\n"
      "ds_read_b128 a[100:103], v39 offset:24576\n"
      "ds_read_b128 a[104:107], v40 offset:24576\n"
      "ds_read_b128 a[108:111], v41 offset:24576\n"
      "s_waitcnt lgkmcnt(6)\n"
      "v_mfma_f32_32x32x16_bf16 v[100:115], v[152:155], a[24:27], v[100:115]\n"
      "ds_read_b128 v[168:171], v45 offset:12288\n"
      "buffer_load_dwordx4 a[124:127], v51, s[4:7], s69 offen offset:2048 sc1\n"
      "s_waitcnt lgkmcnt(6)\n"
      "v_mfma_f32_32x32x16_bf16 v[116:131], v[156:159], a[24:27], v[116:131]\n"
      "ds_read_b128 v[172:175], v45 offset:13312\n"
      "s_waitcnt lgkmcnt(6)\n"
      "v_mfma_f32_32x32x16_bf16 v[132:147], a[24:27], v[160:163], v[132:147]\n"
      "ds_read_b128 v[176:179], v45 offset:14336\n"
      "s_waitcnt lgkmcnt(0)\n"
      "s_barrier\n"
      "v_mfma_f32_32x32x16_bf16 v[100:115], v[168:171], a[28:31], v[100:115]\n"
      "s_add_u32 m0, s56, 12288\n"
      "ds_read_b128 v[152:155], v42 offset:24576\n"
      "buffer_load_dwordx4 v36, s[44:47], s68 offen offset:0 sc1 lds\n"
      "v_mfma_f32_32x32x16_bf16 v[116:131], v[172:175], a[28:31], v[116:131]\n"
      "ds_read_b128 v[156:159], v42 offset:25600\n"
      "buffer_load_dwordx4 v36, s[44:47], s68 offen offset:1024 sc1 lds\n"
      "v_mfma_f32_32x32x16_bf16 v[132:147], a[28:31], v[176:179], v[132:147]\n"
      "ds_read_b128 v[160:163], v42 offset:26624\n"
      "buffer_load_dwordx4 v36, s[44:47], s68 offen offset:2048 sc1 lds\n"
      "s_add_u32 s68, s68, 12288\n"
      "s_cmp_eq_u32 s68, s70\n"
      "s_cselect_b32 s68, s67, s68\n"
      "s_waitcnt lgkmcnt(2)\n"
      "v_mfma_f32_32x32x16_bf16 v[100:115], v[152:155], a[32:35], v[100:115]\n"
      "ds_read_b128 v[168:171], v43 offset:24576\n"
      "s_xor_b32 s69, s59, 4096\n"
      "buffer_load_dwordx4 a[128:131], v50, s[4:7], s69 offen offset:0 sc1\n"
      "v_mfma_f32_16x16x32_bf16 v[180:183], v[152:155], v[96:99], v[180:183]\n"
      "s_waitcnt lgkmcnt(2)\n"
      "v_mfma_f32_32x32x16_bf16 v[116:131], v[156:159], a[32:35], v[116:131]\n"
      "ds_read_b128 v[172:175], v43 offset:25600\n"
      "buffer_load_dwordx4 a[132:135], v51, s[4:7], s69 offen offset:0 sc1\n"
      "v_mfma_f32_16x16x32_bf16 v[184:187], v[156:159], v[96:99], v[184:187]\n"
      "s_waitcnt lgkmcnt(2)\n"
      "v_mfma_f32_32x32x16_bf16 v[132:147], a[32:35], v[160:163], v[132:147]\n"
      "ds_read_b128 v[176:179], v43 offset:26624\n"
      "buffer_load_dwordx4 a[136:139], v50, s[4:7], s69 offen offset:2048 sc1\n"
      "v_mfma_f32_16x16x32_bf16 v[188:191], v[160:163], v[96:99], v[188:191]\n"
      "s_waitcnt lgkmcnt(2)\n"
      "v_mfma_f32_32x32x16_bf16 v[100:115], v[168:171], a[36:39], v[100:115]\n"
      "ds_read_b128 v[152:155], v44 offset:24576\n"
      "buffer_load_dwordx4 a[140:143], v51, s[4:7], s69 offen offset:2048 sc1\n"
      "s_waitcnt lgkmcnt(2)\n"
      "v_mfma_f32_32x32x16_bf16 v[116:131], v[172:175], a[36:39], v[116:131]\n"
      "ds_read_b128 v[156:159], v44 offset:25600\n"
      "s_xor_b32 s69, s59, 8192\n"
      "buffer_load_dwordx4 a[144:147], v50, s[4:7], s69 offen offset:0 sc1\n"
      "s_waitcnt lgkmcnt(2)\n"
      "v_mfma_f32_32x32x16_bf16 v[132:147], a[36:39], v[176:179], v[132:147]\n"
      "ds_read_b128 v[160:163], v44 offset:26624\n"
      "s_waitcnt lgkmcnt(2)\n"
      "v_mfma_f32_32x32x16_bf16 v[100:115], v[152:155], a[40:43], v[100:115]\n"
      "ds_read_b128 v[168:171], v45 offset:24576\n"
      "s_waitcnt lgkmcnt(2)\n"
      "v_mfma_f32_32x32x16_bf16 v[116:131], v[156:159], a[40:43], v[116:131]\n"
      "ds_read_b128 v[172:175], v45 offset:25600\n"
      "s_waitcnt lgkmcnt(2)\n"
      "v_mfma_f32_32x32x16_bf16 v[132:147], a[40:43], v[160:163], v[132:147]\n"
      "ds_read_b128 v[176:179], v45 offset:26624\n"
      "s_waitcnt vmcnt(13) lgkmcnt(0)\n"
      "s_barrier\n"
      "v_mfma_f32_32x32x16_bf16 v[100:115], v[168:171], a[44:47], v[100:115]\n"
      "s_add_u32 m0, s56, 24576\n"
      "ds_read_b128 v[152:155], v42 offset:0\n"
      "buffer_load_dwordx4 v36, s[44:47], s68 offen offset:0 sc1 lds\n"
      "v_mfma_f32_32x32x16_bf16 v[116:131], v[172:175], a[44:47], v[116:131]\n"
      "ds_read_b128 v[156:159], v42 offset:1024\n"
      "buffer_load_dwordx4 v36, s[44:47], s68 offen offset:1024 sc1 lds\n"
      "v_mfma_f32_32x32x16_bf16 v[132:147], a[44:47], v[176:179], v[132:147]\n"
      "ds_read_b128 v[160:163], v42 offset:2048\n"
      "buffer_load_dwordx4 v36, s[44:47], s68 offen offset:2048 sc1 lds\n"
      "s_add_u32 s68, s68, 12288\n"
      "s_cmp_eq_u32 s68, s70\n"
      "s_cselect_b32 s68, s67, s68\n"
      "s_waitcnt lgkmcnt(2)\n"
      "v_mfma_f32_32x32x16_bf16 v[100:115], v[152:155], a[48:51], v[100:115]\n"
      "ds_read_b128 v[168:171], v43 offset:0\n"
      "buffer_load_dwordx4 a[148:151], v51, s[4:7], s69 offen offset:0 sc1\n"
      "v_mfma_f32_16x16x32_bf16 v[180:183], v[152:155], v[96:99], v[180:183]\n"
      "s_waitcnt lgkmcnt(2)\n"
      "v_mfma_f32_32x32x16_bf16 v[116:131], v[156:159], a[48:51], v[116:131]\n"
      "ds_read_b128 v[172:175], v43 offset:1024\n"
      "buffer_load_dwordx4 a[152:155], v50, s[4:7], s69 offen offset:2048 sc1\n"
      "v_mfma_f32_16x16x32_bf16 v[184:187], v[156:159], v[96:99], v[184:187]\n"
      "s_waitcnt lgkmcnt(2)\n"
      "v_mfma_f32_32x32x16_bf16 v[132:147], a[48:51], v[160:163], v[132:147]\n"
      "ds_read_b128 v[176:179], v43 offset:2048\n"
      "buffer_load_dwordx4 a[156:159], v51, s[4:7], s69 offen offset:2048 sc1\n"
      "v_mfma_f32_16x16x32_bf16 v[188:191], v[160:163], v[96:99], v[188:191]\n"
      "s_waitcnt lgkmcnt(2)\n"
      "v_mfma_f32_32x32x16_bf16 v[100:115], v[168:171], a[52:55], v[100:115]\n"
      "ds_read_b128 v[152:155], v44 offset:0\n"
      "s_xor_b32 s69, s59, 12288\n"
      "buffer_load_dwordx4 a[160:163], v50, s[4:7], s69 offen offset:0 sc1\n"
      "s_waitcnt lgkmcnt(2)\n"
      "v_mfma_f32_32x32x16_bf16 v[116:131], v[172:175], a[52:55], v[116:131]\n"
      "ds_read_b128 v[156:159], v44 offset:1024\n"
      "buffer_load_dwordx4 a[164:167], v51, s[4:7], s69 offen offset:0 sc1\n"
      "s_waitcnt lgkmcnt(2)\n"
      "v_mfma_f32_32x32x16_bf16 v[132:147], a[52:55], v[176:179], v[132:147]\n"
      "ds_read_b128 v[160:163], v44 offset:2048\n"
      "s_waitcnt lgkmcnt(2)\n"
      "v_mfma_f32_32x32x16_bf16 v[100:115], v[152:155], a[56:59], v[100:115]\n"
      "ds_read_b128 v[168:171], v45 offset:0\n"
      "s_waitcnt lgkmcnt(2)\n"
      "v_mfma_f32_32x32x16_bf16 v[116:131], v[156:159], a[56:59], v[116:131]\n"
      "ds_read_b128 v[172:175], v45 offset:1024\n"
      "s_waitcnt lgkmcnt(2)\n"
      "v_mfma_f32_32x32x16_bf16 v[132:147], a[56:59], v[160:163], v[132:147]\n"
      "ds_read_b128 v[176:179], v45 offset:2048\n"
      "s_waitcnt vmcnt(13) lgkmcnt(0)\n"
      "s_barrier\n"
      "v_mfma_f32_32x32x16_bf16 v[100:115], v[168:171], a[60:63], v[100:115]\n"
      "s_mov_b32 m0, s56\n"
      "ds_read_b128 v[152:155], v42 offset:12288\n"
      "buffer_load_dwordx4 v36, s[44:47], s68 offen offset:0 sc1 lds\n"
      "v_mfma_f32_32x32x16_bf16 v[116:131], v[172:175], a[60:63], v[116:131]\n"
      "ds_read_b128 v[156:159], v42 offset:13312\n"
      "buffer_load_dwordx4 v36, s[44:47], s68 offen offset:1024 sc1 lds\n"
      "v_mfma_f32_32x32x16_bf16 v[132:147], a[60:63], v[176:179], v[132:147]\n"
      "ds_read_b128 v[160:163], v42 offset:14336\n"
      "buffer_load_dwordx4 v36, s[44:47], s68 offen offset:2048 sc1 lds\n"
      "s_waitcnt lgkmcnt(2)\n"
      "v_mfma_f32_32x32x16_bf16 v[100:115], v[152:155], a[64:67], v[100:115]\n"
      "ds_read_b128 v[168:171], v43 offset:12288\n"
      "s_xor_b32 s55, s65, 0\n"
      "s_mul_i32 s55, s55, 114688\n"
      "s_mov_b32 m0, s61\n"
      "s_nop 0\n"
      "buffer_load_dwordx4 v52, s[24:27], s55 offen offset:0 lds\n"
      "v_mfma_f32_16x16x32_bf16 v[180:183], v[152:155], v[96:99], v[180:183]\n"
      "s_waitcnt lgkmcnt(2)\n"
      "v_mfma_f32_32x32x16_bf16 v[116:131], v[156:159], a[64:67], v[116:131]\n"
      "ds_read_b128 v[172:175], v43 offset:13312\n"
      "buffer_load_dwordx4 v53, s[24:27], s55 offen offset:1024 lds\n"
      "v_mfma_f32_16x16x32_bf16 v[184:187], v[156:159], v[96:99], v[184:187]\n"
      "s_waitcnt lgkmcnt(2)\n"
      "v_mfma_f32_32x32x16_bf16 v[132:147], a[64:67], v[160:163], v[132:147]\n"
      "ds_read_b128 v[176:179], v43 offset:14336\n"
      "buffer_load_dwordx4 v54, s[24:27], s55 offen offset:2048 lds\n"
      "v_mfma_f32_16x16x32_bf16 v[188:191], v[160:163], v[96:99], v[188:191]\n"
      "s_waitcnt lgkmcnt(2)\n"
      "v_mfma_f32_32x32x16_bf16 v[100:115], v[168:171], a[68:71], v[100:115]\n"
      "ds_read_b128 v[152:155], v44 offset:12288\n"
      "buffer_load_dwordx4 v55, s[24:27], s55 offen offset:3072 lds\n"
      "s_waitcnt lgkmcnt(2)\n"
      "v_mfma_f32_32x32x16_bf16 v[116:131], v[172:175], a[68:71], v[116:131]\n"
      "ds_read_b128 v[156:159], v44 offset:13312\n"
      "buffer_load_dwordx4 a[168:171], v50, s[4:7], s69 offen offset:2048 sc1\n"
      "s_waitcnt lgkmcnt(2)\n"
      "v_mfma_f32_32x32x16_bf16 v[132:147], a[68:71], v[176:179], v[132:147]\n"
      "ds_read_b128 v[160:163], v44 offset:14336\n"
      "s_waitcnt lgkmcnt(2)\n"
      "v_mfma_f32_32x32x16_bf16 v[100:115], v[152:155], a[72:75], v[100:115]\n"
      "ds_read_b128 v[168:171], v45 offset:12288\n"
      "s_waitcnt lgkmcnt(2)\n"
      "v_mfma_f32_32x32x16_bf16 v[116:131], v[156:159], a[72:75], v[116:131]\n"
      "ds_read_b128 v[172:175], v45 offset:13312\n"
      "s_waitcnt lgkmcnt(2)\n"
      "v_mfma_f32_32x32x16_bf16 v[132:147], a[72:75], v[160:163], v[132:147]\n"
      "ds_read_b128 v[176:179], v45 offset:14336\n"
      "s_waitcnt vmcnt(13) lgkmcnt(0)\n"
      "s_barrier\n"
      "v_mfma_f32_32x32x16_bf16 v[100:115], v[168:171], a[76:79], v[100:115]\n"
      "ds_read_b128 v[152:155], v42 offset:24576\n"
      "buffer_load_dwordx4 a[172:175], v51, s[4:7], s69 offen offset:2048 sc1\n"
      "v_mfma_f32_32x32x16_bf16 v[116:131], v[172:175], a[76:79], v[116:131]\n"
      "ds_read_b128 v[156:159], v42 offset:25600\n"
      "s_xor_b32 s55, s65, 1\n"
      "s_mul_i32 s55, s55, 114688\n"
      "s_add_u32 m0, s61, 4096\n"
      "s_nop 0\n"
      "buffer_load_dwordx4 v52, s[24:27], s55 offen offset:0 lds\n"
      "v_mfma_f32_32x32x16_bf16 v[132:147], a[76:79], v[176:179], v[132:147]\n"
      "ds_read_b128 v[160:163], v42 offset:26624\n"
      "buffer_load_dwordx4 v53, s[24:27], s55 offen offset:1024 lds\n"
      "s_waitcnt lgkmcnt(2)\n"
      "v_mfma_f32_32x32x16_bf16 v[100:115], v[152:155], a[80:83], v[100:115]\n"
      "ds_read_b128 v[168:171], v43 offset:24576\n"
      "buffer_load_dwordx4 v54, s[24:27], s55 offen offset:2048 lds\n"
      "v_mfma_f32_16x16x32_bf16 v[180:183], v[152:155], v[96:99], v[180:183]\n"
      "s_waitcnt lgkmcnt(2)\n"
      "v_mfma_f32_32x32x16_bf16 v[116:131], v[156:159], a[80:83], v[116:131]\n"
      "ds_read_b128 v[172:175], v43 offset:25600\n"
      "buffer_load_dwordx4 v55, s[24:27], s55 offen offset:3072 lds\n"
      "v_mfma_f32_16x16x32_bf16 v[184:187], v[156:159], v[96:99], v[184:187]\n"
      "s_waitcnt lgkmcnt(2)\n"
      "v_mfma_f32_32x32x16_bf16 v[132:147], a[80:83], v[160:163], v[132:147]\n"
      "ds_read_b128 v[176:179], v43 offset:26624\n"
      "s_xor_b32 s69, s59, 16384\n"
      "buffer_load_dwordx4 a[176:179], v50, s[4:7], s69 offen offset:0 sc1\n"
      "v_mfma_f32_16x16x32_bf16 v[188:191], v[160:163], v[96:99], v[188:191]\n"
      "s_waitcnt lgkmcnt(2)\n"
      "v_mfma_f32_32x32x16_bf16 v[100:115], v[168:171], a[84:87], v[100:115]\n"
      "ds_read_b128 v[152:155], v44 offset:24576\n"
      "buffer_load_dwordx4 a[180:183], v51, s[4:7], s69 offen offset:0 sc1\n"
      "s_waitcnt lgkmcnt(2)\n"
      "v_mfma_f32_32x32x16_bf16 v[116:131], v[172:175], a[84:87], v[116:131]\n"
      "ds_read_b128 v[156:159], v44 offset:25600\n"
      "buffer_load_dwordx4 a[184:187], v50, s[4:7], s69 offen offset:2048 sc1\n"
      "s_waitcnt lgkmcnt(2)\n"
      "v_mfma_f32_32x32x16_bf16 v[132:147], a[84:87], v[176:179], v[132:147]\n"
      "ds_read_b128 v[160:163], v44 offset:26624\n"
      "s_waitcnt lgkmcnt(2)\n"
      "v_mfma_f32_32x32x16_bf16 v[100:115], v[152:155], a[88:91], v[100:115]\n"
      "ds_read_b128 v[168:171], v45 offset:24576\n"
      "s_waitcnt lgkmcnt(2)\n"
      "v_mfma_f32_32x32x16_bf16 v[116:131], v[156:159], a[88:91], v[116:131]\n"
      "ds_read_b128 v[172:175], v45 offset:25600\n"
      "s_waitcnt lgkmcnt(2)\n"
      "v_mfma_f32_32x32x16_bf16 v[132:147], a[88:91], v[160:163], v[132:147]\n"
      "ds_read_b128 v[176:179], v45 offset:26624\n"
      "s_waitcnt vmcnt(13) lgkmcnt(0)\n"
      "s_barrier\n"
      "v_mfma_f32_32x32x16_bf16 v[100:115], v[168:171], a[92:95], v[100:115]\n"
      "ds_read_b128 v[152:155], v42 offset:0\n"
      "buffer_load_dwordx4 a[188:191], v51, s[4:7], s69 offen offset:2048 sc1\n"
      "v_mfma_f32_32x32x16_bf16 v[116:131], v[172:175], a[92:95], v[116:131]\n"
      "ds_read_b128 v[156:159], v42 offset:1024\n"
      "s_xor_b32 s55, s65, 2\n"
      "s_mul_i32 s55, s55, 114688\n"
      "s_add_u32 m0, s61, 8192\n"
      "s_nop 0\n"
      "buffer_load_dwordx4 v52, s[24:27], s55 offen offset:0 lds\n"
      "v_mfma_f32_32x32x16_bf16 v[132:147], a[92:95], v[176:179], v[132:147]\n"
      "ds_read_b128 v[160:163], v42 offset:2048\n"
      "buffer_load_dwordx4 v53, s[24:27], s55 offen offset:1024 lds\n"
      "s_waitcnt lgkmcnt(2)\n"
      "v_mfma_f32_32x32x16_bf16 v[100:115], v[152:155], a[96:99], v[100:115]\n"
      "ds_read_b128 v[168:171], v43 offset:0\n"
      "buffer_load_dwordx4 v54, s[24:27], s55 offen offset:2048 lds\n"
      "v_mfma_f32_16x16x32_bf16 v[180:183], v[152:155], v[96:99], v[180:183]\n"
      "s_waitcnt lgkmcnt(2)\n"
      "v_mfma_f32_32x32x16_bf16 v[116:131], v[156:159], a[96:99], v[116:131]\n"
      "ds_read_b128 v[172:175], v43 offset:1024\n"
      "buffer_load_dwordx4 v55, s[24:27], s55 offen offset:3072 lds\n"
      "v_mfma_f32_16x16x32_bf16 v[184:187], v[156:159], v[96:99], v[184:187]\n"
      "s_waitcnt lgkmcnt(2)\n"
      "v_mfma_f32_32x32x16_bf16 v[132:147], a[96:99], v[160:163], v[132:147]\n"
      "ds_read_b128 v[176:179], v43 offset:2048\n"
      "s_xor_b32 s69, s59, 20480\n"
      "buffer_load_dwordx4 a[192:195], v50, s[4:7], s69 offen offset:0 sc1\n"
      "v_mfma_f32_16x16x32_bf16 v[188:191], v[160:163], v[96:99], v[188:191]\n"
      "s_waitcnt lgkmcnt(2)\n"
      "v_mfma_f32_32x32x16_bf16 v[100:115], v[168:171], a[100:103], v[100:115]\n"
      "ds_read_b128 v[152:155], v44 offset:0\n"
      "buffer_load_dwordx4 a[196:199], v51, s[4:7], s69 offen offset:0 sc1\n"
      "s_waitcnt lgkmcnt(2)\n"
      "v_mfma_f32_32x32x16_bf16 v[116:131], v[172:175], a[100:103], v[116:131]\n"
      "ds_read_b128 v[156:159], v44 offset:1024\n"
      "buffer_load_dwordx4 a[200:203], v50, s[4:7], s69 offen offset:2048 sc1\n"
      "s_waitcnt lgkmcnt(2)\n"
      "v_mfma_f32_32x32x16_bf16 v[132:147], a[100:103], v[176:179], v[132:147]\n"
      "ds_read_b128 v[160:163], v44 offset:2048\n"
      "ds_write_b128 v6, v[180:183] offset:0\n"
      "ds_write_b128 v6, v[184:187] offset:512\n"
      "ds_write_b128 v6, v[188:191] offset:1024\n"
      "s_waitcnt lgkmcnt(5)\n"
      "v_mfma_f32_32x32x16_bf16 v[100:115], v[152:155], a[104:107], v[100:115]\n"
      "ds_read_b128 v[168:171], v45 offset:0\n"
      "s_waitcnt lgkmcnt(5)\n"
      "v_mfma_f32_32x32x16_bf16 v[116:131], v[156:159], a[104:107], v[116:131]\n"
      "ds_read_b128 v[172:175], v45 offset:1024\n"
      "s_waitcnt lgkmcnt(5)\n"
      "v_mfma_f32_32x32x16_bf16 v[132:147], a[104:107], v[160:163], v[132:147]\n"
      "ds_read_b128 v[176:179], v45 offset:2048\n"
      "s_waitcnt lgkmcnt(2)\n"
      "v_mfma_f32_32x32x16_bf16 v[100:115], v[168:171], a[108:111], v[100:115]\n"
      "s_waitcnt lgkmcnt(1)\n"
      "v_mfma_f32_32x32x16_bf16 v[116:131], v[172:175], a[108:111], v[116:131]\n"
      "s_waitcnt lgkmcnt(0)\n"
      "v_mfma_f32_32x32x16_bf16 v[132:147], a[108:111], v[176:179], v[132:147]\n"
      "s_barrier\n"
      "ds_read2_b32 v[148:149], v18 offset0:0 offset1:32\n"
      "buffer_load_dwordx4 a[204:207], v51, s[4:7], s69 offen offset:2048 sc1\n"
      "ds_read2_b32 v[150:151], v18 offset0:64 offset1:96\n"
      "v_add_u32 v2, 1024, v18\n"
      "ds_read2_b32 v[152:153], v18 offset0:128 offset1:160\n"
      "ds_read2_b32 v[154:155], v18 offset0:192 offset1:224\n"
      "ds_read2_b32 v[156:157], v2 offset0:0 offset1:32\n"
      "ds_read2_b32 v[158:159], v2 offset0:64 offset1:96\n"
      "ds_read_b64_tr_b16 v[164:165], v21 offset:0\n"
      "ds_read_b64_tr_b16 v[166:167], v21 offset:512\n"
      "ds_read_b64_tr_b16 v[168:169], v21 offset:1024\n"
      "s_xor_b32 s55, s65, 3\n"
      "s_mul_i32 s55, s55, 114688\n"
      "s_add_u32 m0, s61, 12288\n"
      "s_nop 0\n"
      "buffer_load_dwordx4 v52, s[24:27], s55 offen offset:0 lds\n"
      "ds_read_b64_tr_b16 v[170:171], v21 offset:1536\n"
      "v_cmp_eq_u32 vcc, 0, v11\n"
      "ds_write_b32 v19, v15\n"
      "v_cvt_pk_bf16_f32 v3, v14, v14\n"
      "v_lshlrev_b32 v3, 16, v3\n"
      "v_sub_f32 v3, v14, v3\n"
      "buffer_load_dwordx4 v53, s[24:27], s55 offen offset:1024 lds\n"
      "v_cvt_pk_bf16_f32 v3, v14, v3\n"
      "v_cndmask_b32 v92, 0, v3, vcc\n"
      "v_cndmask_b32 v93, 0, v3, vcc\n"
      "v_mul_f32 v32, 0x3fb8aa3b, v15\n"
      "v_mul_f32 v33, 0xbfb8aa3b, v15\n"
      "s_waitcnt lgkmcnt(9)\n"
      "v_pk_add_f32 v[148:149], v[148:149], v[150:151]\n"
      "s_waitcnt lgkmcnt(7)\n"
      "v_pk_add_f32 v[152:153], v[152:153], v[154:155]\n"
      "s_waitcnt lgkmcnt(5)\n"
      "v_pk_add_f32 v[156:157], v[156:157], v[158:159]\n"
      "buffer_load_dwordx4 v54, s[24:27], s55 offen offset:2048 lds\n"
      "v_add_f32 v148, v148, v149\n"
      "v_add_f32 v152, v152, v153\n"
      "v_add_f32 v156, v156, v157\n"
      "v_cvt_pk_bf16_f32 v84, v148, v148\n"
      "v_cvt_pk_bf16_f32 v86, v152, v152\n"
      "v_cvt_pk_bf16_f32 v88, v156, v156\n"
      "v_lshlrev_b32 v149, 16, v84\n"
      "v_lshlrev_b32 v153, 16, v86\n"
      "v_lshlrev_b32 v157, 16, v88\n"
      "buffer_load_dwordx4 v55, s[24:27], s55 offen offset:3072 lds\n"
      "v_sub_f32 v148, v148, v149\n"
      "v_sub_f32 v152, v152, v153\n"
      "v_sub_f32 v156, v156, v157\n"
      "v_cvt_pk_bf16_f32 v87, v152, v152\n"
      "v_cvt_pk_bf16_f32 v85, v148, v148\n"
      "v_cvt_pk_bf16_f32 v89, v156, v156\n"
      "s_nop 1\n"
      "v_mfma_f32_32x32x16_bf16 v[116:131], v[86:89], v[92:95], v[116:131]\n"
      "s_xor_b32 s69, s59, 24576\n"
      "buffer_load_dwordx4 a[208:211], v50, s[4:7], s69 offen offset:0 sc1\n"
      "v_mfma_f32_32x32x16_bf16 v[100:115], v[84:87], v[92:95], v[100:115]\n"
      "v_mfma_f32_32x32x16_bf16 v[132:147], v[92:95], v[88:91], v[132:147]\n"
      "buffer_load_dwordx4 a[212:215], v51, s[4:7], s69 offen offset:0 sc1\n"
      "ds_read_b128 v[180:183], v20 offset:0\n"
      "ds_read_b128 v[184:187], v20 offset:32\n"
      "ds_read_b128 v[188:191], v20 offset:64\n"
      "ds_read_b128 v[192:195], v20 offset:96\n"
      "v_cmp_eq_u32 vcc, 28, v10\n"
      "buffer_load_dwordx4 a[216:219], v50, s[4:7], s69 offen offset:2048 sc1\n"
      "s_nop 0\n"
      "v_cvt_pk_bf16_f32 v148, v116, v117\n"
      "v_cvt_pk_bf16_f32 v149, v118, v119\n"
      "v_cvt_pk_bf16_f32 v150, v120, v121\n"
      "v_cvt_pk_bf16_f32 v151, v122, v123\n"
      "v_cvt_pk_bf16_f32 v152, v124, v125\n"
      "v_cvt_pk_bf16_f32 v153, v126, v127\n"
      "v_cvt_pk_bf16_f32 v154, v128, v129\n"
      "v_cvt_pk_bf16_f32 v155, v130, v131\n"
      "s_waitcnt lgkmcnt(7)\n"
      "v_mfma_f32_32x32x16_bf16 v[196:211], v[164:167], v[148:151], 0\n"
      "buffer_load_dwordx4 a[220:223], v51, s[4:7], s69 offen offset:2048 sc1\n"
      "s_waitcnt lgkmcnt(5)\n"
      "v_mfma_f32_32x32x16_bf16 v[196:211], v[168:171], v[152:155], v[196:211]\n"
      "v_cvt_pk_bf16_f32 v156, v100, v101\n"
      "s_xor_b32 s55, s65, 4\n"
      "s_mul_i32 s55, s55, 114688\n"
      "s_add_u32 m0, s61, 16384\n"
      "s_nop 0\n"
      "buffer_load_dwordx4 v52, s[24:27], s55 offen offset:0 lds\n"
      "v_cvt_pk_bf16_f32 v157, v102, v103\n"
      "v_cvt_pk_bf16_f32 v158, v104, v105\n"
      "v_cvt_pk_bf16_f32 v159, v106, v107\n"
      "v_cvt_pk_bf16_f32 v160, v108, v109\n"
      "v_cvt_pk_bf16_f32 v161, v110, v111\n"
      "v_cvt_pk_bf16_f32 v162, v112, v113\n"
      "buffer_load_dwordx4 v53, s[24:27], s55 offen offset:1024 lds\n"
      "v_cvt_pk_bf16_f32 v163, v114, v115\n"
      "v_mfma_f32_32x32x16_bf16 v[212:227], v[164:167], v[156:159], 0\n"
      "s_nop 0\n"
      "v_mfma_f32_32x32x16_bf16 v[212:227], v[168:171], v[160:163], v[212:227]\n"
      "buffer_load_dwordx4 v54, s[24:27], s55 offen offset:2048 lds\n"
      "s_waitcnt lgkmcnt(3)\n"
      "v_pk_mul_f32 v[132:133], v[132:133], v[180:181]\n"
      "v_pk_mul_f32 v[134:135], v[134:135], v[182:183]\n"
      "s_waitcnt lgkmcnt(2)\n"
      "v_pk_mul_f32 v[136:137], v[136:137], v[184:185]\n"
      "v_pk_mul_f32 v[138:139], v[138:139], v[186:187]\n"
      "s_waitcnt lgkmcnt(1)\n"
      "v_pk_mul_f32 v[140:141], v[140:141], v[188:189]\n"
      "v_pk_mul_f32 v[142:143], v[142:143], v[190:191]\n"
      "buffer_load_dwordx4 v55, s[24:27], s55 offen offset:3072 lds\n"
      "s_waitcnt lgkmcnt(0)\n"
      "v_pk_mul_f32 v[144:145], v[144:145], v[192:193]\n"
      "v_pk_mul_f32 v[146:147], v[146:147], v[194:195]\n"
      "v_cvt_pk_bf16_f32 v172, v132, v133\n"
      "v_cvt_pk_bf16_f32 v173, v134, v135\n"
      "v_cvt_pk_bf16_f32 v174, v136, v137\n"
      "v_cvt_pk_bf16_f32 v175, v138, v139\n"
      "v_cvt_pk_bf16_f32 v176, v140, v141\n"
      "v_cvt_pk_bf16_f32 v177, v142, v143\n"
      "v_cvt_pk_bf16_f32 v178, v144, v145\n"
      "s_xor_b32 s69, s59, 28672\n"
      "buffer_load_dwordx4 a[224:227], v50, s[4:7], s69 offen offset:0 sc1\n"
      "v_cvt_pk_bf16_f32 v179, v146, v147\n"
      "v_cndmask_b32 v172, v172, v28, vcc\n"
      "v_cndmask_b32 v173, v173, v28, vcc\n"
      "v_cndmask_b32 v174, v174, v28, vcc\n"
      "v_cndmask_b32 v175, v175, v28, vcc\n"
      "v_cndmask_b32 v176, v176, v28, vcc\n"
      "v_cndmask_b32 v177, v177, v28, vcc\n"
      "v_cndmask_b32 v178, v178, v28, vcc\n"
      "v_cndmask_b32 v179, v179, v28, vcc\n"
      "buffer_load_dwordx4 a[228:231], v51, s[4:7], s69 offen offset:0 sc1\n"
      "v_max3_f32 v14, v196, v197, v198\n"
      "v_min3_f32 v15, v196, v197, v198\n"
      "v_max3_f32 v14, v14, v199, v200\n"
      "v_min3_f32 v15, v15, v199, v200\n"
      "v_max3_f32 v14, v14, v201, v202\n"
      "v_min3_f32 v15, v15, v201, v202\n"
      "v_max3_f32 v14, v14, v203, v204\n"
      "v_min3_f32 v15, v15, v203, v204\n"
      "v_max3_f32 v14, v14, v205, v206\n"
      "v_min3_f32 v15, v15, v205, v206\n"
      "buffer_load_dwordx4 a[232:235], v50, s[4:7], s69 offen offset:2048 sc1\n"
      "v_max3_f32 v14, v14, v207, v208\n"
      "v_min3_f32 v15, v15, v207, v208\n"
      "v_max3_f32 v14, v14, v209, v210\n"
      "v_min3_f32 v15, v15, v209, v210\n"
      "v_max_f32 v14, v14, v211\n"
      "v_min_f32 v15, v15, v211\n"
      "v_mov_b32 v18, v14\n"
      "v_mov_b32 v19, v15\n"
      "s_nop 0\n"
      "v_permlane32_swap_b32 v14, v18\n"
      "buffer_load_dwordx4 a[236:239], v51, s[4:7], s69 offen offset:2048 sc1\n"
      "v_permlane32_swap_b32 v15, v19\n"
      "v_max_f32 v14, v14, v18\n"
      "v_min_f32 v15, v15, v19\n"
      "v_pk_mul_f32 v[14:15], v[14:15], v[32:33] op_sel:[0,1] op_sel_hi:[1,0]\n"
      "v_add_f32 v18, v14, v46\n"
      "v_add_f32 v19, v15, v46\n"
      "v_pk_fma_f32 v[116:117], v[196:197], v[32:33], v[14:15] op_sel_hi:[1,0,0]\n"
      "v_pk_fma_f32 v[100:101], v[196:197], v[32:33], v[14:15] op_sel:[0,1,1] op_sel_hi:[1,1,1]\n"
      "v_pk_fma_f32 v[118:119], v[198:199], v[32:33], v[14:15] op_sel_hi:[1,0,0]\n"
      "v_pk_fma_f32 v[102:103], v[198:199], v[32:33], v[14:15] op_sel:[0,1,1] op_sel_hi:[1,1,1]\n"
      "s_xor_b32 s55, s65, 5\n"
      "s_mul_i32 s55, s55, 114688\n"
      "s_add_u32 m0, s61, 20480\n"
      "s_nop 0\n"
      "buffer_load_dwordx4 v52, s[24:27], s55 offen offset:0 lds\n"
      "v_pk_fma_f32 v[120:121], v[200:201], v[32:33], v[14:15] op_sel_hi:[1,0,0]\n"
      "v_pk_fma_f32 v[104:105], v[200:201], v[32:33], v[14:15] op_sel:[0,1,1] op_sel_hi:[1,1,1]\n"
      "v_pk_fma_f32 v[122:123], v[202:203], v[32:33], v[14:15] op_sel_hi:[1,0,0]\n"
      "v_pk_fma_f32 v[106:107], v[202:203], v[32:33], v[14:15] op_sel:[0,1,1] op_sel_hi:[1,1,1]\n"
      "v_pk_fma_f32 v[124:125], v[204:205], v[32:33], v[14:15] op_sel_hi:[1,0,0]\n"
      "v_pk_fma_f32 v[108:109], v[204:205], v[32:33], v[14:15] op_sel:[0,1,1] op_sel_hi:[1,1,1]\n"
      "buffer_load_dwordx4 v53, s[24:27], s55 offen offset:1024 lds\n"
      "v_pk_fma_f32 v[126:127], v[206:207], v[32:33], v[14:15] op_sel_hi:[1,0,0]\n"
      "v_pk_fma_f32 v[110:111], v[206:207], v[32:33], v[14:15] op_sel:[0,1,1] op_sel_hi:[1,1,1]\n"
      "v_pk_fma_f32 v[128:129], v[208:209], v[32:33], v[18:19] op_sel_hi:[1,0,0]\n"
      "v_pk_fma_f32 v[112:113], v[208:209], v[32:33], v[18:19] op_sel:[0,1,1] op_sel_hi:[1,1,1]\n"
      "v_pk_fma_f32 v[130:131], v[210:211], v[32:33], v[18:19] op_sel_hi:[1,0,0]\n"
      "v_pk_fma_f32 v[114:115], v[210:211], v[32:33], v[18:19] op_sel:[0,1,1] op_sel_hi:[1,1,1]\n"
      "v_exp_f32 v116, v116\n"
      "v_exp_f32 v117, v117\n"
      "buffer_load_dwordx4 v54, s[24:27], s55 offen offset:2048 lds\n"
      "v_exp_f32 v100, v100\n"
      "v_exp_f32 v101, v101\n"
      "v_exp_f32 v118, v118\n"
      "v_exp_f32 v119, v119\n"
      "v_exp_f32 v102, v102\n"
      "buffer_load_dwordx4 v55, s[24:27], s55 offen offset:3072 lds\n"
      "v_exp_f32 v103, v103\n"
      "v_exp_f32 v120, v120\n"
      "v_exp_f32 v121, v121\n"
      "v_exp_f32 v104, v104\n"
      "v_exp_f32 v105, v105\n"
      "s_xor_b32 s69, s59, 32768\n"
      "buffer_load_dwordx4 a[240:243], v50, s[4:7], s69 offen offset:0 sc1\n"
      "v_pk_add_f32 v[38:39], v[116:117], v[118:119]\n"
      "v_pk_add_f32 v[40:41], v[100:101], v[102:103]\n"
      "v_exp_f32 v122, v122\n"
      "v_exp_f32 v123, v123\n"
      "v_exp_f32 v106, v106\n"
      "v_exp_f32 v107, v107\n"
      "buffer_load_dwordx4 a[244:247], v51, s[4:7], s69 offen offset:0 sc1\n"
      "v_pk_add_f32 v[38:39], v[38:39], v[120:121]\n"
      "v_pk_add_f32 v[40:41], v[40:41], v[104:105]\n"
      "v_exp_f32 v124, v124\n"
      "v_exp_f32 v125, v125\n"
      "v_exp_f32 v108, v108\n"
      "v_exp_f32 v109, v109\n"
      "buffer_load_dwordx4 a[248:251], v50, s[4:7], s69 offen offset:2048 sc1\n"
      "v_pk_add_f32 v[38:39], v[38:39], v[122:123]\n"
      "v_pk_add_f32 v[40:41], v[40:41], v[106:107]\n"
      "v_exp_f32 v126, v126\n"
      "v_exp_f32 v127, v127\n"
      "v_exp_f32 v110, v110\n"
      "v_exp_f32 v111, v111\n"
      "buffer_load_dwordx4 a[252:255], v51, s[4:7], s69 offen offset:2048 sc1\n"
      "v_pk_add_f32 v[38:39], v[38:39], v[124:125]\n"
      "v_pk_add_f32 v[40:41], v[40:41], v[108:109]\n"
      "v_exp_f32 v128, v128\n"
      "v_exp_f32 v129, v129\n"
      "v_exp_f32 v112, v112\n"
      "v_exp_f32 v113, v113\n"
      "s_xor_b32 s55, s65, 6\n"
      "s_mul_i32 s55, s55, 114688\n"
      "s_add_u32 m0, s61, 24576\n"
      "s_nop 0\n"
      "buffer_load_dwordx4 v52, s[24:27], s55 offen offset:0 lds\n"
      "v_pk_add_f32 v[38:39], v[38:39], v[126:127]\n"
      "v_pk_add_f32 v[40:41], v[40:41], v[110:111]\n"
      "v_exp_f32 v130, v130\n"
      "v_exp_f32 v131, v131\n"
      "buffer_load_dwordx4 v53, s[24:27], s55 offen offset:1024 lds\n"
      "v_exp_f32 v114, v114\n"
      "v_exp_f32 v115, v115\n"
      "v_pk_add_f32 v[38:39], v[38:39], v[128:129]\n"
      "v_pk_add_f32 v[40:41], v[40:41], v[112:113]\n"
      "v_pk_add_f32 v[38:39], v[38:39], v[130:131]\n"
      "v_pk_add_f32 v[40:41], v[40:41], v[114:115]\n"
      "v_add_f32 v38, v38, v39\n"
      "buffer_load_dwordx4 v54, s[24:27], s55 offen offset:2048 lds\n"
      "v_add_f32 v39, v40, v41\n"
      "s_nop 1\n"
      "v_permlane32_swap_b32 v38, v39\n"
      "v_add_f32 v38, v38, v39\n"
      "v_rcp_f32 v38, v38\n"
      "s_nop 0\n"
      "v_mov_b32 v39, v38\n"
      "s_nop 1\n"
      "v_permlane32_swap_b32 v38, v39\n"
      "buffer_load_dwordx4 v55, s[24:27], s55 offen offset:3072 lds\n"
      "v_pk_mul_f32 v[116:117], v[116:117], v[38:39] op_sel_hi:[1,0]\n"
      "v_pk_mul_f32 v[100:101], v[100:101], v[38:39] op_sel:[0,1] op_sel_hi:[1,1]\n"
      "v_pk_mul_f32 v[118:119], v[118:119], v[38:39] op_sel_hi:[1,0]\n"
      "v_pk_mul_f32 v[102:103], v[102:103], v[38:39] op_sel:[0,1] op_sel_hi:[1,1]\n"
      "v_pk_mul_f32 v[120:121], v[120:121], v[38:39] op_sel_hi:[1,0]\n"
      "v_pk_mul_f32 v[104:105], v[104:105], v[38:39] op_sel:[0,1] op_sel_hi:[1,1]\n"
      "v_pk_mul_f32 v[122:123], v[122:123], v[38:39] op_sel_hi:[1,0]\n"
      "v_pk_mul_f32 v[106:107], v[106:107], v[38:39] op_sel:[0,1] op_sel_hi:[1,1]\n"
      "s_xor_b32 s69, s59, 36864\n"
      "buffer_load_dwordx4 a[0:3], v50, s[4:7], s69 offen offset:0 sc1\n"
      "v_pk_mul_f32 v[124:125], v[124:125], v[38:39] op_sel_hi:[1,0]\n"
      "v_pk_mul_f32 v[108:109], v[108:109], v[38:39] op_sel:[0,1] op_sel_hi:[1,1]\n"
      "v_pk_mul_f32 v[126:127], v[126:127], v[38:39] op_sel_hi:[1,0]\n"
      "v_pk_mul_f32 v[110:111], v[110:111], v[38:39] op_sel:[0,1] op_sel_hi:[1,1]\n"
      "v_pk_mul_f32 v[128:129], v[128:129], v[38:39] op_sel_hi:[1,0]\n"
      "v_pk_mul_f32 v[112:113], v[112:113], v[38:39] op_sel:[0,1] op_sel_hi:[1,1]\n"
      "v_pk_mul_f32 v[130:131], v[130:131], v[38:39] op_sel_hi:[1,0]\n"
      "v_pk_mul_f32 v[114:115], v[114:115], v[38:39] op_sel:[0,1] op_sel_hi:[1,1]\n"
      "v_cvt_pk_bf16_f32 v180, v116, v117\n"
      "buffer_load_dwordx4 a[4:7], v51, s[4:7], s69 offen offset:0 sc1\n"
      "v_cvt_pk_bf16_f32 v188, v100, v101\n"
      "v_cvt_pk_bf16_f32 v181, v118, v119\n"
      "v_cvt_pk_bf16_f32 v189, v102, v103\n"
      "v_cvt_pk_bf16_f32 v182, v120, v121\n"
      "v_cvt_pk_bf16_f32 v190, v104, v105\n"
      "v_cvt_pk_bf16_f32 v183, v122, v123\n"
      "v_cvt_pk_bf16_f32 v191, v106, v107\n"
      "v_cvt_pk_bf16_f32 v184, v124, v125\n"
      "v_cvt_pk_bf16_f32 v192, v108, v109\n"
      "v_cvt_pk_bf16_f32 v185, v126, v127\n"
      "buffer_load_dwordx4 a[8:11], v50, s[4:7], s69 offen offset:2048 sc1\n"
      "v_cvt_pk_bf16_f32 v193, v110, v111\n"
      "v_cvt_pk_bf16_f32 v186, v128, v129\n"
      "v_cvt_pk_bf16_f32 v194, v112, v113\n"
      "v_cvt_pk_bf16_f32 v187, v130, v131\n"
      "v_cvt_pk_bf16_f32 v195, v114, v115\n"
      "ds_write_b64 v22, v[180:181] offset:0\n"
      "ds_write_b64 v22, v[188:189] offset:64\n"
      "ds_write_b64 v22, v[182:183] offset:16\n"
      "ds_write_b64 v22, v[190:191] offset:80\n"
      "ds_write_b64 v22, v[184:185] offset:32\n"
      "buffer_load_dwordx4 a[12:15], v51, s[4:7], s69 offen offset:2048 sc1\n"
      "ds_write_b64 v22, v[192:193] offset:96\n"
      "ds_write_b64 v22, v[186:187] offset:48\n"
      "ds_write_b64 v22, v[194:195] offset:112\n"
      "ds_read_b64_tr_b16 v[148:149], v23 offset:0\n"
      "ds_read_b64_tr_b16 v[150:151], v23 offset:1088\n"
      "ds_read_b64_tr_b16 v[152:153], v23 offset:2176\n"
      "ds_read_b64_tr_b16 v[154:155], v23 offset:3264\n"
      "ds_read_b64_tr_b16 v[156:157], v23 offset:64\n"
      "ds_read_b64_tr_b16 v[158:159], v23 offset:1152\n"
      "ds_read_b64_tr_b16 v[160:161], v23 offset:2240\n"
      "s_xor_b32 s69, s59, 40960\n"
      "buffer_load_dwordx4 a[16:19], v50, s[4:7], s69 offen offset:0 sc1\n"
      "ds_read_b64_tr_b16 v[162:163], v23 offset:3328\n"
      "s_waitcnt lgkmcnt(6)\n"
      "v_mfma_f32_32x32x16_bf16 v[132:147], v[148:151], v[172:175], 0\n"
      "s_waitcnt lgkmcnt(2)\n"
      "v_mfma_f32_32x32x16_bf16 v[196:211], v[156:159], v[172:175], 0\n"
      "buffer_load_dwordx4 a[20:23], v51, s[4:7], s69 offen offset:0 sc1\n"
      "v_mfma_f32_32x32x16_bf16 v[132:147], v[152:155], v[176:179], v[132:147]\n"
      "buffer_load_dwordx4 a[24:27], v50, s[4:7], s69 offen offset:2048 sc1\n"
      "s_waitcnt lgkmcnt(0)\n"
      "v_mfma_f32_32x32x16_bf16 v[196:211], v[160:163], v[176:179], v[196:211]\n"
      "s_nop 8\n"
      "ds_write_b128 v24, v[132:135] offset:0\n"
      "buffer_load_dwordx4 a[28:31], v51, s[4:7], s69 offen offset:2048 sc1\n"
      "ds_write_b128 v24, v[136:139] offset:1024\n"
      "ds_write_b128 v24, v[140:143] offset:2048\n"
      "ds_write_b128 v24, v[144:147] offset:3072\n"
      "s_xor_b32 s69, s59, 45056\n"
      "buffer_load_dwordx4 a[32:35], v50, s[4:7], s69 offen offset:0 sc1\n"
      "ds_write_b128 v24, v[196:199] offset:4096\n"
      "ds_write_b128 v24, v[200:203] offset:5120\n"
      "ds_write_b128 v24, v[204:207] offset:6144\n"
      "ds_write_b128 v24, v[208:211] offset:7168\n"
      "s_waitcnt lgkmcnt(0)\n"
      "s_barrier\n"
      "ds_read_b128 v[116:119], v25 offset:0\n"
      "ds_read_b128 v[120:123], v25 offset:8192\n"
      "ds_read_b128 v[124:127], v25 offset:16384\n"
      "buffer_load_dwordx4 a[36:39], v51, s[4:7], s69 offen offset:0 sc1\n"
      "ds_read_b128 v[128:131], v25 offset:24576\n"
      "ds_read_b128 v[100:103], v25 offset:1024\n"
      "ds_read_b128 v[104:107], v25 offset:9216\n"
      "ds_read_b128 v[108:111], v25 offset:17408\n"
      "ds_read_b128 v[112:115], v25 offset:25600\n"
      "s_lshl_b32 s52, s58, 4\n"
      "s_add_u32 s52, s52, s57\n"
      "s_lshl_b32 s52, s52, 2\n"
      "s_add_u32 s52, s52, s50\n"
      "s_lshl_b32 s52, s52, 10\n"
      "s_add_u32 s52, s52, 5537792\n"
      "s_waitcnt lgkmcnt(6)\n"
      "v_pk_add_f32 v[116:117], v[116:117], v[120:121]\n"
      "buffer_load_dwordx4 a[40:43], v50, s[4:7], s69 offen offset:2048 sc1\n"
      "s_waitcnt lgkmcnt(4)\n"
      "v_pk_add_f32 v[124:125], v[124:125], v[128:129]\n"
      "v_pk_add_f32 v[118:119], v[118:119], v[122:123]\n"
      "v_pk_add_f32 v[126:127], v[126:127], v[130:131]\n"
      "v_pk_add_f32 v[116:117], v[116:117], v[124:125]\n"
      "v_pk_add_f32 v[118:119], v[118:119], v[126:127]\n"
      "v_cvt_pk_bf16_f32 v180, v116, v117\n"
      "buffer_load_dwordx4 a[44:47], v51, s[4:7], s69 offen offset:2048 sc1\n"
      "v_cvt_pk_bf16_f32 v181, v118, v119\n"
      "s_waitcnt lgkmcnt(2)\n"
      "v_pk_add_f32 v[100:101], v[100:101], v[104:105]\n"
      "s_waitcnt lgkmcnt(0)\n"
      "v_pk_add_f32 v[108:109], v[108:109], v[112:113]\n"
      "v_pk_add_f32 v[102:103], v[102:103], v[106:107]\n"
      "v_pk_add_f32 v[110:111], v[110:111], v[114:115]\n"
      "v_pk_add_f32 v[100:101], v[100:101], v[108:109]\n"
      "v_pk_add_f32 v[102:103], v[102:103], v[110:111]\n"
      "v_cvt_pk_bf16_f32 v182, v100, v101\n"
      "s_xor_b32 s69, s59, 49152\n"
      "buffer_load_dwordx4 a[48:51], v50, s[4:7], s69 offen offset:0 sc1\n"
      "v_cvt_pk_bf16_f32 v183, v102, v103\n"
      "buffer_store_dwordx4 v[180:183], v36, s[44:47], s52 offen sc1\n"
      "s_barrier\n"
      "s_lshl_b32 s56, s58, 2\n"
      "s_add_u32 s56, s56, s50\n"
      "s_lshl_b32 s56, s56, 4\n"
      "s_add_u32 s56, s56, s57\n"
      "s_lshl_b32 s56, s56, 3\n"
      "s_add_u32 s56, s56, 9101312\n"
      "s_lshl_b32 s63, s60, 17\n"
      "s_waitcnt vmcnt(60)\n"
      "ds_read_b64_tr_b16 v[148:149], v56 offset:0\n"
      "ds_read_b64_tr_b16 v[150:151], v56 offset:512\n"
      "ds_read_b64_tr_b16 v[152:153], v57 offset:0\n"
      "ds_read_b64_tr_b16 v[154:155], v57 offset:512\n"
      "ds_read_b64_tr_b16 v[156:157], v56 offset:2048\n"
      "ds_read_b64_tr_b16 v[158:159], v56 offset:2560\n"
      "ds_read_b64_tr_b16 v[160:161], v57 offset:2048\n"
      "ds_read_b64_tr_b16 v[162:163], v57 offset:2560\n"
      "s_waitcnt vmcnt(54) lgkmcnt(6)\n"
      "v_mfma_f32_32x32x16_bf16 v[84:99], v[148:151], a[112:115], 0\n"
      "ds_read_b64_tr_b16 v[164:165], v56 offset:4096\n"
      "v_mfma_f32_32x32x16_bf16 v[100:115], v[148:151], a[116:119], 0\n"
      "ds_read_b64_tr_b16 v[166:167], v56 offset:4608\n"
      "s_xor_b32 s55, s65, 7\n"
      "s_mul_i32 s55, s55, 114688\n"
      "s_mov_b32 m0, s61\n"
      "s_waitcnt lgkmcnt(2)\n"
      "buffer_load_dwordx4 v52, s[24:27], s55 offen offset:0 lds\n"
      "v_mfma_f32_32x32x16_bf16 v[116:131], v[152:155], a[112:115], 0\n"
      "ds_read_b64_tr_b16 v[168:169], v57 offset:4096\n"
      "v_mfma_f32_32x32x16_bf16 v[132:147], v[152:155], a[116:119], 0\n"
      "ds_read_b64_tr_b16 v[170:171], v57 offset:4608\n"
      "buffer_load_dwordx4 v53, s[24:27], s55 offen offset:1024 lds\n"
      "v_mfma_f32_32x32x16_bf16 v[84:99], v[156:159], a[120:123], v[84:99]\n"
      "ds_read_b64_tr_b16 v[172:173], v56 offset:6144\n"
      "v_mfma_f32_32x32x16_bf16 v[100:115], v[156:159], a[124:127], v[100:115]\n"
      "ds_read_b64_tr_b16 v[174:175], v56 offset:6656\n"
      "buffer_load_dwordx4 v54, s[24:27], s55 offen offset:2048 lds\n"
      "v_mfma_f32_32x32x16_bf16 v[116:131], v[160:163], a[120:123], v[116:131]\n"
      "ds_read_b64_tr_b16 v[176:177], v57 offset:6144\n"
      "v_mfma_f32_32x32x16_bf16 v[132:147], v[160:163], a[124:127], v[132:147]\n"
      "ds_read_b64_tr_b16 v[178:179], v57 offset:6656\n"
      "buffer_load_dwordx4 v55, s[24:27], s55 offen offset:3072 lds\n"
      "s_lshl_b32 s52, s49, 1\n"
      "s_lshr_b32 s53, s50, 1\n"
      "s_add_u32 s52, s52, s53\n"
      "s_lshl_b32 s52, s52, 6\n"
      "v_lshl_add_u32 v5, v11, 3, s52\n"
      "buffer_load_dwordx2 v[244:245], v5, s[28:31], 0 offen offset:0\n"
      "buffer_load_dwordx2 v[246:247], v5, s[28:31], 0 offen offset:16\n"
      "buffer_load_dwordx2 v[248:249], v5, s[28:31], 0 offen offset:32\n"
      "buffer_load_dwordx2 v[250:251], v5, s[28:31], 0 offen offset:48\n"
      "s_waitcnt vmcnt(8)\n"
      "buffer_store_dwordx2 v[16:17], v37, s[44:47], s56 offen sc1\n"
      "s_waitcnt lgkmcnt(6)\n"
      "v_mfma_f32_32x32x16_bf16 v[84:99], v[164:167], a[128:131], v[84:99]\n"
      "ds_read_b64_tr_b16 v[148:149], v56 offset:8192\n"
      "v_max3_f32 v14, v212, v213, v214\n"
      "v_min3_f32 v15, v212, v213, v214\n"
      "v_max3_f32 v14, v14, v215, v216\n"
      "v_min3_f32 v15, v15, v215, v216\n"
      "v_max3_f32 v14, v14, v217, v218\n"
      "v_mfma_f32_32x32x16_bf16 v[100:115], v[164:167], a[132:135], v[100:115]\n"
      "ds_read_b64_tr_b16 v[150:151], v56 offset:8704\n"
      "s_xor_b32 s55, s65, 8\n"
      "s_mul_i32 s55, s55, 114688\n"
      "s_add_u32 m0, s61, 4096\n"
      "s_waitcnt lgkmcnt(2)\n"
      "buffer_load_dwordx4 v52, s[24:27], s55 offen offset:0 lds\n"
      "v_mfma_f32_32x32x16_bf16 v[116:131], v[168:171], a[128:131], v[116:131]\n"
      "ds_read_b64_tr_b16 v[152:153], v57 offset:8192\n"
      "v_min3_f32 v15, v15, v217, v218\n"
      "v_max3_f32 v14, v14, v219, v220\n"
      "v_min3_f32 v15, v15, v219, v220\n"
      "v_max3_f32 v14, v14, v221, v222\n"
      "v_min3_f32 v15, v15, v221, v222\n"
      "v_mfma_f32_32x32x16_bf16 v[132:147], v[168:171], a[132:135], v[132:147]\n"
      "ds_read_b64_tr_b16 v[154:155], v57 offset:8704\n"
      "buffer_load_dwordx4 v53, s[24:27], s55 offen offset:1024 lds\n"
      "v_mfma_f32_32x32x16_bf16 v[84:99], v[172:175], a[136:139], v[84:99]\n"
      "ds_read_b64_tr_b16 v[156:157], v56 offset:10240\n"
      "v_max3_f32 v14, v14, v223, v224\n"
      "v_min3_f32 v15, v15, v223, v224\n"
      "v_max3_f32 v14, v14, v225, v226\n"
      "v_min3_f32 v15, v15, v225, v226\n"
      "v_max_f32 v14, v14, v227\n"
      "v_mfma_f32_32x32x16_bf16 v[100:115], v[172:175], a[140:143], v[100:115]\n"
      "ds_read_b64_tr_b16 v[158:159], v56 offset:10752\n"
      "buffer_load_dwordx4 v54, s[24:27], s55 offen offset:2048 lds\n"
      "v_mfma_f32_32x32x16_bf16 v[116:131], v[176:179], a[136:139], v[116:131]\n"
      "ds_read_b64_tr_b16 v[160:161], v57 offset:10240\n"
      "v_min_f32 v15, v15, v227\n"
      "v_mov_b32 v18, v14\n"
      "v_mov_b32 v19, v15\n"
      "v_mfma_f32_32x32x16_bf16 v[132:147], v[176:179], a[140:143], v[132:147]\n"
      "ds_read_b64_tr_b16 v[162:163], v57 offset:10752\n"
      "buffer_load_dwordx4 v55, s[24:27], s55 offen offset:3072 lds\n"
      "s_waitcnt lgkmcnt(6)\n"
      "v_mfma_f32_32x32x16_bf16 v[84:99], v[148:151], a[144:147], v[84:99]\n"
      "ds_read_b64_tr_b16 v[164:165], v56 offset:12288\n"
      "v_permlane32_swap_b32 v14, v18\n"
      "v_permlane32_swap_b32 v15, v19\n"
      "v_max_f32 v14, v14, v18\n"
      "v_min_f32 v15, v15, v19\n"
      "v_mul_f32 v14, v14, v33\n"
      "v_mfma_f32_32x32x16_bf16 v[100:115], v[148:151], a[148:151], v[100:115]\n"
      "ds_read_b64_tr_b16 v[166:167], v56 offset:12800\n"
      "s_xor_b32 s55, s65, 9\n"
      "s_mul_i32 s55, s55, 114688\n"
      "s_add_u32 m0, s61, 8192\n"
      "s_waitcnt lgkmcnt(2)\n"
      "buffer_load_dwordx4 v52, s[24:27], s55 offen offset:0 lds\n"
      "v_mfma_f32_32x32x16_bf16 v[116:131], v[152:155], a[144:147], v[116:131]\n"
      "ds_read_b64_tr_b16 v[168:169], v57 offset:12288\n"
      "v_mul_f32 v15, v15, v32\n"
      "v_add_f32 v18, v14, v46\n"
      "v_add_f32 v19, v15, v46\n"
      "v_fma_f32 v180, v212, v32, v14\n"
      "v_fma_f32 v196, v212, v33, v15\n"
      "v_mfma_f32_32x32x16_bf16 v[132:147], v[152:155], a[148:151], v[132:147]\n"
      "ds_read_b64_tr_b16 v[170:171], v57 offset:12800\n"
      "buffer_load_dwordx4 v53, s[24:27], s55 offen offset:1024 lds\n"
      "v_mfma_f32_32x32x16_bf16 v[84:99], v[156:159], a[152:155], v[84:99]\n"
      "ds_read_b64_tr_b16 v[172:173], v56 offset:14336\n"
      "v_exp_f32 v180, v180\n"
      "v_exp_f32 v196, v196\n"
      "v_fma_f32 v181, v213, v32, v14\n"
      "v_mfma_f32_32x32x16_bf16 v[100:115], v[156:159], a[156:159], v[100:115]\n"
      "ds_read_b64_tr_b16 v[174:175], v56 offset:14848\n"
      "buffer_load_dwordx4 v54, s[24:27], s55 offen offset:2048 lds\n"
      "v_mfma_f32_32x32x16_bf16 v[116:131], v[160:163], a[152:155], v[116:131]\n"
      "ds_read_b64_tr_b16 v[176:177], v57 offset:14336\n"
      "v_fma_f32 v197, v213, v33, v15\n"
      "v_exp_f32 v181, v181\n"
      "v_exp_f32 v197, v197\n"
      "v_mfma_f32_32x32x16_bf16 v[132:147], v[160:163], a[156:159], v[132:147]\n"
      "ds_read_b64_tr_b16 v[178:179], v57 offset:14848\n"
      "buffer_load_dwordx4 v55, s[24:27], s55 offen offset:3072 lds\n"
      "s_waitcnt lgkmcnt(6)\n"
      "v_mfma_f32_32x32x16_bf16 v[84:99], v[164:167], a[160:163], v[84:99]\n"
      "ds_read_b64_tr_b16 v[148:149], v56 offset:16384\n"
      "v_fma_f32 v182, v214, v32, v14\n"
      "v_fma_f32 v198, v214, v33, v15\n"
      "v_exp_f32 v182, v182\n"
      "v_mfma_f32_32x32x16_bf16 v[100:115], v[164:167], a[164:167], v[100:115]\n"
      "ds_read_b64_tr_b16 v[150:151], v56 offset:16896\n"
      "s_xor_b32 s55, s65, 10\n"
      "s_mul_i32 s55, s55, 114688\n"
      "s_add_u32 m0, s61, 12288\n"
      "s_waitcnt lgkmcnt(2)\n"
      "buffer_load_dwordx4 v52, s[24:27], s55 offen offset:0 lds\n"
      "v_mfma_f32_32x32x16_bf16 v[116:131], v[168:171], a[160:163], v[116:131]\n"
      "ds_read_b64_tr_b16 v[152:153], v57 offset:16384\n"
      "v_exp_f32 v198, v198\n"
      "v_fma_f32 v183, v215, v32, v14\n"
      "v_fma_f32 v199, v215, v33, v15\n"
      "v_mfma_f32_32x32x16_bf16 v[132:147], v[168:171], a[164:167], v[132:147]\n"
      "ds_read_b64_tr_b16 v[154:155], v57 offset:16896\n"
      "buffer_load_dwordx4 v53, s[24:27], s55 offen offset:1024 lds\n"
      "v_mfma_f32_32x32x16_bf16 v[84:99], v[172:175], a[168:171], v[84:99]\n"
      "ds_read_b64_tr_b16 v[156:157], v56 offset:18432\n"
      "v_exp_f32 v183, v183\n"
      "v_add_f32 v38, v180, v181\n"
      "v_exp_f32 v199, v199\n"
      "v_mfma_f32_32x32x16_bf16 v[100:115], v[172:175], a[172:175], v[100:115]\n"
      "ds_read_b64_tr_b16 v[158:159], v56 offset:18944\n"
      "buffer_load_dwordx4 v54, s[24:27], s55 offen offset:2048 lds\n"
      "v_mfma_f32_32x32x16_bf16 v[116:131], v[176:179], a[168:171], v[116:131]\n"
      "ds_read_b64_tr_b16 v[160:161], v57 offset:18432\n"
      "v_add_f32 v39, v196, v197\n"
      "v_fma_f32 v184, v216, v32, v14\n"
      "v_fma_f32 v200, v216, v33, v15\n"
      "v_exp_f32 v184, v184\n"
      "v_mfma_f32_32x32x16_bf16 v[132:147], v[176:179], a[172:175], v[132:147]\n"
      "ds_read_b64_tr_b16 v[162:163], v57 offset:18944\n"
      "buffer_load_dwordx4 v55, s[24:27], s55 offen offset:3072 lds\n"
      "s_waitcnt lgkmcnt(6)\n"
      "v_mfma_f32_32x32x16_bf16 v[84:99], v[148:151], a[176:179], v[84:99]\n"
      "ds_read_b64_tr_b16 v[164:165], v56 offset:20480\n"
      "v_add_f32 v38, v38, v182\n"
      "v_exp_f32 v200, v200\n"
      "v_add_f32 v39, v39, v198\n"
      "v_fma_f32 v185, v217, v32, v14\n"
      "v_mfma_f32_32x32x16_bf16 v[100:115], v[148:151], a[180:183], v[100:115]\n"
      "ds_read_b64_tr_b16 v[166:167], v56 offset:20992\n"
      "s_xor_b32 s55, s65, 11\n"
      "s_mul_i32 s55, s55, 114688\n"
      "s_add_u32 m0, s61, 16384\n"
      "s_waitcnt lgkmcnt(2)\n"
      "buffer_load_dwordx4 v52, s[24:27], s55 offen offset:0 lds\n"
      "v_mfma_f32_32x32x16_bf16 v[116:131], v[152:155], a[176:179], v[116:131]\n"
      "ds_read_b64_tr_b16 v[168:169], v57 offset:20480\n"
      "v_fma_f32 v201, v217, v33, v15\n"
      "v_exp_f32 v185, v185\n"
      "v_add_f32 v38, v38, v183\n"
      "v_mfma_f32_32x32x16_bf16 v[132:147], v[152:155], a[180:183], v[132:147]\n"
      "ds_read_b64_tr_b16 v[170:171], v57 offset:20992\n"
      "buffer_load_dwordx4 v53, s[24:27], s55 offen offset:1024 lds\n"
      "v_mfma_f32_32x32x16_bf16 v[84:99], v[156:159], a[184:187], v[84:99]\n"
      "ds_read_b64_tr_b16 v[172:173], v56 offset:22528\n"
      "v_exp_f32 v201, v201\n"
      "v_add_f32 v39, v39, v199\n"
      "v_fma_f32 v186, v218, v32, v14\n"
      "v_fma_f32 v202, v218, v33, v15\n"
      "v_mfma_f32_32x32x16_bf16 v[100:115], v[156:159], a[188:191], v[100:115]\n"
      "ds_read_b64_tr_b16 v[174:175], v56 offset:23040\n"
      "buffer_load_dwordx4 v54, s[24:27], s55 offen offset:2048 lds\n"
      "v_mfma_f32_32x32x16_bf16 v[116:131], v[160:163], a[184:187], v[116:131]\n"
      "ds_read_b64_tr_b16 v[176:177], v57 offset:22528\n"
      "v_exp_f32 v186, v186\n"
      "v_add_f32 v38, v38, v184\n"
      "v_exp_f32 v202, v202\n"
      "v_mfma_f32_32x32x16_bf16 v[132:147], v[160:163], a[188:191], v[132:147]\n"
      "ds_read_b64_tr_b16 v[178:179], v57 offset:23040\n"
      "buffer_load_dwordx4 v55, s[24:27], s55 offen offset:3072 lds\n"
      "s_waitcnt lgkmcnt(6)\n"
      "v_mfma_f32_32x32x16_bf16 v[84:99], v[164:167], a[192:195], v[84:99]\n"
      "ds_read_b64_tr_b16 v[148:149], v56 offset:24576\n"
      "v_add_f32 v39, v39, v200\n"
      "v_fma_f32 v187, v219, v32, v14\n"
      "v_fma_f32 v203, v219, v33, v15\n"
      "v_exp_f32 v187, v187\n"
      "v_mfma_f32_32x32x16_bf16 v[100:115], v[164:167], a[196:199], v[100:115]\n"
      "ds_read_b64_tr_b16 v[150:151], v56 offset:25088\n"
      "s_xor_b32 s55, s65, 12\n"
      "s_mul_i32 s55, s55, 114688\n"
      "s_add_u32 m0, s61, 20480\n"
      "s_waitcnt lgkmcnt(2)\n"
      "buffer_load_dwordx4 v52, s[24:27], s55 offen offset:0 lds\n"
      "v_mfma_f32_32x32x16_bf16 v[116:131], v[168:171], a[192:195], v[116:131]\n"
      "ds_read_b64_tr_b16 v[152:153], v57 offset:24576\n"
      "v_add_f32 v38, v38, v185\n"
      "v_exp_f32 v203, v203\n"
      "v_add_f32 v39, v39, v201\n"
      "v_fma_f32 v188, v220, v32, v14\n"
      "v_mfma_f32_32x32x16_bf16 v[132:147], v[168:171], a[196:199], v[132:147]\n"
      "ds_read_b64_tr_b16 v[154:155], v57 offset:25088\n"
      "buffer_load_dwordx4 v53, s[24:27], s55 offen offset:1024 lds\n"
      "v_mfma_f32_32x32x16_bf16 v[84:99], v[172:175], a[200:203], v[84:99]\n"
      "ds_read_b64_tr_b16 v[156:157], v56 offset:26624\n"
      "v_fma_f32 v204, v220, v33, v15\n"
      "v_exp_f32 v188, v188\n"
      "v_add_f32 v38, v38, v186\n"
      "v_mfma_f32_32x32x16_bf16 v[100:115], v[172:175], a[204:207], v[100:115]\n"
      "ds_read_b64_tr_b16 v[158:159], v56 offset:27136\n"
      "buffer_load_dwordx4 v54, s[24:27], s55 offen offset:2048 lds\n"
      "v_mfma_f32_32x32x16_bf16 v[116:131], v[176:179], a[200:203], v[116:131]\n"
      "ds_read_b64_tr_b16 v[160:161], v57 offset:26624\n"
      "v_exp_f32 v204, v204\n"
      "v_add_f32 v39, v39, v202\n"
      "v_fma_f32 v189, v221, v32, v14\n"
      "v_fma_f32 v205, v221, v33, v15\n"
      "v_mfma_f32_32x32x16_bf16 v[132:147], v[176:179], a[204:207], v[132:147]\n"
      "ds_read_b64_tr_b16 v[162:163], v57 offset:27136\n"
      "buffer_load_dwordx4 v55, s[24:27], s55 offen offset:3072 lds\n"
      "s_waitcnt vmcnt(25) lgkmcnt(6)\n"
      "v_mfma_f32_32x32x16_bf16 v[84:99], v[148:151], a[208:211], v[84:99]\n"
      "ds_read_b64_tr_b16 v[164:165], v56 offset:0\n"
      "v_exp_f32 v189, v189\n"
      "v_add_f32 v38, v38, v187\n"
      "v_exp_f32 v205, v205\n"
      "v_mfma_f32_32x32x16_bf16 v[100:115], v[148:151], a[212:215], v[100:115]\n"
      "ds_read_b64_tr_b16 v[166:167], v56 offset:512\n"
      "s_xor_b32 s55, s65, 13\n"
      "s_mul_i32 s55, s55, 114688\n"
      "s_add_u32 m0, s61, 24576\n"
      "s_waitcnt lgkmcnt(2)\n"
      "buffer_load_dwordx4 v52, s[24:27], s55 offen offset:0 lds\n"
      "v_mfma_f32_32x32x16_bf16 v[116:131], v[152:155], a[208:211], v[116:131]\n"
      "ds_read_b64_tr_b16 v[168:169], v57 offset:0\n"
      "v_add_f32 v39, v39, v203\n"
      "v_fma_f32 v190, v222, v32, v14\n"
      "v_fma_f32 v206, v222, v33, v15\n"
      "v_exp_f32 v190, v190\n"
      "v_mfma_f32_32x32x16_bf16 v[132:147], v[152:155], a[212:215], v[132:147]\n"
      "ds_read_b64_tr_b16 v[170:171], v57 offset:512\n"
      "buffer_load_dwordx4 v53, s[24:27], s55 offen offset:1024 lds\n"
      "v_mfma_f32_32x32x16_bf16 v[84:99], v[156:159], a[216:219], v[84:99]\n"
      "ds_read_b64_tr_b16 v[172:173], v56 offset:2048\n"
      "v_add_f32 v38, v38, v188\n"
      "v_exp_f32 v206, v206\n"
      "v_add_f32 v39, v39, v204\n"
      "v_fma_f32 v191, v223, v32, v14\n"
      "v_mfma_f32_32x32x16_bf16 v[100:115], v[156:159], a[220:223], v[100:115]\n"
      "ds_read_b64_tr_b16 v[174:175], v56 offset:2560\n"
      "buffer_load_dwordx4 v54, s[24:27], s55 offen offset:2048 lds\n"
      "v_mfma_f32_32x32x16_bf16 v[116:131], v[160:163], a[216:219], v[116:131]\n"
      "ds_read_b64_tr_b16 v[176:177], v57 offset:2048\n"
      "v_fma_f32 v207, v223, v33, v15\n"
      "v_exp_f32 v191, v191\n"
      "v_add_f32 v38, v38, v189\n"
      "v_mfma_f32_32x32x16_bf16 v[132:147], v[160:163], a[220:223], v[132:147]\n"
      "ds_read_b64_tr_b16 v[178:179], v57 offset:2560\n"
      "buffer_load_dwordx4 v55, s[24:27], s55 offen offset:3072 lds\n"
      "s_waitcnt vmcnt(20) lgkmcnt(6)\n"
      "v_mfma_f32_32x32x16_bf16 v[84:99], v[164:167], a[224:227], v[84:99]\n"
      "ds_read_b64_tr_b16 v[148:149], v56 offset:4096\n"
      "v_exp_f32 v207, v207\n"
      "v_add_f32 v39, v39, v205\n"
      "v_fma_f32 v192, v224, v32, v18\n"
      "v_fma_f32 v208, v224, v33, v19\n"
      "v_mfma_f32_32x32x16_bf16 v[100:115], v[164:167], a[228:231], v[100:115]\n"
      "ds_read_b64_tr_b16 v[150:151], v56 offset:4608\n"
      "buffer_load_dwordx4 a[52:55], v51, s[4:7], s69 offen offset:0 sc1\n"
      "s_waitcnt lgkmcnt(6)\n"
      "v_mfma_f32_32x32x16_bf16 v[116:131], v[168:171], a[224:227], v[116:131]\n"
      "ds_read_b64_tr_b16 v[152:153], v57 offset:4096\n"
      "v_exp_f32 v192, v192\n"
      "v_add_f32 v38, v38, v190\n"
      "v_exp_f32 v208, v208\n"
      "v_mfma_f32_32x32x16_bf16 v[132:147], v[168:171], a[228:231], v[132:147]\n"
      "ds_read_b64_tr_b16 v[154:155], v57 offset:4608\n"
      "buffer_load_dwordx4 a[56:59], v50, s[4:7], s69 offen offset:2048 sc1\n"
      "s_waitcnt lgkmcnt(6)\n"
      "v_mfma_f32_32x32x16_bf16 v[84:99], v[172:175], a[232:235], v[84:99]\n"
      "ds_read_b64_tr_b16 v[156:157], v56 offset:6144\n"
      "v_add_f32 v39, v39, v206\n"
      "v_fma_f32 v193, v225, v32, v18\n"
      "v_fma_f32 v209, v225, v33, v19\n"
      "v_exp_f32 v193, v193\n"
      "v_mfma_f32_32x32x16_bf16 v[100:115], v[172:175], a[236:239], v[100:115]\n"
      "ds_read_b64_tr_b16 v[158:159], v56 offset:6656\n"
      "buffer_load_dwordx4 a[60:63], v51, s[4:7], s69 offen offset:2048 sc1\n"
      "s_waitcnt lgkmcnt(6)\n"
      "v_mfma_f32_32x32x16_bf16 v[116:131], v[176:179], a[232:235], v[116:131]\n"
      "ds_read_b64_tr_b16 v[160:161], v57 offset:6144\n"
      "v_add_f32 v38, v38, v191\n"
      "v_exp_f32 v209, v209\n"
      "v_add_f32 v39, v39, v207\n"
      "v_fma_f32 v194, v226, v32, v18\n"
      "v_mfma_f32_32x32x16_bf16 v[132:147], v[176:179], a[236:239], v[132:147]\n"
      "ds_read_b64_tr_b16 v[162:163], v57 offset:6656\n"
      "s_xor_b32 s55, s65, 14\n"
      "s_mul_i32 s55, s55, 114688\n"
      "s_mov_b32 m0, s61\n"
      "s_nop 0\n"
      "buffer_load_dwordx4 v52, s[24:27], s55 offen offset:0 lds\n"
      "s_waitcnt vmcnt(20) lgkmcnt(6)\n"
      "v_mfma_f32_32x32x16_bf16 v[84:99], v[148:151], a[240:243], v[84:99]\n"
      "ds_read_b64_tr_b16 v[164:165], v56 offset:8192\n"
      "v_fma_f32 v210, v226, v33, v19\n"
      "v_exp_f32 v194, v194\n"
      "v_add_f32 v38, v38, v192\n"
      "v_mfma_f32_32x32x16_bf16 v[100:115], v[148:151], a[244:247], v[100:115]\n"
      "ds_read_b64_tr_b16 v[166:167], v56 offset:8704\n"
      "buffer_load_dwordx4 v53, s[24:27], s55 offen offset:1024 lds\n"
      "s_waitcnt lgkmcnt(6)\n"
      "v_mfma_f32_32x32x16_bf16 v[116:131], v[152:155], a[240:243], v[116:131]\n"
      "ds_read_b64_tr_b16 v[168:169], v57 offset:8192\n"
      "v_exp_f32 v210, v210\n"
      "v_add_f32 v39, v39, v208\n"
      "v_fma_f32 v195, v227, v32, v18\n"
      "v_fma_f32 v211, v227, v33, v19\n"
      "v_mfma_f32_32x32x16_bf16 v[132:147], v[152:155], a[244:247], v[132:147]\n"
      "ds_read_b64_tr_b16 v[170:171], v57 offset:8704\n"
      "buffer_load_dwordx4 v54, s[24:27], s55 offen offset:2048 lds\n"
      "s_waitcnt lgkmcnt(6)\n"
      "v_mfma_f32_32x32x16_bf16 v[84:99], v[156:159], a[248:251], v[84:99]\n"
      "ds_read_b64_tr_b16 v[172:173], v56 offset:10240\n"
      "v_exp_f32 v195, v195\n"
      "v_add_f32 v38, v38, v193\n"
      "v_exp_f32 v211, v211\n"
      "v_mfma_f32_32x32x16_bf16 v[100:115], v[156:159], a[252:255], v[100:115]\n"
      "ds_read_b64_tr_b16 v[174:175], v56 offset:10752\n"
      "buffer_load_dwordx4 v55, s[24:27], s55 offen offset:3072 lds\n"
      "s_waitcnt lgkmcnt(6)\n"
      "v_mfma_f32_32x32x16_bf16 v[116:131], v[160:163], a[248:251], v[116:131]\n"
      "ds_read_b64_tr_b16 v[176:177], v57 offset:10240\n"
      "v_add_f32 v39, v39, v209\n"
      "v_add_f32 v38, v38, v194\n"
      "v_add_f32 v39, v39, v210\n"
      "v_add_f32 v38, v38, v195\n"
      "v_add_f32 v39, v39, v211\n"
      "v_mfma_f32_32x32x16_bf16 v[132:147], v[160:163], a[252:255], v[132:147]\n"
      "ds_read_b64_tr_b16 v[178:179], v57 offset:10752\n"
      "s_xor_b32 s69, s59, 53248\n"
      "buffer_load_dwordx4 a[64:67], v50, s[4:7], s69 offen offset:0 sc1\n"
      "s_waitcnt vmcnt(20) lgkmcnt(6)\n"
      "v_mfma_f32_32x32x16_bf16 v[84:99], v[164:167], a[0:3], v[84:99]\n"
      "ds_read_b64_tr_b16 v[148:149], v56 offset:12288\n"
      "v_permlane32_swap_b32 v38, v39\n"
      "v_add_f32 v38, v38, v39\n"
      "v_rcp_f32 v38, v38\n"
      "v_mfma_f32_32x32x16_bf16 v[100:115], v[164:167], a[4:7], v[100:115]\n"
      "ds_read_b64_tr_b16 v[150:151], v56 offset:12800\n"
      "buffer_load_dwordx4 a[68:71], v51, s[4:7], s69 offen offset:0 sc1\n"
      "s_waitcnt lgkmcnt(6)\n"
      "v_mfma_f32_32x32x16_bf16 v[116:131], v[168:171], a[0:3], v[116:131]\n"
      "ds_read_b64_tr_b16 v[152:153], v57 offset:12288\n"
      "v_mov_b32 v39, v38\n"
      "v_mfma_f32_32x32x16_bf16 v[132:147], v[168:171], a[4:7], v[132:147]\n"
      "ds_read_b64_tr_b16 v[154:155], v57 offset:12800\n"
      "buffer_load_dwordx4 a[72:75], v50, s[4:7], s69 offen offset:2048 sc1\n"
      "s_waitcnt lgkmcnt(6)\n"
      "v_mfma_f32_32x32x16_bf16 v[84:99], v[172:175], a[8:11], v[84:99]\n"
      "ds_read_b64_tr_b16 v[156:157], v56 offset:14336\n"
      "v_permlane32_swap_b32 v38, v39\n"
      "v_mul_f32 v180, v180, v38\n"
      "v_mul_f32 v181, v181, v38\n"
      "v_mul_f32 v196, v196, v39\n"
      "v_mul_f32 v197, v197, v39\n"
      "v_mfma_f32_32x32x16_bf16 v[100:115], v[172:175], a[12:15], v[100:115]\n"
      "ds_read_b64_tr_b16 v[158:159], v56 offset:14848\n"
      "buffer_load_dwordx4 a[76:79], v51, s[4:7], s69 offen offset:2048 sc1\n"
      "s_waitcnt lgkmcnt(6)\n"
      "v_mfma_f32_32x32x16_bf16 v[116:131], v[176:179], a[8:11], v[116:131]\n"
      "ds_read_b64_tr_b16 v[160:161], v57 offset:14336\n"
      "v_cvt_pk_bf16_f32 v228, v180, v181\n"
      "v_cvt_pk_bf16_f32 v236, v196, v197\n"
      "v_mul_f32 v182, v182, v38\n"
      "v_mul_f32 v183, v183, v38\n"
      "v_mul_f32 v198, v198, v39\n"
      "v_mfma_f32_32x32x16_bf16 v[132:147], v[176:179], a[12:15], v[132:147]\n"
      "ds_read_b64_tr_b16 v[162:163], v57 offset:14848\n"
      "s_xor_b32 s55, s65, 15\n"
      "s_mul_i32 s55, s55, 114688\n"
      "s_add_u32 m0, s61, 4096\n"
      "s_nop 0\n"
      "buffer_load_dwordx4 v52, s[24:27], s55 offen offset:0 lds\n"
      "s_waitcnt vmcnt(20) lgkmcnt(6)\n"
      "v_mfma_f32_32x32x16_bf16 v[84:99], v[148:151], a[16:19], v[84:99]\n"
      "ds_read_b64_tr_b16 v[164:165], v56 offset:16384\n"
      "v_mul_f32 v199, v199, v39\n"
      "v_cvt_pk_bf16_f32 v229, v182, v183\n"
      "v_cvt_pk_bf16_f32 v237, v198, v199\n"
      "v_mul_f32 v184, v184, v38\n"
      "v_mul_f32 v185, v185, v38\n"
      "v_mfma_f32_32x32x16_bf16 v[100:115], v[148:151], a[20:23], v[100:115]\n"
      "ds_read_b64_tr_b16 v[166:167], v56 offset:16896\n"
      "buffer_load_dwordx4 v53, s[24:27], s55 offen offset:1024 lds\n"
      "s_waitcnt lgkmcnt(6)\n"
      "v_mfma_f32_32x32x16_bf16 v[116:131], v[152:155], a[16:19], v[116:131]\n"
      "ds_read_b64_tr_b16 v[168:169], v57 offset:16384\n"
      "v_mul_f32 v200, v200, v39\n"
      "v_mul_f32 v201, v201, v39\n"
      "v_cvt_pk_bf16_f32 v230, v184, v185\n"
      "v_cvt_pk_bf16_f32 v238, v200, v201\n"
      "v_mul_f32 v186, v186, v38\n"
      "v_mfma_f32_32x32x16_bf16 v[132:147], v[152:155], a[20:23], v[132:147]\n"
      "ds_read_b64_tr_b16 v[170:171], v57 offset:16896\n"
      "buffer_load_dwordx4 v54, s[24:27], s55 offen offset:2048 lds\n"
      "s_waitcnt lgkmcnt(6)\n"
      "v_mfma_f32_32x32x16_bf16 v[84:99], v[156:159], a[24:27], v[84:99]\n"
      "ds_read_b64_tr_b16 v[172:173], v56 offset:18432\n"
      "v_mul_f32 v187, v187, v38\n"
      "v_mul_f32 v202, v202, v39\n"
      "v_mul_f32 v203, v203, v39\n"
      "v_cvt_pk_bf16_f32 v231, v186, v187\n"
      "v_cvt_pk_bf16_f32 v239, v202, v203\n"
      "v_mfma_f32_32x32x16_bf16 v[100:115], v[156:159], a[28:31], v[100:115]\n"
      "ds_read_b64_tr_b16 v[174:175], v56 offset:18944\n"
      "buffer_load_dwordx4 v55, s[24:27], s55 offen offset:3072 lds\n"
      "s_waitcnt lgkmcnt(6)\n"
      "v_mfma_f32_32x32x16_bf16 v[116:131], v[160:163], a[24:27], v[116:131]\n"
      "ds_read_b64_tr_b16 v[176:177], v57 offset:18432\n"
      "v_mul_f32 v188, v188, v38\n"
      "v_mul_f32 v189, v189, v38\n"
      "v_mul_f32 v204, v204, v39\n"
      "v_mul_f32 v205, v205, v39\n"
      "v_cvt_pk_bf16_f32 v232, v188, v189\n"
      "v_mfma_f32_32x32x16_bf16 v[132:147], v[160:163], a[28:31], v[132:147]\n"
      "ds_read_b64_tr_b16 v[178:179], v57 offset:18944\n"
      "s_xor_b32 s69, s59, 57344\n"
      "buffer_load_dwordx4 a[80:83], v50, s[4:7], s69 offen offset:0 sc1\n"
      "s_waitcnt vmcnt(20) lgkmcnt(6)\n"
      "v_mfma_f32_32x32x16_bf16 v[84:99], v[164:167], a[32:35], v[84:99]\n"
      "ds_read_b64_tr_b16 v[148:149], v56 offset:20480\n"
      "v_cvt_pk_bf16_f32 v240, v204, v205\n"
      "v_mul_f32 v190, v190, v38\n"
      "v_mul_f32 v191, v191, v38\n"
      "v_mul_f32 v206, v206, v39\n"
      "v_mul_f32 v207, v207, v39\n"
      "v_mfma_f32_32x32x16_bf16 v[100:115], v[164:167], a[36:39], v[100:115]\n"
      "ds_read_b64_tr_b16 v[150:151], v56 offset:20992\n"
      "buffer_load_dwordx4 a[84:87], v51, s[4:7], s69 offen offset:0 sc1\n"
      "s_waitcnt lgkmcnt(6)\n"
      "v_mfma_f32_32x32x16_bf16 v[116:131], v[168:171], a[32:35], v[116:131]\n"
      "ds_read_b64_tr_b16 v[152:153], v57 offset:20480\n"
      "v_cvt_pk_bf16_f32 v233, v190, v191\n"
      "v_cvt_pk_bf16_f32 v241, v206, v207\n"
      "v_mul_f32 v192, v192, v38\n"
      "v_mul_f32 v193, v193, v38\n"
      "v_mul_f32 v208, v208, v39\n"
      "v_mfma_f32_32x32x16_bf16 v[132:147], v[168:171], a[36:39], v[132:147]\n"
      "ds_read_b64_tr_b16 v[154:155], v57 offset:20992\n"
      "buffer_load_dwordx4 a[88:91], v50, s[4:7], s69 offen offset:2048 sc1\n"
      "s_waitcnt lgkmcnt(6)\n"
      "v_mfma_f32_32x32x16_bf16 v[84:99], v[172:175], a[40:43], v[84:99]\n"
      "ds_read_b64_tr_b16 v[156:157], v56 offset:22528\n"
      "v_mul_f32 v209, v209, v39\n"
      "v_cvt_pk_bf16_f32 v234, v192, v193\n"
      "v_cvt_pk_bf16_f32 v242, v208, v209\n"
      "v_mul_f32 v194, v194, v38\n"
      "v_mul_f32 v195, v195, v38\n"
      "v_mfma_f32_32x32x16_bf16 v[100:115], v[172:175], a[44:47], v[100:115]\n"
      "ds_read_b64_tr_b16 v[158:159], v56 offset:23040\n"
      "buffer_load_dwordx4 a[92:95], v51, s[4:7], s69 offen offset:2048 sc1\n"
      "s_waitcnt lgkmcnt(6)\n"
      "v_mfma_f32_32x32x16_bf16 v[116:131], v[176:179], a[40:43], v[116:131]\n"
      "ds_read_b64_tr_b16 v[160:161], v57 offset:22528\n"
      "v_mul_f32 v210, v210, v39\n"
      "v_mul_f32 v211, v211, v39\n"
      "v_cvt_pk_bf16_f32 v235, v194, v195\n"
      "v_cvt_pk_bf16_f32 v243, v210, v211\n"
      "v_mfma_f32_32x32x16_bf16 v[132:147], v[176:179], a[44:47], v[132:147]\n"
      "ds_read_b64_tr_b16 v[162:163], v57 offset:23040\n"
      "s_xor_b32 s69, s59, 61440\n"
      "buffer_load_dwordx4 a[96:99], v50, s[4:7], s69 offen offset:0 sc1\n"
      "s_waitcnt vmcnt(20) lgkmcnt(6)\n"
      "v_mfma_f32_32x32x16_bf16 v[84:99], v[148:151], a[48:51], v[84:99]\n"
      "ds_read_b64_tr_b16 v[164:165], v56 offset:24576\n"
      "s_waitcnt vmcnt(19)\n"
      "v_mfma_f32_32x32x16_bf16 v[100:115], v[148:151], a[52:55], v[100:115]\n"
      "ds_read_b64_tr_b16 v[166:167], v56 offset:25088\n"
      "buffer_load_dwordx4 a[100:103], v51, s[4:7], s69 offen offset:0 sc1\n"
      "s_waitcnt lgkmcnt(6)\n"
      "v_mfma_f32_32x32x16_bf16 v[116:131], v[152:155], a[48:51], v[116:131]\n"
      "ds_read_b64_tr_b16 v[168:169], v57 offset:24576\n"
      "v_mfma_f32_32x32x16_bf16 v[132:147], v[152:155], a[52:55], v[132:147]\n"
      "ds_read_b64_tr_b16 v[170:171], v57 offset:25088\n"
      "buffer_load_dwordx4 a[104:107], v50, s[4:7], s69 offen offset:2048 sc1\n"
      "s_waitcnt vmcnt(20) lgkmcnt(6)\n"
      "v_mfma_f32_32x32x16_bf16 v[84:99], v[156:159], a[56:59], v[84:99]\n"
      "ds_read_b64_tr_b16 v[172:173], v56 offset:26624\n"
      "s_waitcnt vmcnt(19)\n"
      "v_mfma_f32_32x32x16_bf16 v[100:115], v[156:159], a[60:63], v[100:115]\n"
      "ds_read_b64_tr_b16 v[174:175], v56 offset:27136\n"
      "buffer_load_dwordx4 a[108:111], v51, s[4:7], s69 offen offset:2048 sc1\n"
      "s_waitcnt lgkmcnt(6)\n"
      "v_mfma_f32_32x32x16_bf16 v[116:131], v[160:163], a[56:59], v[116:131]\n"
      "ds_read_b64_tr_b16 v[176:177], v57 offset:26624\n"
      "v_mfma_f32_32x32x16_bf16 v[132:147], v[160:163], a[60:63], v[132:147]\n"
      "s_mov_b32 m0, s66\n"
      "ds_read_b64_tr_b16 v[178:179], v57 offset:27136\n"
      "buffer_load_dwordx4 v60, s[32:35], s63 offen offset:0 lds\n"
      "s_waitcnt vmcnt(16) lgkmcnt(6)\n"
      "v_mfma_f32_32x32x16_bf16 v[84:99], v[164:167], a[64:67], v[84:99]\n"
      "ds_read_b64_tr_b16 v[148:149], v56 offset:0\n"
      "s_waitcnt vmcnt(15)\n"
      "v_mfma_f32_32x32x16_bf16 v[100:115], v[164:167], a[68:71], v[100:115]\n"
      "ds_read_b64_tr_b16 v[150:151], v56 offset:512\n"
      "buffer_load_dwordx4 v61, s[32:35], s63 offen offset:1024 lds\n"
      "s_waitcnt lgkmcnt(6)\n"
      "v_mfma_f32_32x32x16_bf16 v[116:131], v[168:171], a[64:67], v[116:131]\n"
      "ds_read_b64_tr_b16 v[152:153], v57 offset:0\n"
      "v_mfma_f32_32x32x16_bf16 v[132:147], v[168:171], a[68:71], v[132:147]\n"
      "ds_read_b64_tr_b16 v[154:155], v57 offset:512\n"
      "buffer_load_dwordx4 v62, s[32:35], s63 offen offset:2048 lds\n"
      "s_waitcnt vmcnt(16) lgkmcnt(6)\n"
      "v_mfma_f32_32x32x16_bf16 v[84:99], v[172:175], a[72:75], v[84:99]\n"
      "ds_read_b64_tr_b16 v[156:157], v56 offset:2048\n"
      "s_waitcnt vmcnt(15)\n"
      "v_mfma_f32_32x32x16_bf16 v[100:115], v[172:175], a[76:79], v[100:115]\n"
      "ds_read_b64_tr_b16 v[158:159], v56 offset:2560\n"
      "buffer_load_dwordx4 v63, s[32:35], s63 offen offset:3072 lds\n"
      "s_add_u32 s63, s63, 131072\n"
      "s_cmp_eq_u32 s63, 1835008\n"
      "s_cselect_b32 s63, 0, s63\n"
      "s_waitcnt lgkmcnt(6)\n"
      "v_mfma_f32_32x32x16_bf16 v[116:131], v[176:179], a[72:75], v[116:131]\n"
      "ds_read_b64_tr_b16 v[160:161], v57 offset:2048\n"
      "v_mfma_f32_32x32x16_bf16 v[132:147], v[176:179], a[76:79], v[132:147]\n"
      "s_add_u32 m0, s66, 4096\n"
      "ds_read_b64_tr_b16 v[162:163], v57 offset:2560\n"
      "buffer_load_dwordx4 v60, s[32:35], s63 offen offset:0 lds\n"
      "s_waitcnt vmcnt(12) lgkmcnt(6)\n"
      "v_mfma_f32_32x32x16_bf16 v[84:99], v[148:151], a[80:83], v[84:99]\n"
      "ds_read_b64_tr_b16 v[164:165], v56 offset:4096\n"
      "s_waitcnt vmcnt(11)\n"
      "v_mfma_f32_32x32x16_bf16 v[100:115], v[148:151], a[84:87], v[100:115]\n"
      "ds_read_b64_tr_b16 v[166:167], v56 offset:4608\n"
      "buffer_load_dwordx4 v61, s[32:35], s63 offen offset:1024 lds\n"
      "s_waitcnt lgkmcnt(6)\n"
      "v_mfma_f32_32x32x16_bf16 v[116:131], v[152:155], a[80:83], v[116:131]\n"
      "ds_read_b64_tr_b16 v[168:169], v57 offset:4096\n"
      "v_mfma_f32_32x32x16_bf16 v[132:147], v[152:155], a[84:87], v[132:147]\n"
      "ds_read_b64_tr_b16 v[170:171], v57 offset:4608\n"
      "buffer_load_dwordx4 v62, s[32:35], s63 offen offset:2048 lds\n"
      "s_waitcnt vmcnt(12) lgkmcnt(6)\n"
      "v_mfma_f32_32x32x16_bf16 v[84:99], v[156:159], a[88:91], v[84:99]\n"
      "ds_read_b64_tr_b16 v[172:173], v56 offset:6144\n"
      "s_waitcnt vmcnt(11)\n"
      "v_mfma_f32_32x32x16_bf16 v[100:115], v[156:159], a[92:95], v[100:115]\n"
      "ds_read_b64_tr_b16 v[174:175], v56 offset:6656\n"
      "buffer_load_dwordx4 v63, s[32:35], s63 offen offset:3072 lds\n"
      "s_add_u32 s63, s63, 131072\n"
      "s_cmp_eq_u32 s63, 1835008\n"
      "s_cselect_b32 s63, 0, s63\n"
      "s_waitcnt lgkmcnt(6)\n"
      "v_mfma_f32_32x32x16_bf16 v[116:131], v[160:163], a[88:91], v[116:131]\n"
      "ds_read_b64_tr_b16 v[176:177], v57 offset:6144\n"
      "v_mfma_f32_32x32x16_bf16 v[132:147], v[160:163], a[92:95], v[132:147]\n"
      "s_add_u32 m0, s66, 8192\n"
      "ds_read_b64_tr_b16 v[178:179], v57 offset:6656\n"
      "buffer_load_dwordx4 v60, s[32:35], s63 offen offset:0 lds\n"
      "s_waitcnt vmcnt(11) lgkmcnt(6)\n"
      "v_mfma_f32_32x32x16_bf16 v[100:115], v[164:167], a[100:103], v[100:115]\n"
      "v_add_u32 v6, s61, v36\n"
      "s_xor_b32 s52, s50, 1\n"
      "s_mul_i32 s52, s52, 28672\n"
      "s_add_u32 s52, s52, %3\n"
      "v_add_u32 v20, s52, v36\n"
      "s_xor_b32 s52, s50, 2\n"
      "s_mul_i32 s52, s52, 28672\n"
      "s_add_u32 s52, s52, %3\n"
      "v_add_u32 v21, s52, v36\n"
      "s_xor_b32 s52, s50, 3\n"
      "s_mul_i32 s52, s52, 28672\n"
      "s_add_u32 s52, s52, %3\n"
      "v_add_u32 v22, s52, v36\n"
      "s_waitcnt vmcnt(9) lgkmcnt(2)\n"
      "v_mfma_f32_32x32x16_bf16 v[100:115], v[172:175], a[108:111], v[100:115]\n"
      "buffer_load_dwordx4 v61, s[32:35], s63 offen offset:1024 lds\n"
      "v_lshlrev_b32 v148, 16, v244\n"
      "v_and_b32 v149, 0xffff0000, v244\n"
      "v_lshlrev_b32 v150, 16, v245\n"
      "v_and_b32 v151, 0xffff0000, v245\n"
      "v_lshlrev_b32 v152, 16, v246\n"
      "v_and_b32 v153, 0xffff0000, v246\n"
      "v_lshlrev_b32 v154, 16, v247\n"
      "v_and_b32 v155, 0xffff0000, v247\n"
      "v_mfma_f32_32x32x16_bf16 v[116:131], v[168:171], a[96:99], v[116:131]\n"
      "v_lshlrev_b32 v156, 16, v248\n"
      "v_and_b32 v157, 0xffff0000, v248\n"
      "v_lshlrev_b32 v158, 16, v249\n"
      "v_and_b32 v159, 0xffff0000, v249\n"
      "ds_write_b128 v6, v[100:103] offset:24576\n"
      "ds_write_b128 v6, v[104:107] offset:25600\n"
      "s_waitcnt lgkmcnt(2)\n"
      "v_mfma_f32_32x32x16_bf16 v[116:131], v[176:179], a[104:107], v[116:131]\n"
      "buffer_load_dwordx4 v62, s[32:35], s63 offen offset:2048 lds\n"
      "ds_write_b128 v6, v[108:111] offset:26624\n"
      "ds_write_b128 v6, v[112:115] offset:27648\n"
      "v_mfma_f32_32x32x16_bf16 v[132:147], v[168:171], a[100:103], v[132:147]\n"
      "v_lshlrev_b32 v160, 16, v250\n"
      "v_and_b32 v161, 0xffff0000, v250\n"
      "v_lshlrev_b32 v162, 16, v251\n"
      "v_and_b32 v163, 0xffff0000, v251\n"
      "v_mov_b32 v24, 0xbdd2d3e8\n"
      "v_mov_b32 v25, 0xc0135761\n"
      "s_nop 1\n"
      "ds_write_b128 v6, v[116:119] offset:0\n"
      "ds_write_b128 v6, v[120:123] offset:1024\n"
      "v_mfma_f32_32x32x16_bf16 v[132:147], v[176:179], a[108:111], v[132:147]\n"
      "buffer_load_dwordx4 v63, s[32:35], s63 offen offset:3072 lds\n"
      "s_add_u32 s63, s63, 131072\n"
      "s_cmp_eq_u32 s63, 1835008\n"
      "s_cselect_b32 s63, 0, s63\n"
      "ds_write_b128 v6, v[124:127] offset:2048\n"
      "ds_write_b128 v6, v[128:131] offset:3072\n"
      "v_mfma_f32_32x32x16_bf16 v[84:99], v[164:167], a[96:99], v[84:99]\n"
      "s_add_u32 m0, s61, 8192\n"
      "v_mfma_f32_32x32x16_bf16 v[84:99], v[172:175], a[104:107], v[84:99]\n"
      "buffer_load_dwordx4 v60, s[32:35], s63 offen offset:0 lds\n"
      "s_nop 1\n"
      "ds_write_b128 v6, v[132:135] offset:4096\n"
      "ds_write_b128 v6, v[136:139] offset:5120\n"
      "ds_write_b128 v6, v[140:143] offset:6144\n"
      "ds_write_b128 v6, v[144:147] offset:7168\n"
      "s_nop 4\n"
      "v_pk_add_f32 v[84:85], v[84:85], v[148:149]\n"
      "buffer_load_dwordx4 v61, s[32:35], s63 offen offset:1024 lds\n"
      "v_pk_add_f32 v[86:87], v[86:87], v[150:151]\n"
      "v_pk_add_f32 v[88:89], v[88:89], v[152:153]\n"
      "v_pk_add_f32 v[90:91], v[90:91], v[154:155]\n"
      "v_pk_add_f32 v[92:93], v[92:93], v[156:157]\n"
      "buffer_load_dwordx4 v62, s[32:35], s63 offen offset:2048 lds\n"
      "v_pk_add_f32 v[94:95], v[94:95], v[158:159]\n"
      "v_pk_add_f32 v[96:97], v[96:97], v[160:161]\n"
      "v_pk_add_f32 v[98:99], v[98:99], v[162:163]\n"
      "s_waitcnt lgkmcnt(0)\n"
      "s_barrier\n"
      "ds_read_b128 v[100:103], v20 offset:24576\n"
      "ds_read_b128 v[116:119], v21 offset:0\n"
      "ds_read_b128 v[132:135], v22 offset:4096\n"
      "ds_read_b128 v[104:107], v20 offset:25600\n"
      "ds_read_b128 v[120:123], v21 offset:1024\n"
      "buffer_load_dwordx4 v63, s[32:35], s63 offen offset:3072 lds\n"
      "s_add_u32 s63, s63, 131072\n"
      "s_cmp_eq_u32 s63, 1835008\n"
      "s_cselect_b32 s63, 0, s63\n"
      "ds_read_b128 v[136:139], v22 offset:5120\n"
      "ds_read_b128 v[108:111], v20 offset:26624\n"
      "ds_read_b128 v[124:127], v21 offset:2048\n"
      "ds_read_b128 v[140:143], v22 offset:6144\n"
      "ds_read_b128 v[112:115], v20 offset:27648\n"
      "ds_read_b128 v[128:131], v21 offset:3072\n"
      "s_add_u32 m0, s61, 12288\n"
      "ds_read_b128 v[144:147], v22 offset:7168\n"
      "buffer_load_dwordx4 v60, s[32:35], s63 offen offset:0 lds\n"
      "s_lshl_b32 s72, s58, 7\n"
      "s_add_u32 s72, s72, 9109504\n"
      "v_and_b32 v68, 7, v1\n"
      "v_lshlrev_b32 v68, 3, v68\n"
      "buffer_load_dwordx2 v[66:67], v68, s[44:47], s72 offen sc1\n"
      "s_or_b32 s52, %1, s50\n"
      "s_cmp_eq_u32 s52, 0\n"
      "s_cselect_b32 s52, 0, 2147483648\n"
      "s_or_b32 s52, s52, 9142272\n"
      "v_or_b32 v3, s52, v37\n"
      "buffer_load_dwordx4 v61, s[32:35], s63 offen offset:1024 lds\n"
      "buffer_store_dwordx2 v[16:17], v3, s[44:47], 0 offen sc1\n"
      "s_waitcnt lgkmcnt(11)\n"
      "v_pk_add_f32 v[84:85], v[84:85], v[100:101]\n"
      "s_waitcnt lgkmcnt(9)\n"
      "v_pk_add_f32 v[116:117], v[116:117], v[132:133]\n"
      "v_pk_add_f32 v[84:85], v[84:85], v[116:117]\n"
      "v_pk_add_f32 v[86:87], v[86:87], v[102:103]\n"
      "v_pk_add_f32 v[118:119], v[118:119], v[134:135]\n"
      "v_pk_add_f32 v[86:87], v[86:87], v[118:119]\n"
      "buffer_load_dwordx4 v62, s[32:35], s63 offen offset:2048 lds\n"
      "s_waitcnt lgkmcnt(8)\n"
      "v_pk_add_f32 v[88:89], v[88:89], v[104:105]\n"
      "s_waitcnt lgkmcnt(6)\n"
      "v_pk_add_f32 v[120:121], v[120:121], v[136:137]\n"
      "v_pk_add_f32 v[88:89], v[88:89], v[120:121]\n"
      "v_pk_add_f32 v[90:91], v[90:91], v[106:107]\n"
      "v_pk_add_f32 v[122:123], v[122:123], v[138:139]\n"
      "v_pk_add_f32 v[90:91], v[90:91], v[122:123]\n"
      "s_waitcnt lgkmcnt(5)\n"
      "v_pk_add_f32 v[92:93], v[92:93], v[108:109]\n"
      "buffer_load_dwordx4 v63, s[32:35], s63 offen offset:3072 lds\n"
      "s_add_u32 s63, s63, 131072\n"
      "s_cmp_eq_u32 s63, 1835008\n"
      "s_cselect_b32 s63, 0, s63\n"
      "s_waitcnt lgkmcnt(3)\n"
      "v_pk_add_f32 v[124:125], v[124:125], v[140:141]\n"
      "v_pk_add_f32 v[92:93], v[92:93], v[124:125]\n"
      "v_pk_add_f32 v[94:95], v[94:95], v[110:111]\n"
      "v_pk_add_f32 v[126:127], v[126:127], v[142:143]\n"
      "v_pk_add_f32 v[94:95], v[94:95], v[126:127]\n"
      "s_waitcnt lgkmcnt(2)\n"
      "s_add_u32 m0, s61, 16384\n"
      "v_pk_add_f32 v[96:97], v[96:97], v[112:113]\n"
      "buffer_load_dwordx4 v60, s[32:35], s63 offen offset:0 lds\n"
      "s_waitcnt lgkmcnt(0)\n"
      "v_pk_add_f32 v[128:129], v[128:129], v[144:145]\n"
      "v_pk_add_f32 v[96:97], v[96:97], v[128:129]\n"
      "v_pk_add_f32 v[98:99], v[98:99], v[114:115]\n"
      "v_pk_add_f32 v[130:131], v[130:131], v[146:147]\n"
      "v_pk_add_f32 v[98:99], v[98:99], v[130:131]\n"
      "s_barrier\n"
      "v_pk_mul_f32 v[100:101], v[84:85], v[84:85]\n"
      "buffer_load_dwordx4 v61, s[32:35], s63 offen offset:1024 lds\n"
      "v_pk_fma_f32 v[100:101], v[100:101], v[24:25], v[24:25] op_sel:[0,0,1] op_sel_hi:[1,0,1]\n"
      "v_pk_mul_f32 v[100:101], v[100:101], v[84:85]\n"
      "v_pk_mul_f32 v[102:103], v[86:87], v[86:87]\n"
      "v_pk_fma_f32 v[102:103], v[102:103], v[24:25], v[24:25] op_sel:[0,0,1] op_sel_hi:[1,0,1]\n"
      "v_pk_mul_f32 v[102:103], v[102:103], v[86:87]\n"
      "v_pk_mul_f32 v[104:105], v[88:89], v[88:89]\n"
      "v_pk_fma_f32 v[104:105], v[104:105], v[24:25], v[24:25] op_sel:[0,0,1] op_sel_hi:[1,0,1]\n"
      "v_pk_mul_f32 v[104:105], v[104:105], v[88:89]\n"
      "v_pk_mul_f32 v[106:107], v[90:91], v[90:91]\n"
      "v_pk_fma_f32 v[106:107], v[106:107], v[24:25], v[24:25] op_sel:[0,0,1] op_sel_hi:[1,0,1]\n"
      "buffer_load_dwordx4 v62, s[32:35], s63 offen offset:2048 lds\n"
      "v_pk_mul_f32 v[106:107], v[106:107], v[90:91]\n"
      "v_pk_mul_f32 v[108:109], v[92:93], v[92:93]\n"
      "v_pk_fma_f32 v[108:109], v[108:109], v[24:25], v[24:25] op_sel:[0,0,1] op_sel_hi:[1,0,1]\n"
      "v_pk_mul_f32 v[108:109], v[108:109], v[92:93]\n"
      "v_pk_mul_f32 v[110:111], v[94:95], v[94:95]\n"
      "v_pk_fma_f32 v[110:111], v[110:111], v[24:25], v[24:25] op_sel:[0,0,1] op_sel_hi:[1,0,1]\n"
      "v_pk_mul_f32 v[110:111], v[110:111], v[94:95]\n"
      "v_pk_mul_f32 v[112:113], v[96:97], v[96:97]\n"
      "v_pk_fma_f32 v[112:113], v[112:113], v[24:25], v[24:25] op_sel:[0,0,1] op_sel_hi:[1,0,1]\n"
      "v_pk_mul_f32 v[112:113], v[112:113], v[96:97]\n"
      "buffer_load_dwordx4 v63, s[32:35], s63 offen offset:3072 lds\n"
      "s_add_u32 s63, s63, 131072\n"
      "s_cmp_eq_u32 s63, 1835008\n"
      "s_cselect_b32 s63, 0, s63\n"
      "v_pk_mul_f32 v[114:115], v[98:99], v[98:99]\n"
      "v_pk_fma_f32 v[114:115], v[114:115], v[24:25], v[24:25] op_sel:[0,0,1] op_sel_hi:[1,0,1]\n"
      "v_pk_mul_f32 v[114:115], v[114:115], v[98:99]\n"
      "v_exp_f32 v100, v100\n"
      "s_add_u32 m0, s61, 20480\n"
      "v_exp_f32 v101, v101\n"
      "buffer_load_dwordx4 v60, s[32:35], s63 offen offset:0 lds\n"
      "v_exp_f32 v102, v102\n"
      "v_exp_f32 v103, v103\n"
      "v_exp_f32 v104, v104\n"
      "v_exp_f32 v105, v105\n"
      "v_exp_f32 v106, v106\n"
      "buffer_load_dwordx4 v61, s[32:35], s63 offen offset:1024 lds\n"
      "v_exp_f32 v107, v107\n"
      "v_exp_f32 v108, v108\n"
      "v_exp_f32 v109, v109\n"
      "v_exp_f32 v110, v110\n"
      "v_exp_f32 v111, v111\n"
      "buffer_load_dwordx4 v62, s[32:35], s63 offen offset:2048 lds\n"
      "v_exp_f32 v112, v112\n"
      "v_exp_f32 v113, v113\n"
      "v_exp_f32 v114, v114\n"
      "v_exp_f32 v115, v115\n"
      "v_pk_add_f32 v[100:101], v[100:101], 1.0 op_sel_hi:[1,0]\n"
      "buffer_load_dwordx4 v63, s[32:35], s63 offen offset:3072 lds\n"
      "s_add_u32 s63, s63, 131072\n"
      "s_cmp_eq_u32 s63, 1835008\n"
      "s_cselect_b32 s63, 0, s63\n"
      "v_pk_add_f32 v[102:103], v[102:103], 1.0 op_sel_hi:[1,0]\n"
      "v_pk_add_f32 v[104:105], v[104:105], 1.0 op_sel_hi:[1,0]\n"
      "v_pk_add_f32 v[106:107], v[106:107], 1.0 op_sel_hi:[1,0]\n"
      "v_pk_add_f32 v[108:109], v[108:109], 1.0 op_sel_hi:[1,0]\n"
      "v_pk_add_f32 v[110:111], v[110:111], 1.0 op_sel_hi:[1,0]\n"
      "v_pk_add_f32 v[112:113], v[112:113], 1.0 op_sel_hi:[1,0]\n"
      "v_pk_add_f32 v[114:115], v[114:115], 1.0 op_sel_hi:[1,0]\n"
      "s_waitcnt vmcnt(26)\n"
      "ds_read_b64_tr_b16 a[112:113], v78 offset:0\n"
      "ds_read_b64_tr_b16 a[114:115], v78 offset:1024\n"
      "ds_read_b64_tr_b16 a[116:117], v79 offset:0\n"
      "ds_read_b64_tr_b16 a[118:119], v79 offset:1024\n"
      "v_rcp_f32 v100, v100\n"
      "v_rcp_f32 v101, v101\n"
      "v_rcp_f32 v102, v102\n"
      "ds_read_b64_tr_b16 a[120:121], v78 offset:2048\n"
      "ds_read_b64_tr_b16 a[122:123], v78 offset:3072\n"
      "ds_read_b64_tr_b16 a[124:125], v79 offset:2048\n"
      "ds_read_b64_tr_b16 a[126:127], v79 offset:3072\n"
      "v_rcp_f32 v103, v103\n"
      "v_rcp_f32 v104, v104\n"
      "v_rcp_f32 v105, v105\n"
      "v_rcp_f32 v106, v106\n"
      "s_mov_b32 m0, s66\n"
      "s_waitcnt lgkmcnt(0)\n"
      "buffer_load_dwordx4 v60, s[32:35], s63 offen offset:0 lds\n"
      "v_rcp_f32 v107, v107\n"
      "v_rcp_f32 v108, v108\n"
      "v_rcp_f32 v109, v109\n"
      "v_rcp_f32 v110, v110\n"
      "buffer_load_dwordx4 v61, s[32:35], s63 offen offset:1024 lds\n"
      "v_rcp_f32 v111, v111\n"
      "v_rcp_f32 v112, v112\n"
      "v_rcp_f32 v113, v113\n"
      "v_rcp_f32 v114, v114\n"
      "v_rcp_f32 v115, v115\n"
      "buffer_load_dwordx4 v62, s[32:35], s63 offen offset:2048 lds\n"
      "s_mul_i32 s52, s54, 56\n"
      "s_lshl_b32 s53, s49, 1\n"
      "s_add_u32 s52, s52, s53\n"
      "s_lshr_b32 s53, s50, 1\n"
      "s_add_u32 s52, s52, s53\n"
      "s_lshl_b32 s52, s52, 12\n"
      "s_add_u32 s52, s52, 1835008\n"
      "v_pk_mul_f32 v[84:85], v[84:85], v[100:101]\n"
      "v_pk_mul_f32 v[86:87], v[86:87], v[102:103]\n"
      "v_pk_mul_f32 v[88:89], v[88:89], v[104:105]\n"
      "v_pk_mul_f32 v[90:91], v[90:91], v[106:107]\n"
      "v_cvt_pk_bf16_f32 v116, v84, v85\n"
      "v_cvt_pk_bf16_f32 v117, v86, v87\n"
      "v_cvt_pk_bf16_f32 v118, v88, v89\n"
      "v_cvt_pk_bf16_f32 v119, v90, v91\n"
      "buffer_store_dwordx4 v[116:119], v50, s[44:47], s52 offen offset:0 sc1\n"
      "v_pk_mul_f32 v[92:93], v[92:93], v[108:109]\n"
      "v_pk_mul_f32 v[94:95], v[94:95], v[110:111]\n"
      "v_pk_mul_f32 v[96:97], v[96:97], v[112:113]\n"
      "v_pk_mul_f32 v[98:99], v[98:99], v[114:115]\n"
      "v_cvt_pk_bf16_f32 v120, v92, v93\n"
      "v_cvt_pk_bf16_f32 v121, v94, v95\n"
      "v_cvt_pk_bf16_f32 v122, v96, v97\n"
      "v_cvt_pk_bf16_f32 v123, v98, v99\n"
      "buffer_store_dwordx4 v[120:123], v50, s[44:47], s52 offen offset:2048 sc1\n"
      "s_waitcnt vmcnt(17)\n"
      "v_cmp_ne_u64 vcc, v[66:67], v[16:17]\n"
      "s_cmp_eq_u64 vcc, 0\n"
      "s_cselect_b32 s68, 0, 2147483648\n"
      "v_or_b32 v5, s68, v36\n"
      "s_lshl_b32 s52, s58, 12\n"
      "s_add_u32 s52, s52, 7634944\n"
      "buffer_load_dwordx4 v[180:183], v5, s[44:47], s52 offen offset:0 sc1\n"
      "buffer_load_dwordx4 v[184:187], v5, s[44:47], s52 offen offset:1024 sc1\n"
      "buffer_load_dwordx4 v[188:191], v5, s[44:47], s52 offen offset:2048 sc1\n"
      "buffer_load_dwordx4 v[192:195], v5, s[44:47], s52 offen offset:3072 sc1\n"
      "s_mul_i32 s70, s54, 28\n"
      "s_add_u32 s70, s70, s49\n"
      "s_lshl_b32 s70, s70, 2\n"
      "s_add_u32 s70, s70, s50\n"
      "s_mul_i32 s70, s70, 8\n"
      "s_add_u32 s70, s70, 9134080\n"
      "s_cmp_eq_u32 s68, 0\n"
      "s_cbranch_scc0 .Lk_slow\n"
      "buffer_load_dwordx4 v63, s[32:35], s63 offen offset:3072 lds\n"
      "s_add_u32 s63, s63, 131072\n"
      "s_cmp_eq_u32 s63, 1835008\n"
      "s_cselect_b32 s63, 0, s63\n"
      "ds_read_b64_tr_b16 a[128:129], v78 offset:4096\n"
      "ds_read_b64_tr_b16 a[130:131], v78 offset:5120\n"
      "ds_read_b64_tr_b16 a[132:133], v79 offset:4096\n"
      "ds_read_b64_tr_b16 a[134:135], v79 offset:5120\n"
      "ds_read_b64_tr_b16 a[136:137], v78 offset:6144\n"
      "ds_read_b64_tr_b16 a[138:139], v78 offset:7168\n"
      "ds_read_b64_tr_b16 a[140:141], v79 offset:6144\n"
      "ds_read_b64_tr_b16 a[142:143], v79 offset:7168\n"
      "s_add_u32 m0, s66, 4096\n"
      "s_waitcnt lgkmcnt(0)\n"
      "buffer_load_dwordx4 v60, s[32:35], s63 offen offset:0 lds\n"
      "buffer_load_dwordx4 v61, s[32:35], s63 offen offset:1024 lds\n"
      "buffer_load_dwordx4 v62, s[32:35], s63 offen offset:2048 lds\n"
      "buffer_load_dwordx4 v63, s[32:35], s63 offen offset:3072 lds\n"
      "s_add_u32 s63, s63, 131072\n"
      "s_cmp_eq_u32 s63, 1835008\n"
      "s_cselect_b32 s63, 0, s63\n"
      "ds_read_b64_tr_b16 a[144:145], v78 offset:8192\n"
      "ds_read_b64_tr_b16 a[146:147], v78 offset:9216\n"
      "ds_read_b64_tr_b16 a[148:149], v79 offset:8192\n"
      "ds_read_b64_tr_b16 a[150:151], v79 offset:9216\n"
      "ds_read_b64_tr_b16 a[152:153], v78 offset:10240\n"
      "ds_read_b64_tr_b16 a[154:155], v78 offset:11264\n"
      "ds_read_b64_tr_b16 a[156:157], v79 offset:10240\n"
      "ds_read_b64_tr_b16 a[158:159], v79 offset:11264\n"
      "s_add_u32 m0, s66, 8192\n"
      "s_waitcnt lgkmcnt(0)\n"
      "buffer_load_dwordx4 v60, s[32:35], s63 offen offset:0 lds\n"
      "buffer_load_dwordx4 v61, s[32:35], s63 offen offset:1024 lds\n"
      "buffer_load_dwordx4 v62, s[32:35], s63 offen offset:2048 lds\n"
      "buffer_load_dwordx4 v63, s[32:35], s63 offen offset:3072 lds\n"
      "s_add_u32 s63, s63, 131072\n"
      "s_cmp_eq_u32 s63, 1835008\n"
      "s_cselect_b32 s63, 0, s63\n"
      "ds_read_b64_tr_b16 a[160:161], v76 offset:8192\n"
      "ds_read_b64_tr_b16 a[162:163], v76 offset:9216\n"
      "ds_read_b64_tr_b16 a[164:165], v77 offset:8192\n"
      "ds_read_b64_tr_b16 a[166:167], v77 offset:9216\n"
      "ds_read_b64_tr_b16 a[168:169], v76 offset:10240\n"
      "ds_read_b64_tr_b16 a[170:171], v76 offset:11264\n"
      "ds_read_b64_tr_b16 a[172:173], v77 offset:10240\n"
      "ds_read_b64_tr_b16 a[174:175], v77 offset:11264\n"
      "s_add_u32 m0, s61, 8192\n"
      "s_waitcnt lgkmcnt(0)\n"
      "buffer_load_dwordx4 v60, s[32:35], s63 offen offset:0 lds\n"
      "s_waitcnt vmcnt(14)\n"
      "buffer_store_dwordx2 v[16:17], v37, s[44:47], s70 offen sc1\n"
      "buffer_load_dwordx4 v61, s[32:35], s63 offen offset:1024 lds\n"
      "buffer_load_dwordx4 v62, s[32:35], s63 offen offset:2048 lds\n"
      "buffer_load_dwordx4 v63, s[32:35], s63 offen offset:3072 lds\n"
      "s_add_u32 s63, s63, 131072\n"
      "s_cmp_eq_u32 s63, 1835008\n"
      "s_cselect_b32 s63, 0, s63\n"
      "ds_read_b64_tr_b16 a[176:177], v76 offset:12288\n"
      "ds_read_b64_tr_b16 a[178:179], v76 offset:13312\n"
      "ds_read_b64_tr_b16 a[180:181], v77 offset:12288\n"
      "ds_read_b64_tr_b16 a[182:183], v77 offset:13312\n"
      "ds_read_b64_tr_b16 a[184:185], v76 offset:14336\n"
      "ds_read_b64_tr_b16 a[186:187], v76 offset:15360\n"
      "ds_read_b64_tr_b16 a[188:189], v77 offset:14336\n"
      "ds_read_b64_tr_b16 a[190:191], v77 offset:15360\n"
      "s_add_u32 m0, s61, 12288\n"
      "s_waitcnt lgkmcnt(0)\n"
      "buffer_load_dwordx4 v60, s[32:35], s63 offen offset:0 lds\n"
      "s_waitcnt vmcnt(18)\n"
      "v_mfma_f32_32x32x16_bf16 v[196:211], v[180:183], v[228:231], 0\n"
      "s_waitcnt vmcnt(17)\n"
      "v_mfma_f32_32x32x16_bf16 v[196:211], v[184:187], v[232:235], v[196:211]\n"
      "s_waitcnt vmcnt(16)\n"
      "v_mfma_f32_32x32x16_bf16 v[196:211], v[188:191], v[236:239], v[196:211]\n"
      "s_waitcnt vmcnt(15)\n"
      "v_mfma_f32_32x32x16_bf16 v[196:211], v[192:195], v[240:243], v[196:211]\n"
      "s_lshl_b32 s52, s57, 2\n"
      "s_add_u32 s52, s52, s50\n"
      "s_lshl_b32 s52, s52, 4\n"
      "s_add_u32 s52, s52, s58\n"
      "s_lshl_b32 s52, s52, 11\n"
      "s_add_u32 s52, s52, 3440640\n"
      "v_cmp_eq_u32 vcc, 0, v11\n"
      "s_nop 4\n"
      "v_mov_b32 v2, v208\n"
      "v_mov_b32 v3, v208\n"
      "v_cndmask_b32 v208, 0, v208, vcc\n"
      "v_cndmask_b32 v209, 0, v209, vcc\n"
      "v_cndmask_b32 v210, 0, v210, vcc\n"
      "v_cndmask_b32 v211, 0, v211, vcc\n"
      "v_permlane32_swap_b32 v2, v3\n"
      "v_add_f32 v3, 0x358637bd, v3\n"
      "v_rcp_f32 v180, v3\n"
      "s_nop 0\n"
      "v_pk_mul_f32 v[196:197], v[196:197], v[180:181] op_sel_hi:[1,0]\n"
      "v_pk_mul_f32 v[198:199], v[198:199], v[180:181] op_sel_hi:[1,0]\n"
      "v_pk_mul_f32 v[200:201], v[200:201], v[180:181] op_sel_hi:[1,0]\n"
      "v_pk_mul_f32 v[202:203], v[202:203], v[180:181] op_sel_hi:[1,0]\n"
      "v_pk_mul_f32 v[204:205], v[204:205], v[180:181] op_sel_hi:[1,0]\n"
      "v_pk_mul_f32 v[206:207], v[206:207], v[180:181] op_sel_hi:[1,0]\n"
      "v_pk_mul_f32 v[208:209], v[208:209], v[180:181] op_sel_hi:[1,0]\n"
      "v_pk_mul_f32 v[210:211], v[210:211], v[180:181] op_sel_hi:[1,0]\n"
      "v_cvt_pk_bf16_f32 v184, v196, v197\n"
      "v_cvt_pk_bf16_f32 v185, v198, v199\n"
      "v_cvt_pk_bf16_f32 v186, v200, v201\n"
      "v_cvt_pk_bf16_f32 v187, v202, v203\n"
      "v_cvt_pk_bf16_f32 v188, v204, v205\n"
      "v_cvt_pk_bf16_f32 v189, v206, v207\n"
      "v_cvt_pk_bf16_f32 v190, v208, v209\n"
      "v_cvt_pk_bf16_f32 v191, v210, v211\n"
      "buffer_store_dwordx4 v[184:187], v36, s[44:47], s52 offen offset:0 sc1\n"
      "buffer_store_dwordx4 v[188:191], v36, s[44:47], s52 offen offset:1024 sc1\n"
      "buffer_load_dwordx4 v61, s[32:35], s63 offen offset:1024 lds\n"
      "buffer_load_dwordx4 v62, s[32:35], s63 offen offset:2048 lds\n"
      "buffer_load_dwordx4 v63, s[32:35], s63 offen offset:3072 lds\n"
      "s_add_u32 s63, s63, 131072\n"
      "s_cmp_eq_u32 s63, 1835008\n"
      "s_cselect_b32 s63, 0, s63\n"
      "ds_read_b64_tr_b16 a[192:193], v76 offset:16384\n"
      "ds_read_b64_tr_b16 a[194:195], v76 offset:17408\n"
      "ds_read_b64_tr_b16 a[196:197], v77 offset:16384\n"
      "ds_read_b64_tr_b16 a[198:199], v77 offset:17408\n"
      "ds_read_b64_tr_b16 a[200:201], v76 offset:18432\n"
      "ds_read_b64_tr_b16 a[202:203], v76 offset:19456\n"
      "ds_read_b64_tr_b16 a[204:205], v77 offset:18432\n"
      "ds_read_b64_tr_b16 a[206:207], v77 offset:19456\n"
      "s_add_u32 m0, s61, 16384\n"
      "s_waitcnt lgkmcnt(0)\n"
      "buffer_load_dwordx4 v60, s[32:35], s63 offen offset:0 lds\n"
      "buffer_load_dwordx4 v61, s[32:35], s63 offen offset:1024 lds\n"
      "buffer_load_dwordx4 v62, s[32:35], s63 offen offset:2048 lds\n"
      "buffer_load_dwordx4 v63, s[32:35], s63 offen offset:3072 lds\n"
      "s_add_u32 s63, s63, 131072\n"
      "s_cmp_eq_u32 s63, 1835008\n"
      "s_cselect_b32 s63, 0, s63\n"
      "ds_read_b64_tr_b16 a[208:209], v76 offset:20480\n"
      "ds_read_b64_tr_b16 a[210:211], v76 offset:21504\n"
      "ds_read_b64_tr_b16 a[212:213], v77 offset:20480\n"
      "ds_read_b64_tr_b16 a[214:215], v77 offset:21504\n"
      "ds_read_b64_tr_b16 a[216:217], v76 offset:22528\n"
      "ds_read_b64_tr_b16 a[218:219], v76 offset:23552\n"
      "ds_read_b64_tr_b16 a[220:221], v77 offset:22528\n"
      "ds_read_b64_tr_b16 a[222:223], v77 offset:23552\n"
      "s_add_u32 m0, s61, 20480\n"
      "s_waitcnt lgkmcnt(0)\n"
      "buffer_load_dwordx4 v60, s[32:35], s63 offen offset:0 lds\n"
      "buffer_load_dwordx4 v61, s[32:35], s63 offen offset:1024 lds\n"
      "buffer_load_dwordx4 v62, s[32:35], s63 offen offset:2048 lds\n"
      "buffer_load_dwordx4 v63, s[32:35], s63 offen offset:3072 lds\n"
      "s_add_u32 s64, s76, 14\n"
      "s_and_b32 s64, s64, 3\n"
      "s_mul_i32 s64, s64, 25088\n"
      "s_add_u32 m0, s61, 24576\n"
      "s_nop 0\n"
      "buffer_load_dwordx4 v72, s[20:23], s64 offen offset:0 lds\n"
      "buffer_load_dwordx4 v73, s[20:23], s64 offen offset:1024 lds\n"
      "buffer_load_dwordx4 v74, s[20:23], s64 offen offset:2048 lds\n"
      "s_lshl_b32 s52, s57, 1\n"
      "s_lshr_b32 s53, s50, 1\n"
      "s_add_u32 s52, s52, s53\n"
      "s_lshl_b32 s52, s52, 2\n"
      "s_lshr_b32 s53, s58, 2\n"
      "s_add_u32 s52, s52, s53\n"
      "s_lshl_b32 s52, s52, 7\n"
      "s_and_b32 s53, s50, 1\n"
      "s_lshl_b32 s53, s53, 5\n"
      "s_add_u32 s52, s52, s53\n"
      "s_and_b32 s53, s58, 3\n"
      "s_lshl_b32 s53, s53, 3\n"
      "s_add_u32 s52, s52, s53\n"
      "s_add_u32 s52, s52, 9117696\n"
      "s_waitcnt vmcnt(14)\n"
      "buffer_store_dwordx2 v[16:17], v37, s[44:47], s52 offen sc1\n"
      "buffer_load_dwordx4 v75, s[20:23], s64 offen offset:3072 lds\n"
      "s_add_u32 s64, s76, 15\n"
      "s_and_b32 s64, s64, 3\n"
      "s_mul_i32 s64, s64, 25088\n"
      "s_mov_b32 m0, s61\n"
      "s_nop 0\n"
      "buffer_load_dwordx4 v72, s[20:23], s64 offen offset:0 lds\n"
      "buffer_load_dwordx4 v73, s[20:23], s64 offen offset:1024 lds\n"
      "buffer_load_dwordx4 v74, s[20:23], s64 offen offset:2048 lds\n"
      "buffer_load_dwordx4 v75, s[20:23], s64 offen offset:3072 lds\n"
      "s_add_u32 s64, s76, 16\n"
      "s_and_b32 s64, s64, 3\n"
      "s_mul_i32 s64, s64, 25088\n"
      "s_add_u32 m0, s61, 4096\n"
      "s_nop 0\n"
      "buffer_load_dwordx4 v72, s[20:23], s64 offen offset:0 lds\n"
      "buffer_load_dwordx4 v73, s[20:23], s64 offen offset:1024 lds\n"
      "buffer_load_dwordx4 v74, s[20:23], s64 offen offset:2048 lds\n"
      "s_lshl_b32 s73, s76, 2\n"
      "s_add_u32 s73, s73, s50\n"
      "s_mul_i32 s73, s73, 224\n"
      "s_add_u32 s73, s73, 9134080\n"
      "v_min_u32 v69, 27, v1\n"
      "v_mul_u32_u24 v69, 8, v69\n"
      "buffer_load_dwordx2 v[70:71], v69, s[44:47], s73 offen sc1\n"
      "buffer_load_dwordx4 v75, s[20:23], s64 offen offset:3072 lds\n"
      "ds_read_b64_tr_b16 a[224:225], v78 offset:0\n"
      "ds_read_b64_tr_b16 a[226:227], v78 offset:1024\n"
      "ds_read_b64_tr_b16 a[228:229], v79 offset:0\n"
      "ds_read_b64_tr_b16 a[230:231], v79 offset:1024\n"
      "ds_read_b64_tr_b16 a[232:233], v78 offset:2048\n"
      "ds_read_b64_tr_b16 a[234:235], v78 offset:3072\n"
      "ds_read_b64_tr_b16 a[236:237], v79 offset:2048\n"
      "ds_read_b64_tr_b16 a[238:239], v79 offset:3072\n"
      "s_add_u32 s64, s76, 17\n"
      "s_and_b32 s64, s64, 3\n"
      "s_mul_i32 s64, s64, 25088\n"
      "s_mov_b32 m0, s66\n"
      "s_waitcnt lgkmcnt(0)\n"
      "buffer_load_dwordx4 v72, s[20:23], s64 offen offset:0 lds\n"
      "buffer_load_dwordx4 v73, s[20:23], s64 offen offset:1024 lds\n"
      "buffer_load_dwordx4 v74, s[20:23], s64 offen offset:2048 lds\n"
      "buffer_load_dwordx4 v75, s[20:23], s64 offen offset:3072 lds\n"
      ".Lk_g2:\n"
      "s_mul_i32 s67, s76, 4\n"
      "s_add_u32 s67, s67, s50\n"
      "s_mul_i32 s67, s67, 57344\n"
      "s_add_u32 s67, s67, 1835008\n"
      "s_lshl_b32 s52, s60, 12\n"
      "s_add_u32 s62, s67, s52\n"
      "s_add_u32 s67, s67, 57344\n"
      "s_lshl_b32 s52, s75, 1\n"
      "s_and_b32 s53, s50, 1\n"
      "s_add_u32 s52, s52, s53\n"
      "s_lshl_b32 s52, s52, 5\n"
      "v_add_u32 v5, s52, v10\n"
      "v_mul_u32_u24 v6, 896, v5\n"
      "v_lshlrev_b32 v5, 1, v5\n"
      "s_lshl_b32 s52, s76, 1\n"
      "s_lshr_b32 s53, s50, 1\n"
      "s_add_u32 s52, s52, s53\n"
      "s_lshl_b32 s52, s52, 6\n"
      "v_lshl_add_u32 v2, v11, 4, s52\n"
      "v_add_u32 v6, v6, v2\n"
      "s_waitcnt vmcnt(5)\n"
      "v_cmp_ne_u64 vcc, v[70:71], v[16:17]\n"
      "s_cbranch_vccz .Lk_ok4\n"
      "s_mov_b32 s52, 20000\n"
      "s_nop 2\n"
      ".Lk_spin4:\n"
      "s_sleep 2\n"
      "buffer_load_dwordx2 v[70:71], v69, s[44:47], s73 offen sc1\n"
      "s_waitcnt vmcnt(0)\n"
      "v_cmp_ne_u64 vcc, v[70:71], v[16:17]\n"
      "s_cbranch_vccz .Lk_ok4\n"
      "s_sub_u32 s52, s52, 1\n"
      "s_cbranch_scc0 .Lk_spin4\n"
      "s_nop 3\n"
      ".Lk_ok4:\n"
      "buffer_load_dwordx4 a[240:243], v26, s[44:47], s62 offen offset:0 sc1\n"
      "buffer_load_dwordx4 a[244:247], v27, s[44:47], s62 offen offset:0 sc1\n"
      "buffer_load_dwordx4 a[248:251], v26, s[44:47], s62 offen offset:2048 sc1\n"
      "buffer_load_dwordx4 a[252:255], v27, s[44:47], s62 offen offset:2048 sc1\n"
      "s_add_u32 s62, s62, 4096\n"
      "s_sub_u32 s53, s62, 57344\n"
      "s_cmp_eq_u32 s62, s67\n"
      "s_cselect_b32 s62, s53, s62\n"
      "buffer_load_dwordx4 a[0:3], v26, s[44:47], s62 offen offset:0 sc1\n"
      "buffer_load_dwordx4 a[4:7], v27, s[44:47], s62 offen offset:0 sc1\n"
      "buffer_load_dwordx4 a[8:11], v26, s[44:47], s62 offen offset:2048 sc1\n"
      "buffer_load_dwordx4 a[12:15], v27, s[44:47], s62 offen offset:2048 sc1\n"
      "s_add_u32 s62, s62, 4096\n"
      "s_sub_u32 s53, s62, 57344\n"
      "s_cmp_eq_u32 s62, s67\n"
      "s_cselect_b32 s62, s53, s62\n"
      "buffer_load_dwordx4 a[16:19], v26, s[44:47], s62 offen offset:0 sc1\n"
      "buffer_load_dwordx4 a[20:23], v27, s[44:47], s62 offen offset:0 sc1\n"
      "buffer_load_dwordx4 a[24:27], v26, s[44:47], s62 offen offset:2048 sc1\n"
      "buffer_load_dwordx4 a[28:31], v27, s[44:47], s62 offen offset:2048 sc1\n"
      "s_add_u32 s62, s62, 4096\n"
      "s_sub_u32 s53, s62, 57344\n"
      "s_cmp_eq_u32 s62, s67\n"
      "s_cselect_b32 s62, s53, s62\n"
      "buffer_load_dwordx4 a[32:35], v26, s[44:47], s62 offen offset:0 sc1\n"
      "buffer_load_dwordx4 a[36:39], v27, s[44:47], s62 offen offset:0 sc1\n"
      "buffer_load_dwordx4 a[40:43], v26, s[44:47], s62 offen offset:2048 sc1\n"
      "buffer_load_dwordx4 a[44:47], v27, s[44:47], s62 offen offset:2048 sc1\n"
      "s_add_u32 s62, s62, 4096\n"
      "s_sub_u32 s53, s62, 57344\n"
      "s_cmp_eq_u32 s62, s67\n"
      "s_cselect_b32 s62, s53, s62\n"
      "buffer_load_dwordx4 a[48:51], v26, s[44:47], s62 offen offset:0 sc1\n"
      "buffer_load_dwordx4 a[52:55], v27, s[44:47], s62 offen offset:0 sc1\n"
      "buffer_load_dwordx4 a[56:59], v26, s[44:47], s62 offen offset:2048 sc1\n"
      "buffer_load_dwordx4 a[60:63], v27, s[44:47], s62 offen offset:2048 sc1\n"
      "s_add_u32 s62, s62, 4096\n"
      "s_sub_u32 s53, s62, 57344\n"
      "s_cmp_eq_u32 s62, s67\n"
      "s_cselect_b32 s62, s53, s62\n"
      "s_waitcnt vmcnt(19)\n"
      "v_mfma_f32_32x32x16_bf16 v[84:99], a[240:243], a[112:115], 0\n"
      "v_mfma_f32_32x32x16_bf16 v[100:115], a[240:243], a[116:119], 0\n"
      "buffer_load_dwordx4 a[64:67], v26, s[44:47], s62 offen offset:0 sc1\n"
      "s_waitcnt vmcnt(19)\n"
      "v_mfma_f32_32x32x16_bf16 v[116:131], a[244:247], a[112:115], 0\n"
      "v_mfma_f32_32x32x16_bf16 v[132:147], a[244:247], a[116:119], 0\n"
      "buffer_load_dwordx4 a[68:71], v27, s[44:47], s62 offen offset:0 sc1\n"
      "s_waitcnt vmcnt(19)\n"
      "v_mfma_f32_32x32x16_bf16 v[84:99], a[248:251], a[120:123], v[84:99]\n"
      "v_mfma_f32_32x32x16_bf16 v[100:115], a[248:251], a[124:127], v[100:115]\n"
      "buffer_load_dwordx4 a[72:75], v26, s[44:47], s62 offen offset:2048 sc1\n"
      "s_waitcnt vmcnt(19)\n"
      "v_mfma_f32_32x32x16_bf16 v[116:131], a[252:255], a[120:123], v[116:131]\n"
      "v_mfma_f32_32x32x16_bf16 v[132:147], a[252:255], a[124:127], v[132:147]\n"
      "buffer_load_dwordx4 a[76:79], v27, s[44:47], s62 offen offset:2048 sc1\n"
      "s_add_u32 s62, s62, 4096\n"
      "s_sub_u32 s53, s62, 57344\n"
      "s_cmp_eq_u32 s62, s67\n"
      "s_cselect_b32 s62, s53, s62\n"
      "s_waitcnt vmcnt(19)\n"
      "v_mfma_f32_32x32x16_bf16 v[84:99], a[0:3], a[128:131], v[84:99]\n"
      "v_mfma_f32_32x32x16_bf16 v[100:115], a[0:3], a[132:135], v[100:115]\n"
      "buffer_load_dwordx4 a[80:83], v26, s[44:47], s62 offen offset:0 sc1\n"
      "s_waitcnt vmcnt(19)\n"
      "v_mfma_f32_32x32x16_bf16 v[116:131], a[4:7], a[128:131], v[116:131]\n"
      "v_mfma_f32_32x32x16_bf16 v[132:147], a[4:7], a[132:135], v[132:147]\n"
      "buffer_load_dwordx4 a[84:87], v27, s[44:47], s62 offen offset:0 sc1\n"
      "s_waitcnt vmcnt(19)\n"
      "v_mfma_f32_32x32x16_bf16 v[84:99], a[8:11], a[136:139], v[84:99]\n"
      "v_mfma_f32_32x32x16_bf16 v[100:115], a[8:11], a[140:143], v[100:115]\n"
      "buffer_load_dwordx4 a[88:91], v26, s[44:47], s62 offen offset:2048 sc1\n"
      "s_waitcnt vmcnt(19)\n"
      "v_mfma_f32_32x32x16_bf16 v[116:131], a[12:15], a[136:139], v[116:131]\n"
      "v_mfma_f32_32x32x16_bf16 v[132:147], a[12:15], a[140:143], v[132:147]\n"
      "buffer_load_dwordx4 a[92:95], v27, s[44:47], s62 offen offset:2048 sc1\n"
      "s_add_u32 s62, s62, 4096\n"
      "s_sub_u32 s53, s62, 57344\n"
      "s_cmp_eq_u32 s62, s67\n"
      "s_cselect_b32 s62, s53, s62\n"
      "s_waitcnt vmcnt(19)\n"
      "v_mfma_f32_32x32x16_bf16 v[84:99], a[16:19], a[144:147], v[84:99]\n"
      "v_mfma_f32_32x32x16_bf16 v[100:115], a[16:19], a[148:151], v[100:115]\n"
      "buffer_load_dwordx4 a[96:99], v26, s[44:47], s62 offen offset:0 sc1\n"
      "s_waitcnt vmcnt(19)\n"
      "v_mfma_f32_32x32x16_bf16 v[116:131], a[20:23], a[144:147], v[116:131]\n"
      "v_mfma_f32_32x32x16_bf16 v[132:147], a[20:23], a[148:151], v[132:147]\n"
      "buffer_load_dwordx4 a[100:103], v27, s[44:47], s62 offen offset:0 sc1\n"
      "s_waitcnt vmcnt(19)\n"
      "v_mfma_f32_32x32x16_bf16 v[84:99], a[24:27], a[152:155], v[84:99]\n"
      "v_mfma_f32_32x32x16_bf16 v[100:115], a[24:27], a[156:159], v[100:115]\n"
      "buffer_load_dwordx4 a[104:107], v26, s[44:47], s62 offen offset:2048 sc1\n"
      "s_waitcnt vmcnt(19)\n"
      "v_mfma_f32_32x32x16_bf16 v[116:131], a[28:31], a[152:155], v[116:131]\n"
      "v_mfma_f32_32x32x16_bf16 v[132:147], a[28:31], a[156:159], v[132:147]\n"
      "buffer_load_dwordx4 a[108:111], v27, s[44:47], s62 offen offset:2048 sc1\n"
      "s_add_u32 s62, s62, 4096\n"
      "s_sub_u32 s53, s62, 57344\n"
      "s_cmp_eq_u32 s62, s67\n"
      "s_cselect_b32 s62, s53, s62\n"
      "s_waitcnt vmcnt(19)\n"
      "v_mfma_f32_32x32x16_bf16 v[84:99], a[32:35], a[160:163], v[84:99]\n"
      "v_mfma_f32_32x32x16_bf16 v[100:115], a[32:35], a[164:167], v[100:115]\n"
      "buffer_load_dwordx4 a[240:243], v26, s[44:47], s62 offen offset:0 sc1\n"
      "s_waitcnt vmcnt(19)\n"
      "v_mfma_f32_32x32x16_bf16 v[116:131], a[36:39], a[160:163], v[116:131]\n"
      "v_mfma_f32_32x32x16_bf16 v[132:147], a[36:39], a[164:167], v[132:147]\n"
      "buffer_load_dwordx4 a[244:247], v27, s[44:47], s62 offen offset:0 sc1\n"
      "s_waitcnt vmcnt(19)\n"
      "v_mfma_f32_32x32x16_bf16 v[84:99], a[40:43], a[168:171], v[84:99]\n"
      "v_mfma_f32_32x32x16_bf16 v[100:115], a[40:43], a[172:175], v[100:115]\n"
      "buffer_load_dwordx4 a[248:251], v26, s[44:47], s62 offen offset:2048 sc1\n"
      "s_waitcnt vmcnt(19)\n"
      "v_mfma_f32_32x32x16_bf16 v[116:131], a[44:47], a[168:171], v[116:131]\n"
      "v_mfma_f32_32x32x16_bf16 v[132:147], a[44:47], a[172:175], v[132:147]\n"
      "buffer_load_dwordx4 a[252:255], v27, s[44:47], s62 offen offset:2048 sc1\n"
      "s_add_u32 s62, s62, 4096\n"
      "s_sub_u32 s53, s62, 57344\n"
      "s_cmp_eq_u32 s62, s67\n"
      "s_cselect_b32 s62, s53, s62\n"
      "s_waitcnt vmcnt(19)\n"
      "v_mfma_f32_32x32x16_bf16 v[84:99], a[48:51], a[176:179], v[84:99]\n"
      "v_mfma_f32_32x32x16_bf16 v[100:115], a[48:51], a[180:183], v[100:115]\n"
      "buffer_load_dwordx4 a[0:3], v26, s[44:47], s62 offen offset:0 sc1\n"
      "s_waitcnt vmcnt(19)\n"
      "v_mfma_f32_32x32x16_bf16 v[116:131], a[52:55], a[176:179], v[116:131]\n"
      "v_mfma_f32_32x32x16_bf16 v[132:147], a[52:55], a[180:183], v[132:147]\n"
      "buffer_load_dwordx4 a[4:7], v27, s[44:47], s62 offen offset:0 sc1\n"
      "s_waitcnt vmcnt(19)\n"
      "v_mfma_f32_32x32x16_bf16 v[84:99], a[56:59], a[184:187], v[84:99]\n"
      "v_mfma_f32_32x32x16_bf16 v[100:115], a[56:59], a[188:191], v[100:115]\n"
      "buffer_load_dwordx4 a[8:11], v26, s[44:47], s62 offen offset:2048 sc1\n"
      "s_waitcnt vmcnt(19)\n"
      "v_mfma_f32_32x32x16_bf16 v[116:131], a[60:63], a[184:187], v[116:131]\n"
      "v_mfma_f32_32x32x16_bf16 v[132:147], a[60:63], a[188:191], v[132:147]\n"
      "buffer_load_dwordx4 a[12:15], v27, s[44:47], s62 offen offset:2048 sc1\n"
      "s_add_u32 s62, s62, 4096\n"
      "s_sub_u32 s53, s62, 57344\n"
      "s_cmp_eq_u32 s62, s67\n"
      "s_cselect_b32 s62, s53, s62\n"
      "s_waitcnt vmcnt(19)\n"
      "v_mfma_f32_32x32x16_bf16 v[84:99], a[64:67], a[192:195], v[84:99]\n"
      "v_mfma_f32_32x32x16_bf16 v[100:115], a[64:67], a[196:199], v[100:115]\n"
      "buffer_load_dwordx4 a[16:19], v26, s[44:47], s62 offen offset:0 sc1\n"
      "s_waitcnt vmcnt(19)\n"
      "v_mfma_f32_32x32x16_bf16 v[116:131], a[68:71], a[192:195], v[116:131]\n"
      "v_mfma_f32_32x32x16_bf16 v[132:147], a[68:71], a[196:199], v[132:147]\n"
      "buffer_load_dwordx4 a[20:23], v27, s[44:47], s62 offen offset:0 sc1\n"
      "s_waitcnt vmcnt(19)\n"
      "v_mfma_f32_32x32x16_bf16 v[84:99], a[72:75], a[200:203], v[84:99]\n"
      "v_mfma_f32_32x32x16_bf16 v[100:115], a[72:75], a[204:207], v[100:115]\n"
      "buffer_load_dwordx4 a[24:27], v26, s[44:47], s62 offen offset:2048 sc1\n"
      "s_waitcnt vmcnt(19)\n"
      "v_mfma_f32_32x32x16_bf16 v[116:131], a[76:79], a[200:203], v[116:131]\n"
      "v_mfma_f32_32x32x16_bf16 v[132:147], a[76:79], a[204:207], v[132:147]\n"
      "buffer_load_dwordx4 a[28:31], v27, s[44:47], s62 offen offset:2048 sc1\n"
      "s_add_u32 s62, s62, 4096\n"
      "s_sub_u32 s53, s62, 57344\n"
      "s_cmp_eq_u32 s62, s67\n"
      "s_cselect_b32 s62, s53, s62\n"
      "s_waitcnt vmcnt(19)\n"
      "v_mfma_f32_32x32x16_bf16 v[84:99], a[80:83], a[208:211], v[84:99]\n"
      "v_mfma_f32_32x32x16_bf16 v[100:115], a[80:83], a[212:215], v[100:115]\n"
      "buffer_load_dwordx4 a[32:35], v26, s[44:47], s62 offen offset:0 sc1\n"
      "s_waitcnt vmcnt(19)\n"
      "v_mfma_f32_32x32x16_bf16 v[116:131], a[84:87], a[208:211], v[116:131]\n"
      "v_mfma_f32_32x32x16_bf16 v[132:147], a[84:87], a[212:215], v[132:147]\n"
      "buffer_load_dwordx4 a[36:39], v27, s[44:47], s62 offen offset:0 sc1\n"
      "s_waitcnt vmcnt(19)\n"
      "v_mfma_f32_32x32x16_bf16 v[84:99], a[88:91], a[216:219], v[84:99]\n"
      "v_mfma_f32_32x32x16_bf16 v[100:115], a[88:91], a[220:223], v[100:115]\n"
      "buffer_load_dwordx4 a[40:43], v26, s[44:47], s62 offen offset:2048 sc1\n"
      "s_waitcnt vmcnt(19)\n"
      "v_mfma_f32_32x32x16_bf16 v[116:131], a[92:95], a[216:219], v[116:131]\n"
      "v_mfma_f32_32x32x16_bf16 v[132:147], a[92:95], a[220:223], v[132:147]\n"
      "buffer_load_dwordx4 a[44:47], v27, s[44:47], s62 offen offset:2048 sc1\n"
      "s_add_u32 s62, s62, 4096\n"
      "s_sub_u32 s53, s62, 57344\n"
      "s_cmp_eq_u32 s62, s67\n"
      "s_cselect_b32 s62, s53, s62\n"
      "s_lshl_b32 s73, s75, 2\n"
      "s_add_u32 s73, s73, s50\n"
      "s_lshl_b32 s73, s73, 7\n"
      "s_add_u32 s73, s73, 9117696\n"
      "v_and_b32 v69, 7, v1\n"
      "v_lshlrev_b32 v69, 3, v69\n"
      "buffer_load_dwordx2 v[70:71], v69, s[44:47], s73 offen sc1\n"
      "s_waitcnt vmcnt(20)\n"
      "v_mfma_f32_32x32x16_bf16 v[84:99], a[96:99], a[224:227], v[84:99]\n"
      "ds_read_b64_tr_b16 v[148:149], v78 offset:4096\n"
      "v_mfma_f32_32x32x16_bf16 v[100:115], a[96:99], a[228:231], v[100:115]\n"
      "ds_read_b64_tr_b16 v[150:151], v78 offset:5120\n"
      "buffer_load_dwordx4 a[48:51], v26, s[44:47], s62 offen offset:0 sc1\n"
      "s_waitcnt vmcnt(20)\n"
      "v_mfma_f32_32x32x16_bf16 v[116:131], a[100:103], a[224:227], v[116:131]\n"
      "ds_read_b64_tr_b16 v[152:153], v79 offset:4096\n"
      "v_mfma_f32_32x32x16_bf16 v[132:147], a[100:103], a[228:231], v[132:147]\n"
      "ds_read_b64_tr_b16 v[154:155], v79 offset:5120\n"
      "buffer_load_dwordx4 a[52:55], v27, s[44:47], s62 offen offset:0 sc1\n"
      "s_waitcnt vmcnt(20)\n"
      "v_mfma_f32_32x32x16_bf16 v[84:99], a[104:107], a[232:235], v[84:99]\n"
      "ds_read_b64_tr_b16 v[156:157], v78 offset:6144\n"
      "v_mfma_f32_32x32x16_bf16 v[100:115], a[104:107], a[236:239], v[100:115]\n"
      "ds_read_b64_tr_b16 v[158:159], v78 offset:7168\n"
      "buffer_load_dwordx4 a[56:59], v26, s[44:47], s62 offen offset:2048 sc1\n"
      "s_waitcnt vmcnt(20)\n"
      "v_mfma_f32_32x32x16_bf16 v[116:131], a[108:111], a[232:235], v[116:131]\n"
      "ds_read_b64_tr_b16 v[160:161], v79 offset:6144\n"
      "v_mfma_f32_32x32x16_bf16 v[132:147], a[108:111], a[236:239], v[132:147]\n"
      "ds_read_b64_tr_b16 v[162:163], v79 offset:7168\n"
      "buffer_load_dwordx4 a[60:63], v27, s[44:47], s62 offen offset:2048 sc1\n"
      "s_add_u32 s62, s62, 4096\n"
      "s_sub_u32 s53, s62, 57344\n"
      "s_cmp_eq_u32 s62, s67\n"
      "s_cselect_b32 s62, s53, s62\n"
      "s_waitcnt vmcnt(20) lgkmcnt(6)\n"
      "v_mfma_f32_32x32x16_bf16 v[84:99], a[240:243], v[148:151], v[84:99]\n"
      "ds_read_b64_tr_b16 v[164:165], v78 offset:8192\n"
      "s_waitcnt lgkmcnt(5)\n"
      "v_mfma_f32_32x32x16_bf16 v[100:115], a[240:243], v[152:155], v[100:115]\n"
      "ds_read_b64_tr_b16 v[166:167], v78 offset:9216\n"
      "buffer_load_dwordx4 a[64:67], v26, s[44:47], s62 offen offset:0 sc1\n"
      "s_waitcnt vmcnt(20)\n"
      "v_mfma_f32_32x32x16_bf16 v[116:131], a[244:247], v[148:151], v[116:131]\n"
      "ds_read_b64_tr_b16 v[168:169], v79 offset:8192\n"
      "v_mfma_f32_32x32x16_bf16 v[132:147], a[244:247], v[152:155], v[132:147]\n"
      "ds_read_b64_tr_b16 v[170:171], v79 offset:9216\n"
      "buffer_load_dwordx4 a[68:71], v27, s[44:47], s62 offen offset:0 sc1\n"
      "s_waitcnt vmcnt(20) lgkmcnt(6)\n"
      "v_mfma_f32_32x32x16_bf16 v[84:99], a[248:251], v[156:159], v[84:99]\n"
      "ds_read_b64_tr_b16 v[172:173], v78 offset:10240\n"
      "s_waitcnt lgkmcnt(5)\n"
      "v_mfma_f32_32x32x16_bf16 v[100:115], a[248:251], v[160:163], v[100:115]\n"
      "ds_read_b64_tr_b16 v[174:175], v78 offset:11264\n"
      "buffer_load_dwordx4 a[72:75], v26, s[44:47], s62 offen offset:2048 sc1\n"
      "s_waitcnt vmcnt(20)\n"
      "v_mfma_f32_32x32x16_bf16 v[116:131], a[252:255], v[156:159], v[116:131]\n"
      "ds_read_b64_tr_b16 v[176:177], v79 offset:10240\n"
      "v_mfma_f32_32x32x16_bf16 v[132:147], a[252:255], v[160:163], v[132:147]\n"
      "ds_read_b64_tr_b16 v[178:179], v79 offset:11264\n"
      "buffer_load_dwordx4 a[76:79], v27, s[44:47], s62 offen offset:2048 sc1\n"
      "s_waitcnt vmcnt(20) lgkmcnt(6)\n"
      "v_mfma_f32_32x32x16_bf16 v[84:99], a[0:3], v[164:167], v[84:99]\n"
      "ds_read_b64_tr_b16 v[148:149], v76 offset:8192\n"
      "s_waitcnt lgkmcnt(5)\n"
      "v_mfma_f32_32x32x16_bf16 v[100:115], a[0:3], v[168:171], v[100:115]\n"
      "ds_read_b64_tr_b16 v[150:151], v76 offset:9216\n"
      "s_waitcnt vmcnt(19)\n"
      "v_mfma_f32_32x32x16_bf16 v[116:131], a[4:7], v[164:167], v[116:131]\n"
      "ds_read_b64_tr_b16 v[152:153], v77 offset:8192\n"
      "v_mfma_f32_32x32x16_bf16 v[132:147], a[4:7], v[168:171], v[132:147]\n"
      "ds_read_b64_tr_b16 v[154:155], v77 offset:9216\n"
      "s_waitcnt vmcnt(18) lgkmcnt(6)\n"
      "v_mfma_f32_32x32x16_bf16 v[84:99], a[8:11], v[172:175], v[84:99]\n"
      "ds_read_b64_tr_b16 v[156:157], v76 offset:10240\n"
      "s_waitcnt lgkmcnt(5)\n"
      "v_mfma_f32_32x32x16_bf16 v[100:115], a[8:11], v[176:179], v[100:115]\n"
      "ds_read_b64_tr_b16 v[158:159], v76 offset:11264\n"
      "s_waitcnt vmcnt(17)\n"
      "v_mfma_f32_32x32x16_bf16 v[116:131], a[12:15], v[172:175], v[116:131]\n"
      "ds_read_b64_tr_b16 v[160:161], v77 offset:10240\n"
      "v_mfma_f32_32x32x16_bf16 v[132:147], a[12:15], v[176:179], v[132:147]\n"
      "ds_read_b64_tr_b16 v[162:163], v77 offset:11264\n"
      "s_waitcnt vmcnt(16) lgkmcnt(6)\n"
      "v_mfma_f32_32x32x16_bf16 v[84:99], a[16:19], v[148:151], v[84:99]\n"
      "ds_read_b64_tr_b16 v[164:165], v76 offset:12288\n"
      "s_waitcnt lgkmcnt(5)\n"
      "v_mfma_f32_32x32x16_bf16 v[100:115], a[16:19], v[152:155], v[100:115]\n"
      "ds_read_b64_tr_b16 v[166:167], v76 offset:13312\n"
      "s_waitcnt vmcnt(8)\n"
      "v_cmp_ne_u64 vcc, v[70:71], v[16:17]\n"
      "s_cbranch_vccz .Lk_ok5\n"
      "s_mov_b32 s52, 20000\n"
      "s_nop 6\n"
      ".Lk_spin5:\n"
      "s_sleep 2\n"
      "buffer_load_dwordx2 v[70:71], v69, s[44:47], s73 offen sc1\n"
      "s_waitcnt vmcnt(0)\n"
      "v_cmp_ne_u64 vcc, v[70:71], v[16:17]\n"
      "s_cbranch_vccz .Lk_ok5\n"
      "s_sub_u32 s52, s52, 1\n"
      "s_cbranch_scc0 .Lk_spin5\n"
      "s_nop 7\n"
      ".Lk_ok5:\n"
      "s_lshl_b32 s67, s75, 3\n"
      "s_add_u32 s67, s67, s50\n"
      "s_lshl_b32 s67, s67, 13\n"
      "s_add_u32 s67, s67, 3440640\n"
      "s_add_u32 s62, s76, 14\n"
      "s_and_b32 s62, s62, 3\n"
      "s_lshl_b32 s62, s62, 11\n"
      "s_add_u32 s62, s62, s67\n"
      "buffer_load_dwordx4 a[80:83], v30, s[44:47], s62 offen offset:0 sc1\n"
      "v_mfma_f32_32x32x16_bf16 v[116:131], a[20:23], v[148:151], v[116:131]\n"
      "ds_read_b64_tr_b16 v[168:169], v77 offset:12288\n"
      "v_mfma_f32_32x32x16_bf16 v[132:147], a[20:23], v[152:155], v[132:147]\n"
      "ds_read_b64_tr_b16 v[170:171], v77 offset:13312\n"
      "buffer_load_dwordx4 a[84:87], v31, s[44:47], s62 offen offset:0 sc1\n"
      "s_waitcnt lgkmcnt(6)\n"
      "v_mfma_f32_32x32x16_bf16 v[84:99], a[24:27], v[156:159], v[84:99]\n"
      "ds_read_b64_tr_b16 v[172:173], v76 offset:14336\n"
      "s_waitcnt lgkmcnt(5)\n"
      "v_mfma_f32_32x32x16_bf16 v[100:115], a[24:27], v[160:163], v[100:115]\n"
      "ds_read_b64_tr_b16 v[174:175], v76 offset:15360\n"
      "buffer_load_dwordx4 a[88:91], v30, s[44:47], s62 offen offset:1024 sc1\n"
      "v_mfma_f32_32x32x16_bf16 v[116:131], a[28:31], v[156:159], v[116:131]\n"
      "ds_read_b64_tr_b16 v[176:177], v77 offset:14336\n"
      "v_mfma_f32_32x32x16_bf16 v[132:147], a[28:31], v[160:163], v[132:147]\n"
      "ds_read_b64_tr_b16 v[178:179], v77 offset:15360\n"
      "buffer_load_dwordx4 a[92:95], v31, s[44:47], s62 offen offset:1024 sc1\n"
      "s_waitcnt lgkmcnt(6)\n"
      "v_mfma_f32_32x32x16_bf16 v[84:99], a[32:35], v[164:167], v[84:99]\n"
      "ds_read_b64_tr_b16 v[148:149], v76 offset:16384\n"
      "s_waitcnt lgkmcnt(5)\n"
      "v_mfma_f32_32x32x16_bf16 v[100:115], a[32:35], v[168:171], v[100:115]\n"
      "ds_read_b64_tr_b16 v[150:151], v76 offset:17408\n"
      "s_add_u32 s62, s76, 15\n"
      "s_and_b32 s62, s62, 3\n"
      "s_lshl_b32 s62, s62, 11\n"
      "s_add_u32 s62, s62, s67\n"
      "buffer_load_dwordx4 a[96:99], v30, s[44:47], s62 offen offset:0 sc1\n"
      "v_mfma_f32_32x32x16_bf16 v[116:131], a[36:39], v[164:167], v[116:131]\n"
      "ds_read_b64_tr_b16 v[152:153], v77 offset:16384\n"
      "v_mfma_f32_32x32x16_bf16 v[132:147], a[36:39], v[168:171], v[132:147]\n"
      "ds_read_b64_tr_b16 v[154:155], v77 offset:17408\n"
      "buffer_load_dwordx4 a[100:103], v31, s[44:47], s62 offen offset:0 sc1\n"
      "s_waitcnt lgkmcnt(6)\n"
      "v_mfma_f32_32x32x16_bf16 v[84:99], a[40:43], v[172:175], v[84:99]\n"
      "ds_read_b64_tr_b16 v[156:157], v76 offset:18432\n"
      "s_waitcnt lgkmcnt(5)\n"
      "v_mfma_f32_32x32x16_bf16 v[100:115], a[40:43], v[176:179], v[100:115]\n"
      "ds_read_b64_tr_b16 v[158:159], v76 offset:19456\n"
      "buffer_load_dwordx4 a[104:107], v30, s[44:47], s62 offen offset:1024 sc1\n"
      "v_mfma_f32_32x32x16_bf16 v[116:131], a[44:47], v[172:175], v[116:131]\n"
      "ds_read_b64_tr_b16 v[160:161], v77 offset:18432\n"
      "v_mfma_f32_32x32x16_bf16 v[132:147], a[44:47], v[176:179], v[132:147]\n"
      "ds_read_b64_tr_b16 v[162:163], v77 offset:19456\n"
      "buffer_load_dwordx4 a[108:111], v31, s[44:47], s62 offen offset:1024 sc1\n"
      "s_waitcnt vmcnt(15) lgkmcnt(6)\n"
      "v_mfma_f32_32x32x16_bf16 v[84:99], a[48:51], v[148:151], v[84:99]\n"
      "ds_read_b64_tr_b16 v[164:165], v76 offset:20480\n"
      "s_waitcnt lgkmcnt(5)\n"
      "v_mfma_f32_32x32x16_bf16 v[100:115], a[48:51], v[152:155], v[100:115]\n"
      "ds_read_b64_tr_b16 v[166:167], v76 offset:21504\n"
      "s_add_u32 s62, s76, 16\n"
      "s_and_b32 s62, s62, 3\n"
      "s_lshl_b32 s62, s62, 11\n"
      "s_add_u32 s62, s62, s67\n"
      "buffer_load_dwordx4 a[240:243], v30, s[44:47], s62 offen offset:0 sc1\n"
      "s_waitcnt vmcnt(15)\n"
      "v_mfma_f32_32x32x16_bf16 v[116:131], a[52:55], v[148:151], v[116:131]\n"
      "ds_read_b64_tr_b16 v[168:169], v77 offset:20480\n"
      "v_mfma_f32_32x32x16_bf16 v[132:147], a[52:55], v[152:155], v[132:147]\n"
      "ds_read_b64_tr_b16 v[170:171], v77 offset:21504\n"
      "buffer_load_dwordx4 a[244:247], v31, s[44:47], s62 offen offset:0 sc1\n"
      "s_waitcnt vmcnt(15) lgkmcnt(6)\n"
      "v_mfma_f32_32x32x16_bf16 v[84:99], a[56:59], v[156:159], v[84:99]\n"
      "ds_read_b64_tr_b16 v[172:173], v76 offset:22528\n"
      "s_waitcnt lgkmcnt(5)\n"
      "v_mfma_f32_32x32x16_bf16 v[100:115], a[56:59], v[160:163], v[100:115]\n"
      "ds_read_b64_tr_b16 v[174:175], v76 offset:23552\n"
      "buffer_load_dwordx4 a[248:251], v30, s[44:47], s62 offen offset:1024 sc1\n"
      "s_waitcnt vmcnt(15)\n"
      "v_mfma_f32_32x32x16_bf16 v[116:131], a[60:63], v[156:159], v[116:131]\n"
      "ds_read_b64_tr_b16 v[176:177], v77 offset:22528\n"
      "v_mfma_f32_32x32x16_bf16 v[132:147], a[60:63], v[160:163], v[132:147]\n"
      "ds_read_b64_tr_b16 v[178:179], v77 offset:23552\n"
      "buffer_load_dwordx4 a[252:255], v31, s[44:47], s62 offen offset:1024 sc1\n"
      "buffer_load_dwordx4 v[244:247], v6, s[8:11], 0 offen\n"
      "buffer_load_dwordx4 v[248:251], v6, s[8:11], 0 offen offset:32\n"
      "buffer_load_ushort v5, v5, s[36:39], 0 offen\n"
      "s_waitcnt vmcnt(18) lgkmcnt(6)\n"
      "v_mfma_f32_32x32x16_bf16 v[84:99], a[64:67], v[164:167], v[84:99]\n"
      "ds_read_b64_tr_b16 v[148:149], v80 offset:24576\n"
      "s_waitcnt lgkmcnt(5)\n"
      "v_mfma_f32_32x32x16_bf16 v[100:115], a[64:67], v[168:171], v[100:115]\n"
      "ds_read_b64_tr_b16 v[150:151], v80 offset:25600\n"
      "s_add_u32 s62, s76, 17\n"
      "s_and_b32 s62, s62, 3\n"
      "s_lshl_b32 s62, s62, 11\n"
      "s_add_u32 s62, s62, s67\n"
      "buffer_load_dwordx4 a[0:3], v30, s[44:47], s62 offen offset:0 sc1\n"
      "s_waitcnt vmcnt(18)\n"
      "v_mfma_f32_32x32x16_bf16 v[116:131], a[68:71], v[164:167], v[116:131]\n"
      "ds_read_b64_tr_b16 v[152:153], v81 offset:24576\n"
      "v_mfma_f32_32x32x16_bf16 v[132:147], a[68:71], v[168:171], v[132:147]\n"
      "ds_read_b64_tr_b16 v[154:155], v81 offset:25600\n"
      "buffer_load_dwordx4 a[4:7], v31, s[44:47], s62 offen offset:0 sc1\n"
      "s_waitcnt vmcnt(18) lgkmcnt(6)\n"
      "v_mfma_f32_32x32x16_bf16 v[84:99], a[72:75], v[172:175], v[84:99]\n"
      "ds_read_b64_tr_b16 v[156:157], v80 offset:26624\n"
      "s_waitcnt lgkmcnt(5)\n"
      "v_mfma_f32_32x32x16_bf16 v[100:115], a[72:75], v[176:179], v[100:115]\n"
      "ds_read_b64_tr_b16 v[158:159], v80 offset:27648\n"
      "buffer_load_dwordx4 a[8:11], v30, s[44:47], s62 offen offset:1024 sc1\n"
      "s_waitcnt vmcnt(18)\n"
      "v_mfma_f32_32x32x16_bf16 v[116:131], a[76:79], v[172:175], v[116:131]\n"
      "ds_read_b64_tr_b16 v[160:161], v81 offset:26624\n"
      "v_mfma_f32_32x32x16_bf16 v[132:147], a[76:79], v[176:179], v[132:147]\n"
      "ds_read_b64_tr_b16 v[162:163], v81 offset:27648\n"
      "buffer_load_dwordx4 a[12:15], v31, s[44:47], s62 offen offset:1024 sc1\n"
      "s_waitcnt vmcnt(18) lgkmcnt(6)\n"
      "v_mfma_f32_32x32x16_bf16 v[84:99], v[148:151], a[80:83], v[84:99]\n"
      "ds_read_b64_tr_b16 v[164:165], v80 offset:0\n"
      "s_waitcnt vmcnt(17)\n"
      "v_mfma_f32_32x32x16_bf16 v[100:115], v[148:151], a[84:87], v[100:115]\n"
      "ds_read_b64_tr_b16 v[166:167], v80 offset:1024\n"
      "s_waitcnt lgkmcnt(6)\n"
      "v_mfma_f32_32x32x16_bf16 v[116:131], v[152:155], a[80:83], v[116:131]\n"
      "ds_read_b64_tr_b16 v[168:169], v81 offset:0\n"
      "v_mfma_f32_32x32x16_bf16 v[132:147], v[152:155], a[84:87], v[132:147]\n"
      "ds_read_b64_tr_b16 v[170:171], v81 offset:1024\n"
      "s_waitcnt vmcnt(16) lgkmcnt(6)\n"
      "v_mfma_f32_32x32x16_bf16 v[84:99], v[156:159], a[88:91], v[84:99]\n"
      "ds_read_b64_tr_b16 v[172:173], v80 offset:2048\n"
      "s_waitcnt vmcnt(15)\n"
      "v_mfma_f32_32x32x16_bf16 v[100:115], v[156:159], a[92:95], v[100:115]\n"
      "ds_read_b64_tr_b16 v[174:175], v80 offset:3072\n"
      "s_waitcnt lgkmcnt(6)\n"
      "v_mfma_f32_32x32x16_bf16 v[116:131], v[160:163], a[88:91], v[116:131]\n"
      "ds_read_b64_tr_b16 v[176:177], v81 offset:2048\n"
      "v_mfma_f32_32x32x16_bf16 v[132:147], v[160:163], a[92:95], v[132:147]\n"
      "ds_read_b64_tr_b16 v[178:179], v81 offset:3072\n"
      "s_waitcnt vmcnt(14) lgkmcnt(6)\n"
      "v_mfma_f32_32x32x16_bf16 v[84:99], v[164:167], a[96:99], v[84:99]\n"
      "ds_read_b64_tr_b16 v[148:149], v80 offset:4096\n"
      "s_waitcnt vmcnt(13)\n"
      "v_mfma_f32_32x32x16_bf16 v[100:115], v[164:167], a[100:103], v[100:115]\n"
      "ds_read_b64_tr_b16 v[150:151], v80 offset:5120\n"
      "s_waitcnt lgkmcnt(6)\n"
      "v_mfma_f32_32x32x16_bf16 v[116:131], v[168:171], a[96:99], v[116:131]\n"
      "ds_read_b64_tr_b16 v[152:153], v81 offset:4096\n"
      "v_mfma_f32_32x32x16_bf16 v[132:147], v[168:171], a[100:103], v[132:147]\n"
      "ds_read_b64_tr_b16 v[154:155], v81 offset:5120\n"
      "s_waitcnt vmcnt(12) lgkmcnt(6)\n"
      "v_mfma_f32_32x32x16_bf16 v[84:99], v[172:175], a[104:107], v[84:99]\n"
      "ds_read_b64_tr_b16 v[156:157], v80 offset:6144\n"
      "s_waitcnt vmcnt(11)\n"
      "v_mfma_f32_32x32x16_bf16 v[100:115], v[172:175], a[108:111], v[100:115]\n"
      "ds_read_b64_tr_b16 v[158:159], v80 offset:7168\n"
      "s_waitcnt lgkmcnt(6)\n"
      "v_mfma_f32_32x32x16_bf16 v[116:131], v[176:179], a[104:107], v[116:131]\n"
      "ds_read_b64_tr_b16 v[160:161], v81 offset:6144\n"
      "v_mfma_f32_32x32x16_bf16 v[132:147], v[176:179], a[108:111], v[132:147]\n"
      "ds_read_b64_tr_b16 v[162:163], v81 offset:7168\n"
      "s_waitcnt vmcnt(10) lgkmcnt(6)\n"
      "v_mfma_f32_32x32x16_bf16 v[84:99], v[148:151], a[240:243], v[84:99]\n"
      "ds_read_b64_tr_b16 v[164:165], v82 offset:0\n"
      "s_waitcnt vmcnt(9)\n"
      "v_mfma_f32_32x32x16_bf16 v[100:115], v[148:151], a[244:247], v[100:115]\n"
      "ds_read_b64_tr_b16 v[166:167], v82 offset:1024\n"
      "s_waitcnt lgkmcnt(6)\n"
      "v_mfma_f32_32x32x16_bf16 v[116:131], v[152:155], a[240:243], v[116:131]\n"
      "ds_read_b64_tr_b16 v[168:169], v83 offset:0\n"
      "v_mfma_f32_32x32x16_bf16 v[132:147], v[152:155], a[244:247], v[132:147]\n"
      "ds_read_b64_tr_b16 v[170:171], v83 offset:1024\n"
      "s_waitcnt vmcnt(8) lgkmcnt(6)\n"
      "v_mfma_f32_32x32x16_bf16 v[84:99], v[156:159], a[248:251], v[84:99]\n"
      "ds_read_b64_tr_b16 v[172:173], v82 offset:2048\n"
      "s_waitcnt vmcnt(7)\n"
      "v_mfma_f32_32x32x16_bf16 v[100:115], v[156:159], a[252:255], v[100:115]\n"
      "ds_read_b64_tr_b16 v[174:175], v82 offset:3072\n"
      "s_waitcnt lgkmcnt(6)\n"
      "v_mfma_f32_32x32x16_bf16 v[116:131], v[160:163], a[248:251], v[116:131]\n"
      "ds_read_b64_tr_b16 v[176:177], v83 offset:2048\n"
      "v_mfma_f32_32x32x16_bf16 v[132:147], v[160:163], a[252:255], v[132:147]\n"
      "ds_read_b64_tr_b16 v[178:179], v83 offset:3072\n"
      "s_waitcnt vmcnt(2) lgkmcnt(6)\n"
      "v_mfma_f32_32x32x16_bf16 v[100:115], v[164:167], a[4:7], v[100:115]\n"
      "v_add_u32 v20, s61, v36\n"
      "s_xor_b32 s52, s50, 1\n"
      "s_mul_i32 s52, s52, 28672\n"
      "s_add_u32 s52, s52, %3\n"
      "v_add_u32 v21, s52, v36\n"
      "s_xor_b32 s52, s50, 2\n"
      "s_mul_i32 s52, s52, 28672\n"
      "s_add_u32 s52, s52, %3\n"
      "v_add_u32 v22, s52, v36\n"
      "s_xor_b32 s52, s50, 3\n"
      "s_mul_i32 s52, s52, 28672\n"
      "s_add_u32 s52, s52, %3\n"
      "v_add_u32 v23, s52, v36\n"
      "s_waitcnt vmcnt(0) lgkmcnt(2)\n"
      "v_mfma_f32_32x32x16_bf16 v[100:115], v[172:175], a[12:15], v[100:115]\n"
      "v_lshlrev_b32 v148, 16, v244\n"
      "v_and_b32 v149, 0xffff0000, v244\n"
      "v_lshlrev_b32 v150, 16, v245\n"
      "v_and_b32 v151, 0xffff0000, v245\n"
      "v_lshlrev_b32 v152, 16, v246\n"
      "v_and_b32 v153, 0xffff0000, v246\n"
      "v_lshlrev_b32 v154, 16, v247\n"
      "v_and_b32 v155, 0xffff0000, v247\n"
      "v_mfma_f32_32x32x16_bf16 v[116:131], v[168:171], a[0:3], v[116:131]\n"
      "v_lshlrev_b32 v156, 16, v248\n"
      "v_and_b32 v157, 0xffff0000, v248\n"
      "v_lshlrev_b32 v158, 16, v249\n"
      "v_and_b32 v159, 0xffff0000, v249\n"
      "ds_write_b128 v20, v[100:103] offset:4096\n"
      "ds_write_b128 v20, v[104:107] offset:5120\n"
      "s_waitcnt lgkmcnt(2)\n"
      "v_mfma_f32_32x32x16_bf16 v[116:131], v[176:179], a[8:11], v[116:131]\n"
      "ds_write_b128 v20, v[108:111] offset:6144\n"
      "ds_write_b128 v20, v[112:115] offset:7168\n"
      "v_mfma_f32_32x32x16_bf16 v[132:147], v[168:171], a[4:7], v[132:147]\n"
      "v_lshlrev_b32 v160, 16, v250\n"
      "v_and_b32 v161, 0xffff0000, v250\n"
      "v_lshlrev_b32 v162, 16, v251\n"
      "v_and_b32 v163, 0xffff0000, v251\n"
      "v_lshlrev_b32 v244, 16, v5\n"
      "s_nop 3\n"
      "ds_write_b128 v20, v[116:119] offset:8192\n"
      "ds_write_b128 v20, v[120:123] offset:9216\n"
      "v_mfma_f32_32x32x16_bf16 v[132:147], v[176:179], a[12:15], v[132:147]\n"
      "ds_write_b128 v20, v[124:127] offset:10240\n"
      "ds_write_b128 v20, v[128:131] offset:11264\n"
      "v_mfma_f32_32x32x16_bf16 v[84:99], v[164:167], a[0:3], v[84:99]\n"
      "v_mfma_f32_32x32x16_bf16 v[84:99], v[172:175], a[8:11], v[84:99]\n"
      "s_nop 7\n"
      "ds_write_b128 v20, v[132:135] offset:12288\n"
      "ds_write_b128 v20, v[136:139] offset:13312\n"
      "ds_write_b128 v20, v[140:143] offset:14336\n"
      "ds_write_b128 v20, v[144:147] offset:15360\n"
      "s_waitcnt lgkmcnt(0)\n"
      "s_barrier\n"
      "ds_read_b128 v[100:103], v21 offset:4096\n"
      "ds_read_b128 v[116:119], v22 offset:8192\n"
      "ds_read_b128 v[132:135], v23 offset:12288\n"
      "ds_read_b128 v[104:107], v21 offset:5120\n"
      "ds_read_b128 v[120:123], v22 offset:9216\n"
      "ds_read_b128 v[136:139], v23 offset:13312\n"
      "ds_read_b128 v[108:111], v21 offset:6144\n"
      "ds_read_b128 v[124:127], v22 offset:10240\n"
      "ds_read_b128 v[140:143], v23 offset:14336\n"
      "ds_read_b128 v[112:115], v21 offset:7168\n"
      "ds_read_b128 v[128:131], v22 offset:11264\n"
      "ds_read_b128 v[144:147], v23 offset:15360\n"
      "v_pk_add_f32 v[148:149], v[148:149], v[244:245] op_sel_hi:[1,0]\n"
      "v_pk_add_f32 v[150:151], v[150:151], v[244:245] op_sel_hi:[1,0]\n"
      "v_pk_add_f32 v[152:153], v[152:153], v[244:245] op_sel_hi:[1,0]\n"
      "v_pk_add_f32 v[154:155], v[154:155], v[244:245] op_sel_hi:[1,0]\n"
      "v_pk_add_f32 v[156:157], v[156:157], v[244:245] op_sel_hi:[1,0]\n"
      "v_pk_add_f32 v[158:159], v[158:159], v[244:245] op_sel_hi:[1,0]\n"
      "v_pk_add_f32 v[160:161], v[160:161], v[244:245] op_sel_hi:[1,0]\n"
      "v_pk_add_f32 v[162:163], v[162:163], v[244:245] op_sel_hi:[1,0]\n"
      "s_waitcnt lgkmcnt(11)\n"
      "v_pk_add_f32 v[84:85], v[84:85], v[100:101]\n"
      "s_waitcnt lgkmcnt(9)\n"
      "v_pk_add_f32 v[116:117], v[116:117], v[132:133]\n"
      "v_pk_add_f32 v[84:85], v[84:85], v[116:117]\n"
      "v_pk_add_f32 v[86:87], v[86:87], v[102:103]\n"
      "v_pk_add_f32 v[118:119], v[118:119], v[134:135]\n"
      "v_pk_add_f32 v[86:87], v[86:87], v[118:119]\n"
      "s_waitcnt lgkmcnt(8)\n"
      "v_pk_add_f32 v[88:89], v[88:89], v[104:105]\n"
      "s_waitcnt lgkmcnt(6)\n"
      "v_pk_add_f32 v[120:121], v[120:121], v[136:137]\n"
      "v_pk_add_f32 v[88:89], v[88:89], v[120:121]\n"
      "v_pk_add_f32 v[90:91], v[90:91], v[106:107]\n"
      "v_pk_add_f32 v[122:123], v[122:123], v[138:139]\n"
      "v_pk_add_f32 v[90:91], v[90:91], v[122:123]\n"
      "v_permlane32_swap_b32 v84, v88\n"
      "v_permlane32_swap_b32 v85, v89\n"
      "v_permlane32_swap_b32 v86, v90\n"
      "v_permlane32_swap_b32 v87, v91\n"
      "v_pk_add_f32 v[84:85], v[84:85], v[148:149]\n"
      "v_pk_add_f32 v[86:87], v[86:87], v[150:151]\n"
      "v_pk_add_f32 v[88:89], v[88:89], v[152:153]\n"
      "v_pk_add_f32 v[90:91], v[90:91], v[154:155]\n"
      "v_cvt_pk_bf16_f32 v164, v84, v85\n"
      "v_cvt_pk_bf16_f32 v165, v86, v87\n"
      "v_cvt_pk_bf16_f32 v166, v88, v89\n"
      "v_cvt_pk_bf16_f32 v167, v90, v91\n"
      "buffer_store_dwordx4 v[164:167], v6, s[40:43], 0 offen offset:0\n"
      "s_waitcnt lgkmcnt(5)\n"
      "v_pk_add_f32 v[92:93], v[92:93], v[108:109]\n"
      "s_waitcnt lgkmcnt(3)\n"
      "v_pk_add_f32 v[124:125], v[124:125], v[140:141]\n"
      "v_pk_add_f32 v[92:93], v[92:93], v[124:125]\n"
      "v_pk_add_f32 v[94:95], v[94:95], v[110:111]\n"
      "v_pk_add_f32 v[126:127], v[126:127], v[142:143]\n"
      "v_pk_add_f32 v[94:95], v[94:95], v[126:127]\n"
      "s_waitcnt lgkmcnt(2)\n"
      "v_pk_add_f32 v[96:97], v[96:97], v[112:113]\n"
      "s_waitcnt lgkmcnt(0)\n"
      "v_pk_add_f32 v[128:129], v[128:129], v[144:145]\n"
      "v_pk_add_f32 v[96:97], v[96:97], v[128:129]\n"
      "v_pk_add_f32 v[98:99], v[98:99], v[114:115]\n"
      "v_pk_add_f32 v[130:131], v[130:131], v[146:147]\n"
      "v_pk_add_f32 v[98:99], v[98:99], v[130:131]\n"
      "v_permlane32_swap_b32 v92, v96\n"
      "v_permlane32_swap_b32 v93, v97\n"
      "v_permlane32_swap_b32 v94, v98\n"
      "v_permlane32_swap_b32 v95, v99\n"
      "v_pk_add_f32 v[92:93], v[92:93], v[156:157]\n"
      "v_pk_add_f32 v[94:95], v[94:95], v[158:159]\n"
      "v_pk_add_f32 v[96:97], v[96:97], v[160:161]\n"
      "v_pk_add_f32 v[98:99], v[98:99], v[162:163]\n"
      "v_cvt_pk_bf16_f32 v168, v92, v93\n"
      "v_cvt_pk_bf16_f32 v169, v94, v95\n"
      "v_cvt_pk_bf16_f32 v170, v96, v97\n"
      "v_cvt_pk_bf16_f32 v171, v98, v99\n"
      "buffer_store_dwordx4 v[168:171], v6, s[40:43], 0 offen offset:32\n"
      "s_nop 1\n"
      ".Lk_end:\n"
      "s_endpgm\n"
      ".Lk_slow:\n"
      "s_mov_b32 m0, s66\n"
      "s_nop 0\n"
      "buffer_load_dwordx4 v63, s[32:35], s63 offen offset:3072 lds\n"
      "s_add_u32 s63, s63, 131072\n"
      "s_cmp_eq_u32 s63, 1835008\n"
      "s_cselect_b32 s63, 0, s63\n"
      "ds_read_b64_tr_b16 a[128:129], v78 offset:4096\n"
      "ds_read_b64_tr_b16 a[130:131], v78 offset:5120\n"
      "ds_read_b64_tr_b16 a[132:133], v79 offset:4096\n"
      "ds_read_b64_tr_b16 a[134:135], v79 offset:5120\n"
      "ds_read_b64_tr_b16 a[136:137], v78 offset:6144\n"
      "ds_read_b64_tr_b16 a[138:139], v78 offset:7168\n"
      "ds_read_b64_tr_b16 a[140:141], v79 offset:6144\n"
      "ds_read_b64_tr_b16 a[142:143], v79 offset:7168\n"
      "s_add_u32 m0, s66, 4096\n"
      "s_waitcnt lgkmcnt(0)\n"
      "buffer_load_dwordx4 v60, s[32:35], s63 offen offset:0 lds\n"
      "buffer_load_dwordx4 v61, s[32:35], s63 offen offset:1024 lds\n"
      "buffer_load_dwordx4 v62, s[32:35], s63 offen offset:2048 lds\n"
      "buffer_load_dwordx4 v63, s[32:35], s63 offen offset:3072 lds\n"
      "s_add_u32 s63, s63, 131072\n"
      "s_cmp_eq_u32 s63, 1835008\n"
      "s_cselect_b32 s63, 0, s63\n"
      "ds_read_b64_tr_b16 a[144:145], v78 offset:8192\n"
      "ds_read_b64_tr_b16 a[146:147], v78 offset:9216\n"
      "ds_read_b64_tr_b16 a[148:149], v79 offset:8192\n"
      "ds_read_b64_tr_b16 a[150:151], v79 offset:9216\n"
      "ds_read_b64_tr_b16 a[152:153], v78 offset:10240\n"
      "ds_read_b64_tr_b16 a[154:155], v78 offset:11264\n"
      "ds_read_b64_tr_b16 a[156:157], v79 offset:10240\n"
      "ds_read_b64_tr_b16 a[158:159], v79 offset:11264\n"
      "s_add_u32 m0, s66, 8192\n"
      "s_waitcnt lgkmcnt(0)\n"
      "buffer_load_dwordx4 v60, s[32:35], s63 offen offset:0 lds\n"
      "buffer_load_dwordx4 v61, s[32:35], s63 offen offset:1024 lds\n"
      "buffer_load_dwordx4 v62, s[32:35], s63 offen offset:2048 lds\n"
      "buffer_load_dwordx4 v63, s[32:35], s63 offen offset:3072 lds\n"
      "s_add_u32 s63, s63, 131072\n"
      "s_cmp_eq_u32 s63, 1835008\n"
      "s_cselect_b32 s63, 0, s63\n"
      "ds_read_b64_tr_b16 a[160:161], v76 offset:8192\n"
      "ds_read_b64_tr_b16 a[162:163], v76 offset:9216\n"
      "ds_read_b64_tr_b16 a[164:165], v77 offset:8192\n"
      "ds_read_b64_tr_b16 a[166:167], v77 offset:9216\n"
      "s_waitcnt vmcnt(13)\n"
      "buffer_store_dwordx2 v[16:17], v37, s[44:47], s70 offen sc1\n"
      "s_lshl_b32 s72, s58, 7\n"
      "s_add_u32 s72, s72, 9109504\n"
      "v_and_b32 v68, 7, v1\n"
      "v_lshlrev_b32 v68, 3, v68\n"
      "buffer_load_dwordx2 v[66:67], v68, s[44:47], s72 offen sc1\n"
      "ds_read_b64_tr_b16 a[168:169], v76 offset:10240\n"
      "ds_read_b64_tr_b16 a[170:171], v76 offset:11264\n"
      "ds_read_b64_tr_b16 a[172:173], v77 offset:10240\n"
      "ds_read_b64_tr_b16 a[174:175], v77 offset:11264\n"
      "s_add_u32 m0, s61, 8192\n"
      "s_waitcnt lgkmcnt(0)\n"
      "buffer_load_dwordx4 v60, s[32:35], s63 offen offset:0 lds\n"
      "buffer_load_dwordx4 v61, s[32:35], s63 offen offset:1024 lds\n"
      "buffer_load_dwordx4 v62, s[32:35], s63 offen offset:2048 lds\n"
      "buffer_load_dwordx4 v63, s[32:35], s63 offen offset:3072 lds\n"
      "s_add_u32 s63, s63, 131072\n"
      "s_cmp_eq_u32 s63, 1835008\n"
      "s_cselect_b32 s63, 0, s63\n"
      "ds_read_b64_tr_b16 a[176:177], v76 offset:12288\n"
      "ds_read_b64_tr_b16 a[178:179], v76 offset:13312\n"
      "ds_read_b64_tr_b16 a[180:181], v77 offset:12288\n"
      "ds_read_b64_tr_b16 a[182:183], v77 offset:13312\n"
      "ds_read_b64_tr_b16 a[184:185], v76 offset:14336\n"
      "ds_read_b64_tr_b16 a[186:187], v76 offset:15360\n"
      "ds_read_b64_tr_b16 a[188:189], v77 offset:14336\n"
      "ds_read_b64_tr_b16 a[190:191], v77 offset:15360\n"
      "s_add_u32 m0, s61, 12288\n"
      "s_waitcnt lgkmcnt(0)\n"
      "buffer_load_dwordx4 v60, s[32:35], s63 offen offset:0 lds\n"
      "buffer_load_dwordx4 v61, s[32:35], s63 offen offset:1024 lds\n"
      "buffer_load_dwordx4 v62, s[32:35], s63 offen offset:2048 lds\n"
      "buffer_load_dwordx4 v63, s[32:35], s63 offen offset:3072 lds\n"
      "s_add_u32 s63, s63, 131072\n"
      "s_cmp_eq_u32 s63, 1835008\n"
      "s_cselect_b32 s63, 0, s63\n"
      "ds_read_b64_tr_b16 a[192:193], v76 offset:16384\n"
      "ds_read_b64_tr_b16 a[194:195], v76 offset:17408\n"
      "ds_read_b64_tr_b16 a[196:197], v77 offset:16384\n"
      "ds_read_b64_tr_b16 a[198:199], v77 offset:17408\n"
      "ds_read_b64_tr_b16 a[200:201], v76 offset:18432\n"
      "ds_read_b64_tr_b16 a[202:203], v76 offset:19456\n"
      "ds_read_b64_tr_b16 a[204:205], v77 offset:18432\n"
      "ds_read_b64_tr_b16 a[206:207], v77 offset:19456\n"
      "s_add_u32 m0, s61, 16384\n"
      "s_waitcnt lgkmcnt(0)\n"
      "buffer_load_dwordx4 v60, s[32:35], s63 offen offset:0 lds\n"
      "s_waitcnt vmcnt(9)\n"
      "v_cmp_ne_u64 vcc, v[66:67], v[16:17]\n"
      "s_cbranch_vccz .Lk_ok6\n"
      "s_mov_b32 s52, 20000\n"
      "s_nop 2\n"
      ".Lk_spin6:\n"
      "s_sleep 2\n"
      "buffer_load_dwordx2 v[66:67], v68, s[44:47], s72 offen sc1\n"
      "s_waitcnt vmcnt(0)\n"
      "v_cmp_ne_u64 vcc, v[66:67], v[16:17]\n"
      "s_cbranch_vccz .Lk_ok6\n"
      "s_sub_u32 s52, s52, 1\n"
      "s_cbranch_scc0 .Lk_spin6\n"
      "s_nop 3\n"
      ".Lk_ok6:\n"
      "s_lshl_b32 s52, s58, 12\n"
      "s_add_u32 s52, s52, 7634944\n"
      "buffer_load_dwordx4 v[180:183], v36, s[44:47], s52 offen offset:0 sc1\n"
      "buffer_load_dwordx4 v[184:187], v36, s[44:47], s52 offen offset:1024 sc1\n"
      "buffer_load_dwordx4 v[188:191], v36, s[44:47], s52 offen offset:2048 sc1\n"
      "s_add_u32 m0, s61, 16384\n"
      "buffer_load_dwordx4 v[192:195], v36, s[44:47], s52 offen offset:3072 sc1\n"
      "buffer_load_dwordx4 v61, s[32:35], s63 offen offset:1024 lds\n"
      "buffer_load_dwordx4 v62, s[32:35], s63 offen offset:2048 lds\n"
      "buffer_load_dwordx4 v63, s[32:35], s63 offen offset:3072 lds\n"
      "s_add_u32 s63, s63, 131072\n"
      "s_cmp_eq_u32 s63, 1835008\n"
      "s_cselect_b32 s63, 0, s63\n"
      "ds_read_b64_tr_b16 a[208:209], v76 offset:20480\n"
      "ds_read_b64_tr_b16 a[210:211], v76 offset:21504\n"
      "ds_read_b64_tr_b16 a[212:213], v77 offset:20480\n"
      "ds_read_b64_tr_b16 a[214:215], v77 offset:21504\n"
      "ds_read_b64_tr_b16 a[216:217], v76 offset:22528\n"
      "ds_read_b64_tr_b16 a[218:219], v76 offset:23552\n"
      "ds_read_b64_tr_b16 a[220:221], v77 offset:22528\n"
      "ds_read_b64_tr_b16 a[222:223], v77 offset:23552\n"
      "s_add_u32 m0, s61, 20480\n"
      "s_waitcnt lgkmcnt(0)\n"
      "buffer_load_dwordx4 v60, s[32:35], s63 offen offset:0 lds\n"
      "buffer_load_dwordx4 v61, s[32:35], s63 offen offset:1024 lds\n"
      "buffer_load_dwordx4 v62, s[32:35], s63 offen offset:2048 lds\n"
      "buffer_load_dwordx4 v63, s[32:35], s63 offen offset:3072 lds\n"
      "s_add_u32 s64, s76, 14\n"
      "s_and_b32 s64, s64, 3\n"
      "s_mul_i32 s64, s64, 25088\n"
      "s_add_u32 m0, s61, 24576\n"
      "s_nop 0\n"
      "buffer_load_dwordx4 v72, s[20:23], s64 offen offset:0 lds\n"
      "buffer_load_dwordx4 v73, s[20:23], s64 offen offset:1024 lds\n"
      "buffer_load_dwordx4 v74, s[20:23], s64 offen offset:2048 lds\n"
      "buffer_load_dwordx4 v75, s[20:23], s64 offen offset:3072 lds\n"
      "s_add_u32 s64, s76, 15\n"
      "s_and_b32 s64, s64, 3\n"
      "s_mul_i32 s64, s64, 25088\n"
      "s_mov_b32 m0, s61\n"
      "s_nop 0\n"
      "buffer_load_dwordx4 v72, s[20:23], s64 offen offset:0 lds\n"
      "s_waitcnt vmcnt(15)\n"
      "v_mfma_f32_32x32x16_bf16 v[196:211], v[180:183], v[228:231], 0\n"
      "s_waitcnt vmcnt(14)\n"
      "v_mfma_f32_32x32x16_bf16 v[196:211], v[184:187], v[232:235], v[196:211]\n"
      "s_waitcnt vmcnt(13)\n"
      "v_mfma_f32_32x32x16_bf16 v[196:211], v[188:191], v[236:239], v[196:211]\n"
      "s_waitcnt vmcnt(12)\n"
      "v_mfma_f32_32x32x16_bf16 v[196:211], v[192:195], v[240:243], v[196:211]\n"
      "s_lshl_b32 s52, s57, 2\n"
      "s_add_u32 s52, s52, s50\n"
      "s_lshl_b32 s52, s52, 4\n"
      "s_add_u32 s52, s52, s58\n"
      "s_lshl_b32 s52, s52, 11\n"
      "s_add_u32 s52, s52, 3440640\n"
      "v_cmp_eq_u32 vcc, 0, v11\n"
      "s_nop 4\n"
      "v_mov_b32 v2, v208\n"
      "v_mov_b32 v3, v208\n"
      "v_cndmask_b32 v208, 0, v208, vcc\n"
      "v_cndmask_b32 v209, 0, v209, vcc\n"
      "v_cndmask_b32 v210, 0, v210, vcc\n"
      "v_cndmask_b32 v211, 0, v211, vcc\n"
      "v_permlane32_swap_b32 v2, v3\n"
      "v_add_f32 v3, 0x358637bd, v3\n"
      "v_rcp_f32 v180, v3\n"
      "s_nop 0\n"
      "v_pk_mul_f32 v[196:197], v[196:197], v[180:181] op_sel_hi:[1,0]\n"
      "v_pk_mul_f32 v[198:199], v[198:199], v[180:181] op_sel_hi:[1,0]\n"
      "v_pk_mul_f32 v[200:201], v[200:201], v[180:181] op_sel_hi:[1,0]\n"
      "v_pk_mul_f32 v[202:203], v[202:203], v[180:181] op_sel_hi:[1,0]\n"
      "v_pk_mul_f32 v[204:205], v[204:205], v[180:181] op_sel_hi:[1,0]\n"
      "v_pk_mul_f32 v[206:207], v[206:207], v[180:181] op_sel_hi:[1,0]\n"
      "v_pk_mul_f32 v[208:209], v[208:209], v[180:181] op_sel_hi:[1,0]\n"
      "v_pk_mul_f32 v[210:211], v[210:211], v[180:181] op_sel_hi:[1,0]\n"
      "v_cvt_pk_bf16_f32 v184, v196, v197\n"
      "v_cvt_pk_bf16_f32 v185, v198, v199\n"
      "v_cvt_pk_bf16_f32 v186, v200, v201\n"
      "v_cvt_pk_bf16_f32 v187, v202, v203\n"
      "v_cvt_pk_bf16_f32 v188, v204, v205\n"
      "v_cvt_pk_bf16_f32 v189, v206, v207\n"
      "v_cvt_pk_bf16_f32 v190, v208, v209\n"
      "v_cvt_pk_bf16_f32 v191, v210, v211\n"
      "buffer_store_dwordx4 v[184:187], v36, s[44:47], s52 offen offset:0 sc1\n"
      "buffer_store_dwordx4 v[188:191], v36, s[44:47], s52 offen offset:1024 sc1\n"
      "buffer_load_dwordx4 v73, s[20:23], s64 offen offset:1024 lds\n"
      "s_lshl_b32 s73, s76, 2\n"
      "s_add_u32 s73, s73, s50\n"
      "s_mul_i32 s73, s73, 224\n"
      "s_add_u32 s73, s73, 9134080\n"
      "v_min_u32 v69, 27, v1\n"
      "v_mul_u32_u24 v69, 8, v69\n"
      "buffer_load_dwordx2 v[70:71], v69, s[44:47], s73 offen sc1\n"
      "buffer_load_dwordx4 v74, s[20:23], s64 offen offset:2048 lds\n"
      "buffer_load_dwordx4 v75, s[20:23], s64 offen offset:3072 lds\n"
      "s_add_u32 s64, s76, 16\n"
      "s_and_b32 s64, s64, 3\n"
      "s_mul_i32 s64, s64, 25088\n"
      "s_add_u32 m0, s61, 4096\n"
      "s_nop 0\n"
      "buffer_load_dwordx4 v72, s[20:23], s64 offen offset:0 lds\n"
      "buffer_load_dwordx4 v73, s[20:23], s64 offen offset:1024 lds\n"
      "buffer_load_dwordx4 v74, s[20:23], s64 offen offset:2048 lds\n"
      "buffer_load_dwordx4 v75, s[20:23], s64 offen offset:3072 lds\n"
      "ds_read_b64_tr_b16 a[224:225], v78 offset:0\n"
      "ds_read_b64_tr_b16 a[226:227], v78 offset:1024\n"
      "ds_read_b64_tr_b16 a[228:229], v79 offset:0\n"
      "ds_read_b64_tr_b16 a[230:231], v79 offset:1024\n"
      "ds_read_b64_tr_b16 a[232:233], v78 offset:2048\n"
      "ds_read_b64_tr_b16 a[234:235], v78 offset:3072\n"
      "ds_read_b64_tr_b16 a[236:237], v79 offset:2048\n"
      "ds_read_b64_tr_b16 a[238:239], v79 offset:3072\n"
      "s_add_u32 s64, s76, 17\n"
      "s_and_b32 s64, s64, 3\n"
      "s_mul_i32 s64, s64, 25088\n"
      "s_mov_b32 m0, s66\n"
      "s_waitcnt lgkmcnt(0)\n"
      "buffer_load_dwordx4 v72, s[20:23], s64 offen offset:0 lds\n"
      "buffer_load_dwordx4 v73, s[20:23], s64 offen offset:1024 lds\n"
      "buffer_load_dwordx4 v74, s[20:23], s64 offen offset:2048 lds\n"
      "buffer_load_dwordx4 v75, s[20:23], s64 offen offset:3072 lds\n"
      "s_waitcnt vmcnt(0)\n"
      "s_lshl_b32 s52, s57, 1\n"
      "s_lshr_b32 s53, s50, 1\n"
      "s_add_u32 s52, s52, s53\n"
      "s_lshl_b32 s52, s52, 2\n"
      "s_lshr_b32 s53, s58, 2\n"
      "s_add_u32 s52, s52, s53\n"
      "s_lshl_b32 s52, s52, 7\n"
      "s_and_b32 s53, s50, 1\n"
      "s_lshl_b32 s53, s53, 5\n"
      "s_add_u32 s52, s52, s53\n"
      "s_and_b32 s53, s58, 3\n"
      "s_lshl_b32 s53, s53, 3\n"
      "s_add_u32 s52, s52, s53\n"
      "s_add_u32 s52, s52, 9117696\n"
      "buffer_store_dwordx2 v[16:17], v37, s[44:47], s52 offen sc1\n"
      "s_branch .Lk_g2\n"
      ".Lk_help:\n"
      "s_mov_b64 s[12:13], s[24:25]\n"
      "s_and_b32 s14, s19, 7340032\n"
      "v_lshrrev_b32 v7, 2, v1\n"
      "v_mul_u32_u24 v7, 28, v7\n"
      "v_and_b32 v8, 3, v1\n"
      "v_add_lshl_u32 v7, v7, v8, 7\n"
      "s_mul_i32 s18, s48, 7\n"
      "s_lshr_b32 s18, s18, 1\n"
      "v_add_u32 v8, s18, v8\n"
      "v_mov_b32 v9, 0\n"
      "s_add_u32 s17, s16, 0\n"
      "s_lshr_b32 s18, s17, 3\n"
      "s_lshl_b32 vcc_lo, s48, 1\n"
      "s_xor_b32 s18, s18, vcc_lo\n"
      "v_xor_b32 v8, s18, v9\n"
      "v_mul_u32_u24 v8, 114688, v8\n"
      "v_add_u32 v8, v8, v7\n"
      "s_and_b32 s18, s17, 1\n"
      "s_bfe_u32 s17, s17, 0x20001\n"
      "s_mul_i32 s17, s17, 32\n"
      "s_add_u32 s17, s17, s18\n"
      "s_mul_i32 s17, s17, 57344\n"
      "s_mul_i32 s18, s48, 7\n"
      "s_lshr_b32 s18, s18, 1\n"
      "s_lshl_b32 s18, s18, 7\n"
      "s_add_u32 s17, s17, s18\n"
      "buffer_load_dword v13, v8, s[12:15], s17 offen\n"
      "v_subrev_u32 v52, 49152, v42\n"
      "v_subrev_u32 v53, 49152, v43\n"
      "v_subrev_u32 v54, 49152, v44\n"
      "v_subrev_u32 v55, 49152, v45\n"
      "s_waitcnt vmcnt(17) lgkmcnt(0)\n"
      "s_barrier\n"
      "ds_read_b128 v[152:155], v42 offset:0\n"
      "ds_read_b128 v[156:159], v42 offset:1024\n"
      "ds_read_b128 v[160:163], v42 offset:2048\n"
      "s_waitcnt lgkmcnt(2)\n"
      "v_mfma_f32_32x32x16_bf16 v[100:115], v[152:155], a[0:3], 0\n"
      "ds_read_b128 v[168:171], v43 offset:0\n"
      "v_mfma_f32_16x16x32_bf16 v[180:183], v[152:155], v[96:99], 0\n"
      "s_waitcnt lgkmcnt(2)\n"
      "v_mfma_f32_32x32x16_bf16 v[116:131], v[156:159], a[0:3], 0\n"
      "ds_read_b128 v[172:175], v43 offset:1024\n"
      "v_mfma_f32_16x16x32_bf16 v[184:187], v[156:159], v[96:99], 0\n"
      "s_waitcnt lgkmcnt(2)\n"
      "v_mfma_f32_32x32x16_bf16 v[132:147], a[0:3], v[160:163], 0\n"
      "ds_read_b128 v[176:179], v43 offset:2048\n"
      "v_mfma_f32_16x16x32_bf16 v[188:191], v[160:163], v[96:99], 0\n"
      "s_waitcnt vmcnt(15)\n"
      "ds_read_b128 a[64:67], v38 offset:16384\n"
      "ds_read_b128 a[68:71], v39 offset:16384\n"
      "ds_read_b128 a[72:75], v40 offset:16384\n"
      "ds_read_b128 a[76:79], v41 offset:16384\n"
      "s_waitcnt lgkmcnt(6)\n"
      "v_mfma_f32_32x32x16_bf16 v[100:115], v[168:171], a[4:7], v[100:115]\n"
      "ds_read_b128 v[152:155], v44 offset:0\n"
      "s_waitcnt lgkmcnt(6)\n"
      "v_mfma_f32_32x32x16_bf16 v[116:131], v[172:175], a[4:7], v[116:131]\n"
      "ds_read_b128 v[156:159], v44 offset:1024\n"
      "s_waitcnt lgkmcnt(6)\n"
      "v_mfma_f32_32x32x16_bf16 v[132:147], a[4:7], v[176:179], v[132:147]\n"
      "ds_read_b128 v[160:163], v44 offset:2048\n"
      "s_waitcnt vmcnt(11)\n"
      "ds_read_b128 a[80:83], v38 offset:20480\n"
      "ds_read_b128 a[84:87], v39 offset:20480\n"
      "ds_read_b128 a[88:91], v40 offset:20480\n"
      "ds_read_b128 a[92:95], v41 offset:20480\n"
      "s_waitcnt lgkmcnt(6)\n"
      "v_mfma_f32_32x32x16_bf16 v[100:115], v[152:155], a[8:11], v[100:115]\n"
      "ds_read_b128 v[168:171], v45 offset:0\n"
      "s_waitcnt lgkmcnt(6)\n"
      "v_mfma_f32_32x32x16_bf16 v[116:131], v[156:159], a[8:11], v[116:131]\n"
      "ds_read_b128 v[172:175], v45 offset:1024\n"
      "s_waitcnt lgkmcnt(6)\n"
      "v_mfma_f32_32x32x16_bf16 v[132:147], a[8:11], v[160:163], v[132:147]\n"
      "ds_read_b128 v[176:179], v45 offset:2048\n"
      "s_waitcnt vmcnt(7)\n"
      "ds_read_b128 a[96:99], v38 offset:24576\n"
      "ds_read_b128 a[100:103], v39 offset:24576\n"
      "ds_read_b128 a[104:107], v40 offset:24576\n"
      "ds_read_b128 a[108:111], v41 offset:24576\n"
      "s_waitcnt vmcnt(4) lgkmcnt(0)\n"
      "s_barrier\n"
      "v_mfma_f32_32x32x16_bf16 v[100:115], v[168:171], a[12:15], v[100:115]\n"
      "s_add_u32 m0, s56, -49152\n"
      "ds_read_b128 v[152:155], v42 offset:12288\n"
      "buffer_load_dwordx4 v36, s[44:47], s68 offen offset:0 sc1 lds\n"
      "v_mfma_f32_32x32x16_bf16 v[116:131], v[172:175], a[12:15], v[116:131]\n"
      "ds_read_b128 v[156:159], v42 offset:13312\n"
      "buffer_load_dwordx4 v36, s[44:47], s68 offen offset:1024 sc1 lds\n"
      "v_mfma_f32_32x32x16_bf16 v[132:147], a[12:15], v[176:179], v[132:147]\n"
      "ds_read_b128 v[160:163], v42 offset:14336\n"
      "buffer_load_dwordx4 v36, s[44:47], s68 offen offset:2048 sc1 lds\n"
      "s_add_u32 s68, s68, 12288\n"
      "s_cmp_eq_u32 s68, s70\n"
      "s_cselect_b32 s68, s67, s68\n"
      "s_waitcnt lgkmcnt(2)\n"
      "v_mfma_f32_32x32x16_bf16 v[100:115], v[152:155], a[16:19], v[100:115]\n"
      "s_add_u32 m0, s56, -36864\n"
      "ds_read_b128 v[168:171], v43 offset:12288\n"
      "buffer_load_dwordx4 v36, s[44:47], s68 offen offset:0 sc1 lds\n"
      "v_mfma_f32_16x16x32_bf16 v[180:183], v[152:155], v[96:99], v[180:183]\n"
      "s_waitcnt lgkmcnt(2)\n"
      "v_mfma_f32_32x32x16_bf16 v[116:131], v[156:159], a[16:19], v[116:131]\n"
      "ds_read_b128 v[172:175], v43 offset:13312\n"
      "buffer_load_dwordx4 v36, s[44:47], s68 offen offset:1024 sc1 lds\n"
      "v_mfma_f32_16x16x32_bf16 v[184:187], v[156:159], v[96:99], v[184:187]\n"
      "s_waitcnt lgkmcnt(2)\n"
      "v_mfma_f32_32x32x16_bf16 v[132:147], a[16:19], v[160:163], v[132:147]\n"
      "ds_read_b128 v[176:179], v43 offset:14336\n"
      "buffer_load_dwordx4 v36, s[44:47], s68 offen offset:2048 sc1 lds\n"
      "s_add_u32 s68, s68, 12288\n"
      "s_cmp_eq_u32 s68, s70\n"
      "s_cselect_b32 s68, s67, s68\n"
      "v_mfma_f32_16x16x32_bf16 v[188:191], v[160:163], v[96:99], v[188:191]\n"
      "s_waitcnt lgkmcnt(2)\n"
      "v_mfma_f32_32x32x16_bf16 v[100:115], v[168:171], a[20:23], v[100:115]\n"
      "s_add_u32 m0, s56, -24576\n"
      "ds_read_b128 v[152:155], v44 offset:12288\n"
      "buffer_load_dwordx4 v36, s[44:47], s68 offen offset:0 sc1 lds\n"
      "s_waitcnt lgkmcnt(2)\n"
      "v_mfma_f32_32x32x16_bf16 v[116:131], v[172:175], a[20:23], v[116:131]\n"
      "ds_read_b128 v[156:159], v44 offset:13312\n"
      "buffer_load_dwordx4 v36, s[44:47], s68 offen offset:1024 sc1 lds\n"
      "s_waitcnt lgkmcnt(2)\n"
      "v_mfma_f32_32x32x16_bf16 v[132:147], a[20:23], v[176:179], v[132:147]\n"
      "ds_read_b128 v[160:163], v44 offset:14336\n"
      "buffer_load_dwordx4 v36, s[44:47], s68 offen offset:2048 sc1 lds\n"
      "s_add_u32 s68, s68, 12288\n"
      "s_cmp_eq_u32 s68, s70\n"
      "s_cselect_b32 s68, s67, s68\n"
      "s_waitcnt lgkmcnt(2)\n"
      "v_mfma_f32_32x32x16_bf16 v[100:115], v[152:155], a[24:27], v[100:115]\n"
      "s_add_u32 m0, s56, -12288\n"
      "ds_read_b128 v[168:171], v45 offset:12288\n"
      "buffer_load_dwordx4 v36, s[44:47], s68 offen offset:0 sc1 lds\n"
      "s_waitcnt lgkmcnt(2)\n"
      "v_mfma_f32_32x32x16_bf16 v[116:131], v[156:159], a[24:27], v[116:131]\n"
      "ds_read_b128 v[172:175], v45 offset:13312\n"
      "buffer_load_dwordx4 v36, s[44:47], s68 offen offset:1024 sc1 lds\n"
      "s_waitcnt lgkmcnt(2)\n"
      "v_mfma_f32_32x32x16_bf16 v[132:147], a[24:27], v[160:163], v[132:147]\n"
      "ds_read_b128 v[176:179], v45 offset:14336\n"
      "buffer_load_dwordx4 v36, s[44:47], s68 offen offset:2048 sc1 lds\n"
      "buffer_load_dwordx2 v[66:67], v68, s[44:47], s72 offen sc1\n"
      "s_waitcnt vmcnt(14) lgkmcnt(0)\n"
      "s_barrier\n"
      "v_mfma_f32_32x32x16_bf16 v[100:115], v[168:171], a[28:31], v[100:115]\n"
      "ds_read_b128 v[152:155], v42 offset:24576\n"
      "v_mfma_f32_32x32x16_bf16 v[116:131], v[172:175], a[28:31], v[116:131]\n"
      "ds_read_b128 v[156:159], v42 offset:25600\n"
      "v_mfma_f32_32x32x16_bf16 v[132:147], a[28:31], v[176:179], v[132:147]\n"
      "ds_read_b128 v[160:163], v42 offset:26624\n"
      "s_add_u32 s17, s16, 28\n"
      "s_lshr_b32 s18, s17, 3\n"
      "s_lshl_b32 vcc_lo, s48, 1\n"
      "s_xor_b32 s18, s18, vcc_lo\n"
      "v_xor_b32 v8, s18, v9\n"
      "v_mul_u32_u24 v8, 114688, v8\n"
      "v_add_u32 v8, v8, v7\n"
      "s_and_b32 s18, s17, 1\n"
      "s_bfe_u32 s17, s17, 0x20001\n"
      "s_mul_i32 s17, s17, 32\n"
      "s_add_u32 s17, s17, s18\n"
      "s_mul_i32 s17, s17, 57344\n"
      "s_mul_i32 s18, s48, 7\n"
      "s_lshr_b32 s18, s18, 1\n"
      "s_lshl_b32 s18, s18, 7\n"
      "s_add_u32 s17, s17, s18\n"
      "buffer_load_dword v13, v8, s[12:15], s17 offen\n"
      "s_waitcnt lgkmcnt(2)\n"
      "v_mfma_f32_32x32x16_bf16 v[100:115], v[152:155], a[32:35], v[100:115]\n"
      "ds_read_b128 v[168:171], v43 offset:24576\n"
      "v_mfma_f32_16x16x32_bf16 v[180:183], v[152:155], v[96:99], v[180:183]\n"
      "s_waitcnt lgkmcnt(2)\n"
      "v_mfma_f32_32x32x16_bf16 v[116:131], v[156:159], a[32:35], v[116:131]\n"
      "ds_read_b128 v[172:175], v43 offset:25600\n"
      "v_mfma_f32_16x16x32_bf16 v[184:187], v[156:159], v[96:99], v[184:187]\n"
      "s_waitcnt lgkmcnt(2)\n"
      "v_mfma_f32_32x32x16_bf16 v[132:147], a[32:35], v[160:163], v[132:147]\n"
      "ds_read_b128 v[176:179], v43 offset:26624\n"
      "v_mfma_f32_16x16x32_bf16 v[188:191], v[160:163], v[96:99], v[188:191]\n"
      "s_waitcnt lgkmcnt(2)\n"
      "v_mfma_f32_32x32x16_bf16 v[100:115], v[168:171], a[36:39], v[100:115]\n"
      "ds_read_b128 v[152:155], v44 offset:24576\n"
      "s_waitcnt lgkmcnt(2)\n"
      "v_mfma_f32_32x32x16_bf16 v[116:131], v[172:175], a[36:39], v[116:131]\n"
      "ds_read_b128 v[156:159], v44 offset:25600\n"
      "s_waitcnt lgkmcnt(2)\n"
      "v_mfma_f32_32x32x16_bf16 v[132:147], a[36:39], v[176:179], v[132:147]\n"
      "ds_read_b128 v[160:163], v44 offset:26624\n"
      "s_waitcnt lgkmcnt(2)\n"
      "v_mfma_f32_32x32x16_bf16 v[100:115], v[152:155], a[40:43], v[100:115]\n"
      "ds_read_b128 v[168:171], v45 offset:24576\n"
      "s_waitcnt lgkmcnt(2)\n"
      "v_mfma_f32_32x32x16_bf16 v[116:131], v[156:159], a[40:43], v[116:131]\n"
      "ds_read_b128 v[172:175], v45 offset:25600\n"
      "s_waitcnt lgkmcnt(2)\n"
      "v_mfma_f32_32x32x16_bf16 v[132:147], a[40:43], v[160:163], v[132:147]\n"
      "ds_read_b128 v[176:179], v45 offset:26624\n"
      "s_waitcnt vmcnt(2) lgkmcnt(0)\n"
      "s_barrier\n"
      "v_mfma_f32_32x32x16_bf16 v[100:115], v[168:171], a[44:47], v[100:115]\n"
      "ds_read_b128 v[152:155], v52 offset:0\n"
      "v_mfma_f32_32x32x16_bf16 v[116:131], v[172:175], a[44:47], v[116:131]\n"
      "ds_read_b128 v[156:159], v52 offset:1024\n"
      "v_mfma_f32_32x32x16_bf16 v[132:147], a[44:47], v[176:179], v[132:147]\n"
      "ds_read_b128 v[160:163], v52 offset:2048\n"
      "s_waitcnt lgkmcnt(2)\n"
      "v_mfma_f32_32x32x16_bf16 v[100:115], v[152:155], a[48:51], v[100:115]\n"
      "ds_read_b128 v[168:171], v53 offset:0\n"
      "v_mfma_f32_16x16x32_bf16 v[180:183], v[152:155], v[96:99], v[180:183]\n"
      "s_waitcnt lgkmcnt(2)\n"
      "v_mfma_f32_32x32x16_bf16 v[116:131], v[156:159], a[48:51], v[116:131]\n"
      "ds_read_b128 v[172:175], v53 offset:1024\n"
      "v_mfma_f32_16x16x32_bf16 v[184:187], v[156:159], v[96:99], v[184:187]\n"
      "s_waitcnt lgkmcnt(2)\n"
      "v_mfma_f32_32x32x16_bf16 v[132:147], a[48:51], v[160:163], v[132:147]\n"
      "ds_read_b128 v[176:179], v53 offset:2048\n"
      "v_mfma_f32_16x16x32_bf16 v[188:191], v[160:163], v[96:99], v[188:191]\n"
      "s_waitcnt lgkmcnt(2)\n"
      "v_mfma_f32_32x32x16_bf16 v[100:115], v[168:171], a[52:55], v[100:115]\n"
      "ds_read_b128 v[152:155], v54 offset:0\n"
      "s_waitcnt lgkmcnt(2)\n"
      "v_mfma_f32_32x32x16_bf16 v[116:131], v[172:175], a[52:55], v[116:131]\n"
      "ds_read_b128 v[156:159], v54 offset:1024\n"
      "s_waitcnt lgkmcnt(2)\n"
      "v_mfma_f32_32x32x16_bf16 v[132:147], a[52:55], v[176:179], v[132:147]\n"
      "ds_read_b128 v[160:163], v54 offset:2048\n"
      "s_waitcnt lgkmcnt(2)\n"
      "v_mfma_f32_32x32x16_bf16 v[100:115], v[152:155], a[56:59], v[100:115]\n"
      "ds_read_b128 v[168:171], v55 offset:0\n"
      "s_waitcnt lgkmcnt(2)\n"
      "v_mfma_f32_32x32x16_bf16 v[116:131], v[156:159], a[56:59], v[116:131]\n"
      "ds_read_b128 v[172:175], v55 offset:1024\n"
      "s_waitcnt lgkmcnt(2)\n"
      "v_mfma_f32_32x32x16_bf16 v[132:147], a[56:59], v[160:163], v[132:147]\n"
      "ds_read_b128 v[176:179], v55 offset:2048\n"
      "s_waitcnt vmcnt(1)\n"
      "v_cmp_ne_u64 vcc, v[66:67], v[16:17]\n"
      "s_cbranch_vccz .Lk_ok7\n"
      "s_mov_b32 s52, 20000\n"
      "s_nop 6\n"
      ".Lk_spin7:\n"
      "s_sleep 2\n"
      "buffer_load_dwordx2 v[66:67], v68, s[44:47], s72 offen sc1\n"
      "s_waitcnt vmcnt(0)\n"
      "v_cmp_ne_u64 vcc, v[66:67], v[16:17]\n"
      "s_cbranch_vccz .Lk_ok7\n"
      "s_sub_u32 s52, s52, 1\n"
      "s_cbranch_scc0 .Lk_spin7\n"
      "s_nop 7\n"
      ".Lk_ok7:\n"
      "s_lshl_b32 s52, s57, 2\n"
      "s_add_u32 s52, s52, s50\n"
      "s_lshl_b32 s52, s52, 8\n"
      "s_add_u32 s52, s52, 9076736\n"
      "v_lshlrev_b32 v2, 3, v10\n"
      "buffer_load_dwordx2 v[14:15], v2, s[44:47], s52 offen sc1\n"
      "s_waitcnt lgkmcnt(2)\n"
      "v_mfma_f32_32x32x16_bf16 v[100:115], v[168:171], a[60:63], v[100:115]\n"
      "ds_read_b128 v[152:155], v52 offset:12288\n"
      "s_waitcnt lgkmcnt(2)\n"
      "v_mfma_f32_32x32x16_bf16 v[116:131], v[172:175], a[60:63], v[116:131]\n"
      "ds_read_b128 v[156:159], v52 offset:13312\n"
      "s_waitcnt lgkmcnt(2)\n"
      "v_mfma_f32_32x32x16_bf16 v[132:147], a[60:63], v[176:179], v[132:147]\n"
      "ds_read_b128 v[160:163], v52 offset:14336\n"
      "s_add_u32 s17, s16, 56\n"
      "s_lshr_b32 s18, s17, 3\n"
      "s_lshl_b32 vcc_lo, s48, 1\n"
      "s_xor_b32 s18, s18, vcc_lo\n"
      "v_xor_b32 v8, s18, v9\n"
      "v_mul_u32_u24 v8, 114688, v8\n"
      "v_add_u32 v8, v8, v7\n"
      "s_and_b32 s18, s17, 1\n"
      "s_bfe_u32 s17, s17, 0x20001\n"
      "s_mul_i32 s17, s17, 32\n"
      "s_add_u32 s17, s17, s18\n"
      "s_mul_i32 s17, s17, 57344\n"
      "s_mul_i32 s18, s48, 7\n"
      "s_lshr_b32 s18, s18, 1\n"
      "s_lshl_b32 s18, s18, 7\n"
      "s_add_u32 s17, s17, s18\n"
      "buffer_load_dword v13, v8, s[12:15], s17 offen\n"
      "s_waitcnt lgkmcnt(2)\n"
      "v_mfma_f32_32x32x16_bf16 v[100:115], v[152:155], a[64:67], v[100:115]\n"
      "ds_read_b128 v[168:171], v53 offset:12288\n"
      "v_mfma_f32_16x16x32_bf16 v[180:183], v[152:155], v[96:99], v[180:183]\n"
      "s_waitcnt lgkmcnt(2)\n"
      "v_mfma_f32_32x32x16_bf16 v[116:131], v[156:159], a[64:67], v[116:131]\n"
      "ds_read_b128 v[172:175], v53 offset:13312\n"
      "v_mfma_f32_16x16x32_bf16 v[184:187], v[156:159], v[96:99], v[184:187]\n"
      "s_waitcnt lgkmcnt(2)\n"
      "v_mfma_f32_32x32x16_bf16 v[132:147], a[64:67], v[160:163], v[132:147]\n"
      "ds_read_b128 v[176:179], v53 offset:14336\n"
      "v_mfma_f32_16x16x32_bf16 v[188:191], v[160:163], v[96:99], v[188:191]\n"
      "s_waitcnt lgkmcnt(2)\n"
      "v_mfma_f32_32x32x16_bf16 v[100:115], v[168:171], a[68:71], v[100:115]\n"
      "ds_read_b128 v[152:155], v54 offset:12288\n"
      "s_waitcnt lgkmcnt(2)\n"
      "v_mfma_f32_32x32x16_bf16 v[116:131], v[172:175], a[68:71], v[116:131]\n"
      "ds_read_b128 v[156:159], v54 offset:13312\n"
      "s_waitcnt lgkmcnt(2)\n"
      "v_mfma_f32_32x32x16_bf16 v[132:147], a[68:71], v[176:179], v[132:147]\n"
      "ds_read_b128 v[160:163], v54 offset:14336\n"
      "s_waitcnt lgkmcnt(2)\n"
      "v_mfma_f32_32x32x16_bf16 v[100:115], v[152:155], a[72:75], v[100:115]\n"
      "ds_read_b128 v[168:171], v55 offset:12288\n"
      "s_waitcnt lgkmcnt(2)\n"
      "v_mfma_f32_32x32x16_bf16 v[116:131], v[156:159], a[72:75], v[116:131]\n"
      "ds_read_b128 v[172:175], v55 offset:13312\n"
      "s_waitcnt lgkmcnt(2)\n"
      "v_mfma_f32_32x32x16_bf16 v[132:147], a[72:75], v[160:163], v[132:147]\n"
      "ds_read_b128 v[176:179], v55 offset:14336\n"
      "s_waitcnt lgkmcnt(2)\n"
      "v_mfma_f32_32x32x16_bf16 v[100:115], v[168:171], a[76:79], v[100:115]\n"
      "ds_read_b128 v[152:155], v52 offset:24576\n"
      "s_waitcnt lgkmcnt(2)\n"
      "v_mfma_f32_32x32x16_bf16 v[116:131], v[172:175], a[76:79], v[116:131]\n"
      "ds_read_b128 v[156:159], v52 offset:25600\n"
      "s_waitcnt lgkmcnt(2)\n"
      "v_mfma_f32_32x32x16_bf16 v[132:147], a[76:79], v[176:179], v[132:147]\n"
      "ds_read_b128 v[160:163], v52 offset:26624\n"
      "s_waitcnt lgkmcnt(2)\n"
      "v_mfma_f32_32x32x16_bf16 v[100:115], v[152:155], a[80:83], v[100:115]\n"
      "ds_read_b128 v[168:171], v53 offset:24576\n"
      "v_mfma_f32_16x16x32_bf16 v[180:183], v[152:155], v[96:99], v[180:183]\n"
      "s_waitcnt lgkmcnt(2)\n"
      "v_mfma_f32_32x32x16_bf16 v[116:131], v[156:159], a[80:83], v[116:131]\n"
      "ds_read_b128 v[172:175], v53 offset:25600\n"
      "v_mfma_f32_16x16x32_bf16 v[184:187], v[156:159], v[96:99], v[184:187]\n"
      "s_waitcnt lgkmcnt(2)\n"
      "v_mfma_f32_32x32x16_bf16 v[132:147], a[80:83], v[160:163], v[132:147]\n"
      "ds_read_b128 v[176:179], v53 offset:26624\n"
      "v_mfma_f32_16x16x32_bf16 v[188:191], v[160:163], v[96:99], v[188:191]\n"
      "s_waitcnt lgkmcnt(2)\n"
      "v_mfma_f32_32x32x16_bf16 v[100:115], v[168:171], a[84:87], v[100:115]\n"
      "ds_read_b128 v[152:155], v54 offset:24576\n"
      "s_waitcnt lgkmcnt(2)\n"
      "v_mfma_f32_32x32x16_bf16 v[116:131], v[172:175], a[84:87], v[116:131]\n"
      "ds_read_b128 v[156:159], v54 offset:25600\n"
      "s_waitcnt lgkmcnt(2)\n"
      "v_mfma_f32_32x32x16_bf16 v[132:147], a[84:87], v[176:179], v[132:147]\n"
      "ds_read_b128 v[160:163], v54 offset:26624\n"
      "s_waitcnt lgkmcnt(2)\n"
      "v_mfma_f32_32x32x16_bf16 v[100:115], v[152:155], a[88:91], v[100:115]\n"
      "ds_read_b128 v[168:171], v55 offset:24576\n"
      "s_waitcnt lgkmcnt(2)\n"
      "v_mfma_f32_32x32x16_bf16 v[116:131], v[156:159], a[88:91], v[116:131]\n"
      "ds_read_b128 v[172:175], v55 offset:25600\n"
      "s_waitcnt lgkmcnt(2)\n"
      "v_mfma_f32_32x32x16_bf16 v[132:147], a[88:91], v[160:163], v[132:147]\n"
      "ds_read_b128 v[176:179], v55 offset:26624\n"
      "s_waitcnt lgkmcnt(2)\n"
      "v_mfma_f32_32x32x16_bf16 v[100:115], v[168:171], a[92:95], v[100:115]\n"
      "ds_read_b128 v[152:155], v52 offset:36864\n"
      "s_waitcnt lgkmcnt(2)\n"
      "v_mfma_f32_32x32x16_bf16 v[116:131], v[172:175], a[92:95], v[116:131]\n"
      "ds_read_b128 v[156:159], v52 offset:37888\n"
      "s_waitcnt lgkmcnt(2)\n"
      "v_mfma_f32_32x32x16_bf16 v[132:147], a[92:95], v[176:179], v[132:147]\n"
      "ds_read_b128 v[160:163], v52 offset:38912\n"
      "s_add_u32 s17, s16, 84\n"
      "s_lshr_b32 s18, s17, 3\n"
      "s_lshl_b32 vcc_lo, s48, 1\n"
      "s_xor_b32 s18, s18, vcc_lo\n"
      "v_xor_b32 v8, s18, v9\n"
      "v_mul_u32_u24 v8, 114688, v8\n"
      "v_add_u32 v8, v8, v7\n"
      "s_and_b32 s18, s17, 1\n"
      "s_bfe_u32 s17, s17, 0x20001\n"
      "s_mul_i32 s17, s17, 32\n"
      "s_add_u32 s17, s17, s18\n"
      "s_mul_i32 s17, s17, 57344\n"
      "s_mul_i32 s18, s48, 7\n"
      "s_lshr_b32 s18, s18, 1\n"
      "s_lshl_b32 s18, s18, 7\n"
      "s_add_u32 s17, s17, s18\n"
      "buffer_load_dword v13, v8, s[12:15], s17 offen\n"
      "s_waitcnt lgkmcnt(2)\n"
      "v_mfma_f32_32x32x16_bf16 v[100:115], v[152:155], a[96:99], v[100:115]\n"
      "ds_read_b128 v[168:171], v53 offset:36864\n"
      "v_mfma_f32_16x16x32_bf16 v[180:183], v[152:155], v[96:99], v[180:183]\n"
      "s_waitcnt lgkmcnt(2)\n"
      "v_mfma_f32_32x32x16_bf16 v[116:131], v[156:159], a[96:99], v[116:131]\n"
      "ds_read_b128 v[172:175], v53 offset:37888\n"
      "v_mfma_f32_16x16x32_bf16 v[184:187], v[156:159], v[96:99], v[184:187]\n"
      "s_waitcnt lgkmcnt(2)\n"
      "v_mfma_f32_32x32x16_bf16 v[132:147], a[96:99], v[160:163], v[132:147]\n"
      "ds_read_b128 v[176:179], v53 offset:38912\n"
      "v_mfma_f32_16x16x32_bf16 v[188:191], v[160:163], v[96:99], v[188:191]\n"
      "s_waitcnt lgkmcnt(2)\n"
      "v_mfma_f32_32x32x16_bf16 v[100:115], v[168:171], a[100:103], v[100:115]\n"
      "ds_read_b128 v[152:155], v54 offset:36864\n"
      "s_waitcnt lgkmcnt(2)\n"
      "v_mfma_f32_32x32x16_bf16 v[116:131], v[172:175], a[100:103], v[116:131]\n"
      "ds_read_b128 v[156:159], v54 offset:37888\n"
      "s_waitcnt lgkmcnt(2)\n"
      "v_mfma_f32_32x32x16_bf16 v[132:147], a[100:103], v[176:179], v[132:147]\n"
      "ds_read_b128 v[160:163], v54 offset:38912\n"
      "ds_write_b128 v6, v[180:183] offset:0\n"
      "ds_write_b128 v6, v[184:187] offset:512\n"
      "ds_write_b128 v6, v[188:191] offset:1024\n"
      "s_waitcnt lgkmcnt(5)\n"
      "v_mfma_f32_32x32x16_bf16 v[100:115], v[152:155], a[104:107], v[100:115]\n"
      "ds_read_b128 v[168:171], v55 offset:36864\n"
      "s_waitcnt lgkmcnt(5)\n"
      "v_mfma_f32_32x32x16_bf16 v[116:131], v[156:159], a[104:107], v[116:131]\n"
      "ds_read_b128 v[172:175], v55 offset:37888\n"
      "s_waitcnt lgkmcnt(5)\n"
      "v_mfma_f32_32x32x16_bf16 v[132:147], a[104:107], v[160:163], v[132:147]\n"
      "ds_read_b128 v[176:179], v55 offset:38912\n"
      "s_waitcnt lgkmcnt(2)\n"
      "v_mfma_f32_32x32x16_bf16 v[100:115], v[168:171], a[108:111], v[100:115]\n"
      "s_waitcnt lgkmcnt(1)\n"
      "v_mfma_f32_32x32x16_bf16 v[116:131], v[172:175], a[108:111], v[116:131]\n"
      "s_waitcnt lgkmcnt(0)\n"
      "v_mfma_f32_32x32x16_bf16 v[132:147], a[108:111], v[176:179], v[132:147]\n"
      "s_barrier\n"
      "ds_read2_b32 v[148:149], v18 offset0:0 offset1:32\n"
      "ds_read2_b32 v[150:151], v18 offset0:64 offset1:96\n"
      "v_add_u32 v2, 1024, v18\n"
      "ds_read2_b32 v[152:153], v18 offset0:128 offset1:160\n"
      "ds_read2_b32 v[154:155], v18 offset0:192 offset1:224\n"
      "ds_read2_b32 v[156:157], v2 offset0:0 offset1:32\n"
      "ds_read2_b32 v[158:159], v2 offset0:64 offset1:96\n"
      "ds_read_b64_tr_b16 v[164:165], v21 offset:0\n"
      "ds_read_b64_tr_b16 v[166:167], v21 offset:512\n"
      "ds_read_b64_tr_b16 v[168:169], v21 offset:1024\n"
      "ds_read_b64_tr_b16 v[170:171], v21 offset:1536\n"
      "v_cmp_eq_u32 vcc, 0, v11\n"
      "s_waitcnt vmcnt(2)\n"
      "ds_write_b32 v19, v15\n"
      "v_cvt_pk_bf16_f32 v3, v14, v14\n"
      "v_lshlrev_b32 v3, 16, v3\n"
      "v_sub_f32 v3, v14, v3\n"
      "v_cvt_pk_bf16_f32 v3, v14, v3\n"
      "v_cndmask_b32 v92, 0, v3, vcc\n"
      "v_cndmask_b32 v93, 0, v3, vcc\n"
      "v_mul_f32 v32, 0x3fb8aa3b, v15\n"
      "v_mul_f32 v33, 0xbfb8aa3b, v15\n"
      "s_waitcnt lgkmcnt(9)\n"
      "v_pk_add_f32 v[148:149], v[148:149], v[150:151]\n"
      "s_waitcnt lgkmcnt(7)\n"
      "v_pk_add_f32 v[152:153], v[152:153], v[154:155]\n"
      "s_waitcnt lgkmcnt(5)\n"
      "v_pk_add_f32 v[156:157], v[156:157], v[158:159]\n"
      "v_add_f32 v148, v148, v149\n"
      "v_add_f32 v152, v152, v153\n"
      "v_add_f32 v156, v156, v157\n"
      "v_cvt_pk_bf16_f32 v84, v148, v148\n"
      "v_cvt_pk_bf16_f32 v86, v152, v152\n"
      "v_cvt_pk_bf16_f32 v88, v156, v156\n"
      "v_lshlrev_b32 v149, 16, v84\n"
      "v_lshlrev_b32 v153, 16, v86\n"
      "v_lshlrev_b32 v157, 16, v88\n"
      "v_sub_f32 v148, v148, v149\n"
      "v_sub_f32 v152, v152, v153\n"
      "v_sub_f32 v156, v156, v157\n"
      "v_cvt_pk_bf16_f32 v87, v152, v152\n"
      "v_cvt_pk_bf16_f32 v85, v148, v148\n"
      "v_cvt_pk_bf16_f32 v89, v156, v156\n"
      "s_nop 1\n"
      "v_mfma_f32_32x32x16_bf16 v[116:131], v[86:89], v[92:95], v[116:131]\n"
      "v_mfma_f32_32x32x16_bf16 v[100:115], v[84:87], v[92:95], v[100:115]\n"
      "v_mfma_f32_32x32x16_bf16 v[132:147], v[92:95], v[88:91], v[132:147]\n"
      "ds_read_b128 v[180:183], v20 offset:0\n"
      "ds_read_b128 v[184:187], v20 offset:32\n"
      "ds_read_b128 v[188:191], v20 offset:64\n"
      "ds_read_b128 v[192:195], v20 offset:96\n"
      "v_cmp_eq_u32 vcc, 28, v10\n"
      "s_nop 4\n"
      "v_cvt_pk_bf16_f32 v148, v116, v117\n"
      "v_cvt_pk_bf16_f32 v149, v118, v119\n"
      "v_cvt_pk_bf16_f32 v150, v120, v121\n"
      "v_cvt_pk_bf16_f32 v151, v122, v123\n"
      "v_cvt_pk_bf16_f32 v152, v124, v125\n"
      "v_cvt_pk_bf16_f32 v153, v126, v127\n"
      "v_cvt_pk_bf16_f32 v154, v128, v129\n"
      "v_cvt_pk_bf16_f32 v155, v130, v131\n"
      "s_waitcnt lgkmcnt(7)\n"
      "v_mfma_f32_32x32x16_bf16 v[196:211], v[164:167], v[148:151], 0\n"
      "s_waitcnt lgkmcnt(5)\n"
      "v_mfma_f32_32x32x16_bf16 v[196:211], v[168:171], v[152:155], v[196:211]\n"
      "v_cvt_pk_bf16_f32 v156, v100, v101\n"
      "v_cvt_pk_bf16_f32 v157, v102, v103\n"
      "v_cvt_pk_bf16_f32 v158, v104, v105\n"
      "v_cvt_pk_bf16_f32 v159, v106, v107\n"
      "v_cvt_pk_bf16_f32 v160, v108, v109\n"
      "v_cvt_pk_bf16_f32 v161, v110, v111\n"
      "v_cvt_pk_bf16_f32 v162, v112, v113\n"
      "v_cvt_pk_bf16_f32 v163, v114, v115\n"
      "v_mfma_f32_32x32x16_bf16 v[212:227], v[164:167], v[156:159], 0\n"
      "s_nop 0\n"
      "v_mfma_f32_32x32x16_bf16 v[212:227], v[168:171], v[160:163], v[212:227]\n"
      "s_waitcnt lgkmcnt(3)\n"
      "v_pk_mul_f32 v[132:133], v[132:133], v[180:181]\n"
      "v_pk_mul_f32 v[134:135], v[134:135], v[182:183]\n"
      "s_waitcnt lgkmcnt(2)\n"
      "v_pk_mul_f32 v[136:137], v[136:137], v[184:185]\n"
      "v_pk_mul_f32 v[138:139], v[138:139], v[186:187]\n"
      "s_waitcnt lgkmcnt(1)\n"
      "v_pk_mul_f32 v[140:141], v[140:141], v[188:189]\n"
      "v_pk_mul_f32 v[142:143], v[142:143], v[190:191]\n"
      "s_waitcnt lgkmcnt(0)\n"
      "v_pk_mul_f32 v[144:145], v[144:145], v[192:193]\n"
      "v_pk_mul_f32 v[146:147], v[146:147], v[194:195]\n"
      "v_cvt_pk_bf16_f32 v172, v132, v133\n"
      "v_cvt_pk_bf16_f32 v173, v134, v135\n"
      "v_cvt_pk_bf16_f32 v174, v136, v137\n"
      "v_cvt_pk_bf16_f32 v175, v138, v139\n"
      "v_cvt_pk_bf16_f32 v176, v140, v141\n"
      "v_cvt_pk_bf16_f32 v177, v142, v143\n"
      "v_cvt_pk_bf16_f32 v178, v144, v145\n"
      "v_cvt_pk_bf16_f32 v179, v146, v147\n"
      "v_cndmask_b32 v172, v172, v28, vcc\n"
      "v_cndmask_b32 v173, v173, v28, vcc\n"
      "v_cndmask_b32 v174, v174, v28, vcc\n"
      "v_cndmask_b32 v175, v175, v28, vcc\n"
      "v_cndmask_b32 v176, v176, v28, vcc\n"
      "v_cndmask_b32 v177, v177, v28, vcc\n"
      "v_cndmask_b32 v178, v178, v28, vcc\n"
      "v_cndmask_b32 v179, v179, v28, vcc\n"
      "s_add_u32 s17, s16, 112\n"
      "s_lshr_b32 s18, s17, 3\n"
      "s_lshl_b32 vcc_lo, s48, 1\n"
      "s_xor_b32 s18, s18, vcc_lo\n"
      "v_xor_b32 v8, s18, v9\n"
      "v_mul_u32_u24 v8, 114688, v8\n"
      "v_add_u32 v8, v8, v7\n"
      "s_and_b32 s18, s17, 1\n"
      "s_bfe_u32 s17, s17, 0x20001\n"
      "s_mul_i32 s17, s17, 32\n"
      "s_add_u32 s17, s17, s18\n"
      "s_mul_i32 s17, s17, 57344\n"
      "s_mul_i32 s18, s48, 7\n"
      "s_lshr_b32 s18, s18, 1\n"
      "s_lshl_b32 s18, s18, 7\n"
      "s_add_u32 s17, s17, s18\n"
      "buffer_load_dword v13, v8, s[12:15], s17 offen\n"
      "v_max3_f32 v14, v196, v197, v198\n"
      "v_min3_f32 v15, v196, v197, v198\n"
      "v_max3_f32 v14, v14, v199, v200\n"
      "v_min3_f32 v15, v15, v199, v200\n"
      "v_max3_f32 v14, v14, v201, v202\n"
      "v_min3_f32 v15, v15, v201, v202\n"
      "v_max3_f32 v14, v14, v203, v204\n"
      "v_min3_f32 v15, v15, v203, v204\n"
      "v_max3_f32 v14, v14, v205, v206\n"
      "v_min3_f32 v15, v15, v205, v206\n"
      "v_max3_f32 v14, v14, v207, v208\n"
      "v_min3_f32 v15, v15, v207, v208\n"
      "v_max3_f32 v14, v14, v209, v210\n"
      "v_min3_f32 v15, v15, v209, v210\n"
      "v_max_f32 v14, v14, v211\n"
      "v_min_f32 v15, v15, v211\n"
      "v_mov_b32 v18, v14\n"
      "v_mov_b32 v19, v15\n"
      "s_nop 0\n"
      "v_permlane32_swap_b32 v14, v18\n"
      "v_permlane32_swap_b32 v15, v19\n"
      "v_max_f32 v14, v14, v18\n"
      "v_min_f32 v15, v15, v19\n"
      "v_pk_mul_f32 v[14:15], v[14:15], v[32:33] op_sel:[0,1] op_sel_hi:[1,0]\n"
      "v_add_f32 v18, v14, v46\n"
      "v_add_f32 v19, v15, v46\n"
      "v_pk_fma_f32 v[116:117], v[196:197], v[32:33], v[14:15] op_sel_hi:[1,0,0]\n"
      "v_pk_fma_f32 v[100:101], v[196:197], v[32:33], v[14:15] op_sel:[0,1,1] op_sel_hi:[1,1,1]\n"
      "v_pk_fma_f32 v[118:119], v[198:199], v[32:33], v[14:15] op_sel_hi:[1,0,0]\n"
      "v_pk_fma_f32 v[102:103], v[198:199], v[32:33], v[14:15] op_sel:[0,1,1] op_sel_hi:[1,1,1]\n"
      "v_pk_fma_f32 v[120:121], v[200:201], v[32:33], v[14:15] op_sel_hi:[1,0,0]\n"
      "v_pk_fma_f32 v[104:105], v[200:201], v[32:33], v[14:15] op_sel:[0,1,1] op_sel_hi:[1,1,1]\n"
      "v_pk_fma_f32 v[122:123], v[202:203], v[32:33], v[14:15] op_sel_hi:[1,0,0]\n"
      "v_pk_fma_f32 v[106:107], v[202:203], v[32:33], v[14:15] op_sel:[0,1,1] op_sel_hi:[1,1,1]\n"
      "v_pk_fma_f32 v[124:125], v[204:205], v[32:33], v[14:15] op_sel_hi:[1,0,0]\n"
      "v_pk_fma_f32 v[108:109], v[204:205], v[32:33], v[14:15] op_sel:[0,1,1] op_sel_hi:[1,1,1]\n"
      "v_pk_fma_f32 v[126:127], v[206:207], v[32:33], v[14:15] op_sel_hi:[1,0,0]\n"
      "v_pk_fma_f32 v[110:111], v[206:207], v[32:33], v[14:15] op_sel:[0,1,1] op_sel_hi:[1,1,1]\n"
      "v_pk_fma_f32 v[128:129], v[208:209], v[32:33], v[18:19] op_sel_hi:[1,0,0]\n"
      "v_pk_fma_f32 v[112:113], v[208:209], v[32:33], v[18:19] op_sel:[0,1,1] op_sel_hi:[1,1,1]\n"
      "v_pk_fma_f32 v[130:131], v[210:211], v[32:33], v[18:19] op_sel_hi:[1,0,0]\n"
      "v_pk_fma_f32 v[114:115], v[210:211], v[32:33], v[18:19] op_sel:[0,1,1] op_sel_hi:[1,1,1]\n"
      "v_exp_f32 v116, v116\n"
      "v_exp_f32 v117, v117\n"
      "v_exp_f32 v100, v100\n"
      "v_exp_f32 v101, v101\n"
      "v_exp_f32 v118, v118\n"
      "v_exp_f32 v119, v119\n"
      "v_exp_f32 v102, v102\n"
      "v_exp_f32 v103, v103\n"
      "v_exp_f32 v120, v120\n"
      "v_exp_f32 v121, v121\n"
      "v_exp_f32 v104, v104\n"
      "v_exp_f32 v105, v105\n"
      "v_pk_add_f32 v[38:39], v[116:117], v[118:119]\n"
      "v_pk_add_f32 v[40:41], v[100:101], v[102:103]\n"
      "v_exp_f32 v122, v122\n"
      "v_exp_f32 v123, v123\n"
      "v_exp_f32 v106, v106\n"
      "v_exp_f32 v107, v107\n"
      "v_pk_add_f32 v[38:39], v[38:39], v[120:121]\n"
      "v_pk_add_f32 v[40:41], v[40:41], v[104:105]\n"
      "v_exp_f32 v124, v124\n"
      "v_exp_f32 v125, v125\n"
      "v_exp_f32 v108, v108\n"
      "v_exp_f32 v109, v109\n"
      "v_pk_add_f32 v[38:39], v[38:39], v[122:123]\n"
      "v_pk_add_f32 v[40:41], v[40:41], v[106:107]\n"
      "v_exp_f32 v126, v126\n"
      "v_exp_f32 v127, v127\n"
      "v_exp_f32 v110, v110\n"
      "v_exp_f32 v111, v111\n"
      "v_pk_add_f32 v[38:39], v[38:39], v[124:125]\n"
      "v_pk_add_f32 v[40:41], v[40:41], v[108:109]\n"
      "v_exp_f32 v128, v128\n"
      "v_exp_f32 v129, v129\n"
      "v_exp_f32 v112, v112\n"
      "v_exp_f32 v113, v113\n"
      "v_pk_add_f32 v[38:39], v[38:39], v[126:127]\n"
      "v_pk_add_f32 v[40:41], v[40:41], v[110:111]\n"
      "v_exp_f32 v130, v130\n"
      "v_exp_f32 v131, v131\n"
      "v_exp_f32 v114, v114\n"
      "v_exp_f32 v115, v115\n"
      "v_pk_add_f32 v[38:39], v[38:39], v[128:129]\n"
      "v_pk_add_f32 v[40:41], v[40:41], v[112:113]\n"
      "v_pk_add_f32 v[38:39], v[38:39], v[130:131]\n"
      "v_pk_add_f32 v[40:41], v[40:41], v[114:115]\n"
      "v_add_f32 v38, v38, v39\n"
      "v_add_f32 v39, v40, v41\n"
      "s_nop 1\n"
      "v_permlane32_swap_b32 v38, v39\n"
      "v_add_f32 v38, v38, v39\n"
      "v_rcp_f32 v38, v38\n"
      "s_nop 0\n"
      "v_mov_b32 v39, v38\n"
      "s_nop 1\n"
      "v_permlane32_swap_b32 v38, v39\n"
      "v_pk_mul_f32 v[116:117], v[116:117], v[38:39] op_sel_hi:[1,0]\n"
      "v_pk_mul_f32 v[100:101], v[100:101], v[38:39] op_sel:[0,1] op_sel_hi:[1,1]\n"
      "v_pk_mul_f32 v[118:119], v[118:119], v[38:39] op_sel_hi:[1,0]\n"
      "v_pk_mul_f32 v[102:103], v[102:103], v[38:39] op_sel:[0,1] op_sel_hi:[1,1]\n"
      "v_pk_mul_f32 v[120:121], v[120:121], v[38:39] op_sel_hi:[1,0]\n"
      "v_pk_mul_f32 v[104:105], v[104:105], v[38:39] op_sel:[0,1] op_sel_hi:[1,1]\n"
      "v_pk_mul_f32 v[122:123], v[122:123], v[38:39] op_sel_hi:[1,0]\n"
      "v_pk_mul_f32 v[106:107], v[106:107], v[38:39] op_sel:[0,1] op_sel_hi:[1,1]\n"
      "v_pk_mul_f32 v[124:125], v[124:125], v[38:39] op_sel_hi:[1,0]\n"
      "v_pk_mul_f32 v[108:109], v[108:109], v[38:39] op_sel:[0,1] op_sel_hi:[1,1]\n"
      "v_pk_mul_f32 v[126:127], v[126:127], v[38:39] op_sel_hi:[1,0]\n"
      "v_pk_mul_f32 v[110:111], v[110:111], v[38:39] op_sel:[0,1] op_sel_hi:[1,1]\n"
      "v_pk_mul_f32 v[128:129], v[128:129], v[38:39] op_sel_hi:[1,0]\n"
      "v_pk_mul_f32 v[112:113], v[112:113], v[38:39] op_sel:[0,1] op_sel_hi:[1,1]\n"
      "v_pk_mul_f32 v[130:131], v[130:131], v[38:39] op_sel_hi:[1,0]\n"
      "v_pk_mul_f32 v[114:115], v[114:115], v[38:39] op_sel:[0,1] op_sel_hi:[1,1]\n"
      "v_cvt_pk_bf16_f32 v180, v116, v117\n"
      "v_cvt_pk_bf16_f32 v188, v100, v101\n"
      "v_cvt_pk_bf16_f32 v181, v118, v119\n"
      "v_cvt_pk_bf16_f32 v189, v102, v103\n"
      "v_cvt_pk_bf16_f32 v182, v120, v121\n"
      "v_cvt_pk_bf16_f32 v190, v104, v105\n"
      "v_cvt_pk_bf16_f32 v183, v122, v123\n"
      "v_cvt_pk_bf16_f32 v191, v106, v107\n"
      "v_cvt_pk_bf16_f32 v184, v124, v125\n"
      "v_cvt_pk_bf16_f32 v192, v108, v109\n"
      "v_cvt_pk_bf16_f32 v185, v126, v127\n"
      "v_cvt_pk_bf16_f32 v193, v110, v111\n"
      "v_cvt_pk_bf16_f32 v186, v128, v129\n"
      "v_cvt_pk_bf16_f32 v194, v112, v113\n"
      "v_cvt_pk_bf16_f32 v187, v130, v131\n"
      "v_cvt_pk_bf16_f32 v195, v114, v115\n"
      "s_mov_b64 s[12:13], s[32:33]\n"
      "s_and_b32 s14, s19, 7340032\n"
      "v_lshrrev_b32 v7, 2, v1\n"
      "v_mul_u32_u24 v7, 32, v7\n"
      "v_and_b32 v8, 3, v1\n"
      "v_add_lshl_u32 v7, v7, v8, 7\n"
      "v_mov_b32 v9, 0\n"
      "s_add_u32 s17, s16, 0\n"
      "s_lshr_b32 s18, s17, 3\n"
      "s_min_u32 vcc_lo, s48, 6\n"
      "s_lshl1_add_u32 s18, vcc_lo, s18\n"
      "s_sub_u32 vcc_lo, s18, 14\n"
      "s_cselect_b32 s18, s18, vcc_lo\n"
      "v_add_u32 v8, s18, v9\n"
      "v_mul_u32_u24 v8, 131072, v8\n"
      "v_add_u32 v8, v8, v7\n"
      "s_and_b32 s18, s17, 1\n"
      "s_bfe_u32 s17, s17, 0x20001\n"
      "s_mul_i32 s17, s17, 28\n"
      "s_add_u32 s17, s17, s18\n"
      "s_mul_i32 s17, s17, 65536\n"
      "s_lshl_b32 s18, s48, 9\n"
      "s_add_u32 s17, s17, s18\n"
      "buffer_load_dword v13, v8, s[12:15], s17 offen\n"
      "ds_write_b64 v22, v[180:181] offset:0\n"
      "ds_write_b64 v22, v[188:189] offset:64\n"
      "ds_write_b64 v22, v[182:183] offset:16\n"
      "ds_write_b64 v22, v[190:191] offset:80\n"
      "ds_write_b64 v22, v[184:185] offset:32\n"
      "ds_write_b64 v22, v[192:193] offset:96\n"
      "ds_write_b64 v22, v[186:187] offset:48\n"
      "ds_write_b64 v22, v[194:195] offset:112\n"
      "ds_read_b64_tr_b16 v[148:149], v23 offset:0\n"
      "ds_read_b64_tr_b16 v[150:151], v23 offset:1088\n"
      "ds_read_b64_tr_b16 v[152:153], v23 offset:2176\n"
      "ds_read_b64_tr_b16 v[154:155], v23 offset:3264\n"
      "ds_read_b64_tr_b16 v[156:157], v23 offset:64\n"
      "ds_read_b64_tr_b16 v[158:159], v23 offset:1152\n"
      "ds_read_b64_tr_b16 v[160:161], v23 offset:2240\n"
      "ds_read_b64_tr_b16 v[162:163], v23 offset:3328\n"
      "s_waitcnt lgkmcnt(6)\n"
      "v_mfma_f32_32x32x16_bf16 v[132:147], v[148:151], v[172:175], 0\n"
      "s_waitcnt lgkmcnt(2)\n"
      "v_mfma_f32_32x32x16_bf16 v[196:211], v[156:159], v[172:175], 0\n"
      "v_mfma_f32_32x32x16_bf16 v[132:147], v[152:155], v[176:179], v[132:147]\n"
      "s_waitcnt lgkmcnt(0)\n"
      "v_mfma_f32_32x32x16_bf16 v[196:211], v[160:163], v[176:179], v[196:211]\n"
      "s_nop 9\n"
      "ds_write_b128 v24, v[132:135] offset:0\n"
      "ds_write_b128 v24, v[136:139] offset:1024\n"
      "ds_write_b128 v24, v[140:143] offset:2048\n"
      "ds_write_b128 v24, v[144:147] offset:3072\n"
      "ds_write_b128 v24, v[196:199] offset:4096\n"
      "ds_write_b128 v24, v[200:203] offset:5120\n"
      "ds_write_b128 v24, v[204:207] offset:6144\n"
      "ds_write_b128 v24, v[208:211] offset:7168\n"
      "s_waitcnt lgkmcnt(0)\n"
      "s_barrier\n"
      "ds_read_b128 v[116:119], v25 offset:0\n"
      "ds_read_b128 v[120:123], v25 offset:8192\n"
      "ds_read_b128 v[124:127], v25 offset:16384\n"
      "ds_read_b128 v[128:131], v25 offset:24576\n"
      "ds_read_b128 v[100:103], v25 offset:1024\n"
      "ds_read_b128 v[104:107], v25 offset:9216\n"
      "ds_read_b128 v[108:111], v25 offset:17408\n"
      "ds_read_b128 v[112:115], v25 offset:25600\n"
      "s_lshl_b32 s52, s58, 4\n"
      "s_add_u32 s52, s52, s57\n"
      "s_lshl_b32 s52, s52, 2\n"
      "s_add_u32 s52, s52, s50\n"
      "s_lshl_b32 s52, s52, 10\n"
      "s_add_u32 s52, s52, 5537792\n"
      "s_waitcnt lgkmcnt(6)\n"
      "v_pk_add_f32 v[116:117], v[116:117], v[120:121]\n"
      "s_waitcnt lgkmcnt(4)\n"
      "v_pk_add_f32 v[124:125], v[124:125], v[128:129]\n"
      "v_pk_add_f32 v[118:119], v[118:119], v[122:123]\n"
      "v_pk_add_f32 v[126:127], v[126:127], v[130:131]\n"
      "v_pk_add_f32 v[116:117], v[116:117], v[124:125]\n"
      "v_pk_add_f32 v[118:119], v[118:119], v[126:127]\n"
      "v_cvt_pk_bf16_f32 v180, v116, v117\n"
      "v_cvt_pk_bf16_f32 v181, v118, v119\n"
      "s_waitcnt lgkmcnt(2)\n"
      "v_pk_add_f32 v[100:101], v[100:101], v[104:105]\n"
      "s_waitcnt lgkmcnt(0)\n"
      "v_pk_add_f32 v[108:109], v[108:109], v[112:113]\n"
      "v_pk_add_f32 v[102:103], v[102:103], v[106:107]\n"
      "v_pk_add_f32 v[110:111], v[110:111], v[114:115]\n"
      "v_pk_add_f32 v[100:101], v[100:101], v[108:109]\n"
      "v_pk_add_f32 v[102:103], v[102:103], v[110:111]\n"
      "v_cvt_pk_bf16_f32 v182, v100, v101\n"
      "v_cvt_pk_bf16_f32 v183, v102, v103\n"
      "buffer_store_dwordx4 v[180:183], v36, s[44:47], s52 offen sc1\n"
      "s_barrier\n"
      "s_lshl_b32 s56, s58, 2\n"
      "s_add_u32 s56, s56, s50\n"
      "s_lshl_b32 s56, s56, 4\n"
      "s_add_u32 s56, s56, s57\n"
      "s_lshl_b32 s56, s56, 3\n"
      "s_add_u32 s56, s56, 9101312\n"
      "s_waitcnt vmcnt(0)\n"
      "buffer_store_dwordx2 v[16:17], v37, s[44:47], s56 offen sc1\n"
      "s_add_u32 s17, s16, 28\n"
      "s_lshr_b32 s18, s17, 3\n"
      "s_min_u32 vcc_lo, s48, 6\n"
      "s_lshl1_add_u32 s18, vcc_lo, s18\n"
      "s_sub_u32 vcc_lo, s18, 14\n"
      "s_cselect_b32 s18, s18, vcc_lo\n"
      "v_add_u32 v8, s18, v9\n"
      "v_mul_u32_u24 v8, 131072, v8\n"
      "v_add_u32 v8, v8, v7\n"
      "s_and_b32 s18, s17, 1\n"
      "s_bfe_u32 s17, s17, 0x20001\n"
      "s_mul_i32 s17, s17, 28\n"
      "s_add_u32 s17, s17, s18\n"
      "s_mul_i32 s17, s17, 65536\n"
      "s_lshl_b32 s18, s48, 9\n"
      "s_add_u32 s17, s17, s18\n"
      "buffer_load_dword v13, v8, s[12:15], s17 offen\n"
      "s_cmp_lt_u32 s51, 28\n"
      "s_cbranch_scc1 .Lk_free2\n"
      "s_sub_u32 s16, s51, 28\n"
      "s_lshl_b32 s16, s16, 2\n"
      "s_add_u32 s16, s16, s50\n"
      "s_sub_u32 s62, s51, 28\n"
      "s_lshr_b32 s62, s62, 1\n"
      "s_lshl_b32 s52, s48, 1\n"
      "s_add_u32 s62, s62, s52\n"
      "s_and_b32 s67, s51, 1\n"
      "s_lshl_b32 s67, s67, 2\n"
      "s_add_u32 s67, s67, s50\n"
      "s_lshl_b32 s72, s62, 2\n"
      "s_lshr_b32 s52, s67, 1\n"
      "s_add_u32 s72, s72, s52\n"
      "s_lshl_b32 s72, s72, 7\n"
      "s_add_u32 s72, s72, 9101312\n"
      "v_and_b32 v68, 15, v1\n"
      "v_lshlrev_b32 v68, 3, v68\n"
      "buffer_load_dwordx2 v[66:67], v68, s[44:47], s72 offen sc1\n"
      "s_lshl_b32 s64, s62, 7\n"
      "s_add_u32 s64, s64, s67\n"
      "s_lshl_b32 s64, s64, 9\n"
      "s_add_u32 s64, s64, 5537792\n"
      "v_lshlrev_b32 v2, 4, v10\n"
      "v_lshl_add_u32 v2, v11, 12, v2\n"
      "v_max3_f32 v14, v212, v213, v214\n"
      "v_min3_f32 v15, v212, v213, v214\n"
      "v_max3_f32 v14, v14, v215, v216\n"
      "v_min3_f32 v15, v15, v215, v216\n"
      "v_max3_f32 v14, v14, v217, v218\n"
      "v_min3_f32 v15, v15, v217, v218\n"
      "v_max3_f32 v14, v14, v219, v220\n"
      "v_min3_f32 v15, v15, v219, v220\n"
      "v_max3_f32 v14, v14, v221, v222\n"
      "v_min3_f32 v15, v15, v221, v222\n"
      "v_max3_f32 v14, v14, v223, v224\n"
      "v_min3_f32 v15, v15, v223, v224\n"
      "v_max3_f32 v14, v14, v225, v226\n"
      "v_min3_f32 v15, v15, v225, v226\n"
      "v_max_f32 v14, v14, v227\n"
      "v_min_f32 v15, v15, v227\n"
      "v_mov_b32 v18, v14\n"
      "v_mov_b32 v19, v15\n"
      "s_nop 0\n"
      "v_permlane32_swap_b32 v14, v18\n"
      "v_permlane32_swap_b32 v15, v19\n"
      "v_max_f32 v14, v14, v18\n"
      "v_min_f32 v15, v15, v19\n"
      "v_pk_mul_f32 v[14:15], v[14:15], v[32:33] op_sel:[0,1] op_sel_hi:[1,0]\n"
      "v_add_f32 v18, v14, v46\n"
      "v_add_f32 v19, v15, v46\n"
      "v_pk_fma_f32 v[180:181], v[212:213], v[32:33], v[14:15] op_sel_hi:[1,0,0]\n"
      "v_pk_fma_f32 v[196:197], v[212:213], v[32:33], v[14:15] op_sel:[0,1,1] op_sel_hi:[1,1,1]\n"
      "v_pk_fma_f32 v[182:183], v[214:215], v[32:33], v[14:15] op_sel_hi:[1,0,0]\n"
      "v_pk_fma_f32 v[198:199], v[214:215], v[32:33], v[14:15] op_sel:[0,1,1] op_sel_hi:[1,1,1]\n"
      "v_pk_fma_f32 v[184:185], v[216:217], v[32:33], v[14:15] op_sel_hi:[1,0,0]\n"
      "v_pk_fma_f32 v[200:201], v[216:217], v[32:33], v[14:15] op_sel:[0,1,1] op_sel_hi:[1,1,1]\n"
      "v_pk_fma_f32 v[186:187], v[218:219], v[32:33], v[14:15] op_sel_hi:[1,0,0]\n"
      "v_pk_fma_f32 v[202:203], v[218:219], v[32:33], v[14:15] op_sel:[0,1,1] op_sel_hi:[1,1,1]\n"
      "v_pk_fma_f32 v[188:189], v[220:221], v[32:33], v[14:15] op_sel_hi:[1,0,0]\n"
      "v_pk_fma_f32 v[204:205], v[220:221], v[32:33], v[14:15] op_sel:[0,1,1] op_sel_hi:[1,1,1]\n"
      "v_pk_fma_f32 v[190:191], v[222:223], v[32:33], v[14:15] op_sel_hi:[1,0,0]\n"
      "v_pk_fma_f32 v[206:207], v[222:223], v[32:33], v[14:15] op_sel:[0,1,1] op_sel_hi:[1,1,1]\n"
      "v_pk_fma_f32 v[192:193], v[224:225], v[32:33], v[18:19] op_sel_hi:[1,0,0]\n"
      "v_pk_fma_f32 v[208:209], v[224:225], v[32:33], v[18:19] op_sel:[0,1,1] op_sel_hi:[1,1,1]\n"
      "v_pk_fma_f32 v[194:195], v[226:227], v[32:33], v[18:19] op_sel_hi:[1,0,0]\n"
      "v_pk_fma_f32 v[210:211], v[226:227], v[32:33], v[18:19] op_sel:[0,1,1] op_sel_hi:[1,1,1]\n"
      "v_exp_f32 v180, v180\n"
      "v_exp_f32 v181, v181\n"
      "v_exp_f32 v196, v196\n"
      "v_exp_f32 v197, v197\n"
      "v_exp_f32 v182, v182\n"
      "v_exp_f32 v183, v183\n"
      "v_exp_f32 v198, v198\n"
      "v_exp_f32 v199, v199\n"
      "v_exp_f32 v184, v184\n"
      "v_exp_f32 v185, v185\n"
      "v_exp_f32 v200, v200\n"
      "v_exp_f32 v201, v201\n"
      "v_pk_add_f32 v[38:39], v[180:181], v[182:183]\n"
      "v_pk_add_f32 v[40:41], v[196:197], v[198:199]\n"
      "v_exp_f32 v186, v186\n"
      "v_exp_f32 v187, v187\n"
      "v_exp_f32 v202, v202\n"
      "v_exp_f32 v203, v203\n"
      "v_pk_add_f32 v[38:39], v[38:39], v[184:185]\n"
      "v_pk_add_f32 v[40:41], v[40:41], v[200:201]\n"
      "v_exp_f32 v188, v188\n"
      "v_exp_f32 v189, v189\n"
      "v_exp_f32 v204, v204\n"
      "v_exp_f32 v205, v205\n"
      "v_pk_add_f32 v[38:39], v[38:39], v[186:187]\n"
      "v_pk_add_f32 v[40:41], v[40:41], v[202:203]\n"
      "v_exp_f32 v190, v190\n"
      "v_exp_f32 v191, v191\n"
      "v_exp_f32 v206, v206\n"
      "v_exp_f32 v207, v207\n"
      "v_pk_add_f32 v[38:39], v[38:39], v[188:189]\n"
      "v_pk_add_f32 v[40:41], v[40:41], v[204:205]\n"
      "v_exp_f32 v192, v192\n"
      "v_exp_f32 v193, v193\n"
      "v_exp_f32 v208, v208\n"
      "v_exp_f32 v209, v209\n"
      "v_pk_add_f32 v[38:39], v[38:39], v[190:191]\n"
      "v_pk_add_f32 v[40:41], v[40:41], v[206:207]\n"
      "v_exp_f32 v194, v194\n"
      "v_exp_f32 v195, v195\n"
      "v_exp_f32 v210, v210\n"
      "v_exp_f32 v211, v211\n"
      "v_pk_add_f32 v[38:39], v[38:39], v[192:193]\n"
      "v_pk_add_f32 v[40:41], v[40:41], v[208:209]\n"
      "v_pk_add_f32 v[38:39], v[38:39], v[194:195]\n"
      "v_pk_add_f32 v[40:41], v[40:41], v[210:211]\n"
      "v_add_f32 v38, v38, v39\n"
      "v_add_f32 v39, v40, v41\n"
      "s_nop 1\n"
      "v_permlane32_swap_b32 v38, v39\n"
      "v_add_f32 v38, v38, v39\n"
      "v_rcp_f32 v38, v38\n"
      "s_nop 0\n"
      "v_mov_b32 v39, v38\n"
      "s_nop 1\n"
      "v_permlane32_swap_b32 v38, v39\n"
      "v_pk_mul_f32 v[180:181], v[180:181], v[38:39] op_sel_hi:[1,0]\n"
      "v_pk_mul_f32 v[196:197], v[196:197], v[38:39] op_sel:[0,1] op_sel_hi:[1,1]\n"
      "v_pk_mul_f32 v[182:183], v[182:183], v[38:39] op_sel_hi:[1,0]\n"
      "v_pk_mul_f32 v[198:199], v[198:199], v[38:39] op_sel:[0,1] op_sel_hi:[1,1]\n"
      "v_pk_mul_f32 v[184:185], v[184:185], v[38:39] op_sel_hi:[1,0]\n"
      "v_pk_mul_f32 v[200:201], v[200:201], v[38:39] op_sel:[0,1] op_sel_hi:[1,1]\n"
      "v_pk_mul_f32 v[186:187], v[186:187], v[38:39] op_sel_hi:[1,0]\n"
      "v_pk_mul_f32 v[202:203], v[202:203], v[38:39] op_sel:[0,1] op_sel_hi:[1,1]\n"
      "v_pk_mul_f32 v[188:189], v[188:189], v[38:39] op_sel_hi:[1,0]\n"
      "v_pk_mul_f32 v[204:205], v[204:205], v[38:39] op_sel:[0,1] op_sel_hi:[1,1]\n"
      "v_pk_mul_f32 v[190:191], v[190:191], v[38:39] op_sel_hi:[1,0]\n"
      "v_pk_mul_f32 v[206:207], v[206:207], v[38:39] op_sel:[0,1] op_sel_hi:[1,1]\n"
      "v_pk_mul_f32 v[192:193], v[192:193], v[38:39] op_sel_hi:[1,0]\n"
      "v_pk_mul_f32 v[208:209], v[208:209], v[38:39] op_sel:[0,1] op_sel_hi:[1,1]\n"
      "v_pk_mul_f32 v[194:195], v[194:195], v[38:39] op_sel_hi:[1,0]\n"
      "v_pk_mul_f32 v[210:211], v[210:211], v[38:39] op_sel:[0,1] op_sel_hi:[1,1]\n"
      "v_cvt_pk_bf16_f32 v228, v180, v181\n"
      "v_cvt_pk_bf16_f32 v236, v196, v197\n"
      "v_cvt_pk_bf16_f32 v229, v182, v183\n"
      "v_cvt_pk_bf16_f32 v237, v198, v199\n"
      "v_cvt_pk_bf16_f32 v230, v184, v185\n"
      "v_cvt_pk_bf16_f32 v238, v200, v201\n"
      "v_cvt_pk_bf16_f32 v231, v186, v187\n"
      "v_cvt_pk_bf16_f32 v239, v202, v203\n"
      "v_cvt_pk_bf16_f32 v232, v188, v189\n"
      "v_cvt_pk_bf16_f32 v240, v204, v205\n"
      "v_cvt_pk_bf16_f32 v233, v190, v191\n"
      "v_cvt_pk_bf16_f32 v241, v206, v207\n"
      "v_cvt_pk_bf16_f32 v234, v192, v193\n"
      "v_cvt_pk_bf16_f32 v242, v208, v209\n"
      "v_cvt_pk_bf16_f32 v235, v194, v195\n"
      "v_cvt_pk_bf16_f32 v243, v210, v211\n"
      "s_waitcnt vmcnt(0)\n"
      "v_cmp_ne_u64 vcc, v[66:67], v[16:17]\n"
      "s_cbranch_vccz .Lk_ok8\n"
      "s_mov_b32 s52, 20000\n"
      "s_nop 2\n"
      ".Lk_spin8:\n"
      "s_sleep 2\n"
      "buffer_load_dwordx2 v[66:67], v68, s[44:47], s72 offen sc1\n"
      "s_waitcnt vmcnt(0)\n"
      "v_cmp_ne_u64 vcc, v[66:67], v[16:17]\n"
      "s_cbranch_vccz .Lk_ok8\n"
      "s_sub_u32 s52, s52, 1\n"
      "s_cbranch_scc0 .Lk_spin8\n"
      "s_nop 3\n"
      ".Lk_ok8:\n"
      "buffer_load_dwordx4 v[84:87], v2, s[44:47], s64 offen sc1\n"
      "s_add_u32 s64, s64, 8192\n"
      "buffer_load_dwordx4 v[88:91], v2, s[44:47], s64 offen sc1\n"
      "s_add_u32 s64, s64, 8192\n"
      "buffer_load_dwordx4 v[92:95], v2, s[44:47], s64 offen sc1\n"
      "s_add_u32 s64, s64, 8192\n"
      "buffer_load_dwordx4 v[96:99], v2, s[44:47], s64 offen sc1\n"
      "s_add_u32 s64, s64, 8192\n"
      "buffer_load_dwordx4 v[100:103], v2, s[44:47], s64 offen sc1\n"
      "s_add_u32 s64, s64, 8192\n"
      "buffer_load_dwordx4 v[104:107], v2, s[44:47], s64 offen sc1\n"
      "s_add_u32 s64, s64, 8192\n"
      "buffer_load_dwordx4 v[108:111], v2, s[44:47], s64 offen sc1\n"
      "s_add_u32 s64, s64, 8192\n"
      "buffer_load_dwordx4 v[112:115], v2, s[44:47], s64 offen sc1\n"
      "s_lshl_b32 s52, s62, 3\n"
      "s_add_u32 s52, s52, s67\n"
      "s_lshl_b32 s52, s52, 9\n"
      "s_add_u32 s52, s52, 7634944\n"
      "v_lshlrev_b32 v3, 4, v10\n"
      "v_lshl_add_u32 v3, v11, 3, v3\n"
      "s_waitcnt vmcnt(7)\n"
      "v_lshlrev_b32 v116, 16, v84\n"
      "v_and_b32 v117, 0xffff0000, v84\n"
      "v_lshlrev_b32 v118, 16, v85\n"
      "v_and_b32 v119, 0xffff0000, v85\n"
      "v_lshlrev_b32 v120, 16, v86\n"
      "v_and_b32 v121, 0xffff0000, v86\n"
      "v_lshlrev_b32 v122, 16, v87\n"
      "v_and_b32 v123, 0xffff0000, v87\n"
      "s_waitcnt vmcnt(6)\n"
      "v_lshlrev_b32 v140, 16, v88\n"
      "v_and_b32 v141, 0xffff0000, v88\n"
      "v_lshlrev_b32 v142, 16, v89\n"
      "v_and_b32 v143, 0xffff0000, v89\n"
      "v_lshlrev_b32 v144, 16, v90\n"
      "v_and_b32 v145, 0xffff0000, v90\n"
      "v_lshlrev_b32 v146, 16, v91\n"
      "v_and_b32 v147, 0xffff0000, v91\n"
      "v_pk_add_f32 v[116:117], v[116:117], v[140:141]\n"
      "v_pk_add_f32 v[118:119], v[118:119], v[142:143]\n"
      "v_pk_add_f32 v[120:121], v[120:121], v[144:145]\n"
      "v_pk_add_f32 v[122:123], v[122:123], v[146:147]\n"
      "s_waitcnt vmcnt(5)\n"
      "v_lshlrev_b32 v132, 16, v92\n"
      "v_and_b32 v133, 0xffff0000, v92\n"
      "v_lshlrev_b32 v134, 16, v93\n"
      "v_and_b32 v135, 0xffff0000, v93\n"
      "v_lshlrev_b32 v136, 16, v94\n"
      "v_and_b32 v137, 0xffff0000, v94\n"
      "v_lshlrev_b32 v138, 16, v95\n"
      "v_and_b32 v139, 0xffff0000, v95\n"
      "v_pk_add_f32 v[116:117], v[116:117], v[132:133]\n"
      "v_pk_add_f32 v[118:119], v[118:119], v[134:135]\n"
      "v_pk_add_f32 v[120:121], v[120:121], v[136:137]\n"
      "v_pk_add_f32 v[122:123], v[122:123], v[138:139]\n"
      "s_waitcnt vmcnt(4)\n"
      "v_lshlrev_b32 v140, 16, v96\n"
      "v_and_b32 v141, 0xffff0000, v96\n"
      "v_lshlrev_b32 v142, 16, v97\n"
      "v_and_b32 v143, 0xffff0000, v97\n"
      "v_lshlrev_b32 v144, 16, v98\n"
      "v_and_b32 v145, 0xffff0000, v98\n"
      "v_lshlrev_b32 v146, 16, v99\n"
      "v_and_b32 v147, 0xffff0000, v99\n"
      "v_pk_add_f32 v[116:117], v[116:117], v[140:141]\n"
      "v_pk_add_f32 v[118:119], v[118:119], v[142:143]\n"
      "v_pk_add_f32 v[120:121], v[120:121], v[144:145]\n"
      "v_pk_add_f32 v[122:123], v[122:123], v[146:147]\n"
      "s_waitcnt vmcnt(3)\n"
      "v_lshlrev_b32 v132, 16, v100\n"
      "v_and_b32 v133, 0xffff0000, v100\n"
      "v_lshlrev_b32 v134, 16, v101\n"
      "v_and_b32 v135, 0xffff0000, v101\n"
      "v_lshlrev_b32 v136, 16, v102\n"
      "v_and_b32 v137, 0xffff0000, v102\n"
      "v_lshlrev_b32 v138, 16, v103\n"
      "v_and_b32 v139, 0xffff0000, v103\n"
      "v_pk_add_f32 v[116:117], v[116:117], v[132:133]\n"
      "v_pk_add_f32 v[118:119], v[118:119], v[134:135]\n"
      "v_pk_add_f32 v[120:121], v[120:121], v[136:137]\n"
      "v_pk_add_f32 v[122:123], v[122:123], v[138:139]\n"
      "s_waitcnt vmcnt(2)\n"
      "v_lshlrev_b32 v140, 16, v104\n"
      "v_and_b32 v141, 0xffff0000, v104\n"
      "v_lshlrev_b32 v142, 16, v105\n"
      "v_and_b32 v143, 0xffff0000, v105\n"
      "v_lshlrev_b32 v144, 16, v106\n"
      "v_and_b32 v145, 0xffff0000, v106\n"
      "v_lshlrev_b32 v146, 16, v107\n"
      "v_and_b32 v147, 0xffff0000, v107\n"
      "v_pk_add_f32 v[116:117], v[116:117], v[140:141]\n"
      "v_pk_add_f32 v[118:119], v[118:119], v[142:143]\n"
      "v_pk_add_f32 v[120:121], v[120:121], v[144:145]\n"
      "v_pk_add_f32 v[122:123], v[122:123], v[146:147]\n"
      "s_waitcnt vmcnt(1)\n"
      "v_lshlrev_b32 v132, 16, v108\n"
      "v_and_b32 v133, 0xffff0000, v108\n"
      "v_lshlrev_b32 v134, 16, v109\n"
      "v_and_b32 v135, 0xffff0000, v109\n"
      "v_lshlrev_b32 v136, 16, v110\n"
      "v_and_b32 v137, 0xffff0000, v110\n"
      "v_lshlrev_b32 v138, 16, v111\n"
      "v_and_b32 v139, 0xffff0000, v111\n"
      "v_pk_add_f32 v[116:117], v[116:117], v[132:133]\n"
      "v_pk_add_f32 v[118:119], v[118:119], v[134:135]\n"
      "v_pk_add_f32 v[120:121], v[120:121], v[136:137]\n"
      "v_pk_add_f32 v[122:123], v[122:123], v[138:139]\n"
      "s_waitcnt vmcnt(0)\n"
      "v_lshlrev_b32 v140, 16, v112\n"
      "v_and_b32 v141, 0xffff0000, v112\n"
      "v_lshlrev_b32 v142, 16, v113\n"
      "v_and_b32 v143, 0xffff0000, v113\n"
      "v_lshlrev_b32 v144, 16, v114\n"
      "v_and_b32 v145, 0xffff0000, v114\n"
      "v_lshlrev_b32 v146, 16, v115\n"
      "v_and_b32 v147, 0xffff0000, v115\n"
      "v_pk_add_f32 v[116:117], v[116:117], v[140:141]\n"
      "v_pk_add_f32 v[118:119], v[118:119], v[142:143]\n"
      "v_pk_add_f32 v[120:121], v[120:121], v[144:145]\n"
      "v_pk_add_f32 v[122:123], v[122:123], v[146:147]\n"
      "s_nop 0\n"
      "v_permlane32_swap_b32 v116, v120\n"
      "v_permlane32_swap_b32 v117, v121\n"
      "v_permlane32_swap_b32 v118, v122\n"
      "v_permlane32_swap_b32 v119, v123\n"
      "v_pk_add_f32 v[116:117], v[116:117], v[120:121]\n"
      "v_pk_add_f32 v[118:119], v[118:119], v[122:123]\n"
      "v_cvt_pk_bf16_f32 v124, v116, v117\n"
      "v_cvt_pk_bf16_f32 v125, v118, v119\n"
      "buffer_store_dwordx2 v[124:125], v3, s[44:47], s52 offen sc1\n"
      "s_lshl_b32 s52, s62, 4\n"
      "s_add_u32 s52, s52, s67\n"
      "s_lshl_b32 s52, s52, 3\n"
      "s_add_u32 s52, s52, 9109504\n"
      "s_waitcnt vmcnt(0)\n"
      "buffer_store_dwordx2 v[16:17], v37, s[44:47], s52 offen sc1\n"
      "s_add_u32 s17, s16, 80\n"
      "s_lshr_b32 s18, s17, 3\n"
      "s_min_u32 vcc_lo, s48, 6\n"
      "s_lshl1_add_u32 s18, vcc_lo, s18\n"
      "s_sub_u32 vcc_lo, s18, 14\n"
      "s_cselect_b32 s18, s18, vcc_lo\n"
      "v_add_u32 v8, s18, v9\n"
      "v_mul_u32_u24 v8, 131072, v8\n"
      "v_add_u32 v8, v8, v7\n"
      "s_and_b32 s18, s17, 1\n"
      "s_bfe_u32 s17, s17, 0x20001\n"
      "s_mul_i32 s17, s17, 28\n"
      "s_add_u32 s17, s17, s18\n"
      "s_mul_i32 s17, s17, 65536\n"
      "s_lshl_b32 s18, s48, 9\n"
      "s_add_u32 s17, s17, s18\n"
      "buffer_load_dword v13, v8, s[12:15], s17 offen\n"
      "s_add_u32 s17, s16, 96\n"
      "s_lshr_b32 s18, s17, 3\n"
      "s_min_u32 vcc_lo, s48, 6\n"
      "s_lshl1_add_u32 s18, vcc_lo, s18\n"
      "s_sub_u32 vcc_lo, s18, 14\n"
      "s_cselect_b32 s18, s18, vcc_lo\n"
      "v_add_u32 v8, s18, v9\n"
      "v_mul_u32_u24 v8, 131072, v8\n"
      "v_add_u32 v8, v8, v7\n"
      "s_and_b32 s18, s17, 1\n"
      "s_bfe_u32 s17, s17, 0x20001\n"
      "s_mul_i32 s17, s17, 28\n"
      "s_add_u32 s17, s17, s18\n"
      "s_mul_i32 s17, s17, 65536\n"
      "s_lshl_b32 s18, s48, 9\n"
      "s_add_u32 s17, s17, s18\n"
      "buffer_load_dword v13, v8, s[12:15], s17 offen\n"
      "s_mov_b64 s[12:13], s[20:21]\n"
      "s_and_b32 s14, s19, 401408\n"
      "v_lshlrev_b32 v7, 7, v1\n"
      "s_sub_u32 s17, s16, 0\n"
      "s_mul_i32 s18, s48, 7\n"
      "s_add_u32 s18, s18, s17\n"
      "s_cmp_lt_u32 s17, 7\n"
      "s_cselect_b32 s17, s18, 64\n"
      "s_lshl_b32 s17, s17, 13\n"
      "buffer_load_dword v13, v7, s[12:15], s17 offen\n"
      "s_lshl_b32 s72, s58, 7\n"
      "s_add_u32 s72, s72, 9109504\n"
      "v_and_b32 v68, 7, v1\n"
      "v_lshlrev_b32 v68, 3, v68\n"
      "buffer_load_dwordx2 v[66:67], v68, s[44:47], s72 offen sc1\n"
      "s_waitcnt vmcnt(0)\n"
      "v_cmp_ne_u64 vcc, v[66:67], v[16:17]\n"
      "s_cbranch_vccz .Lk_ok9\n"
      "s_mov_b32 s52, 20000\n"
      "s_nop 2\n"
      ".Lk_spin9:\n"
      "s_sleep 2\n"
      "buffer_load_dwordx2 v[66:67], v68, s[44:47], s72 offen sc1\n"
      "s_waitcnt vmcnt(0)\n"
      "v_cmp_ne_u64 vcc, v[66:67], v[16:17]\n"
      "s_cbranch_vccz .Lk_ok9\n"
      "s_sub_u32 s52, s52, 1\n"
      "s_cbranch_scc0 .Lk_spin9\n"
      "s_nop 3\n"
      ".Lk_ok9:\n"
      "s_lshl_b32 s52, s58, 12\n"
      "s_add_u32 s52, s52, 7634944\n"
      "buffer_load_dwordx4 v[180:183], v36, s[44:47], s52 offen offset:0 sc1\n"
      "buffer_load_dwordx4 v[184:187], v36, s[44:47], s52 offen offset:1024 sc1\n"
      "buffer_load_dwordx4 v[188:191], v36, s[44:47], s52 offen offset:2048 sc1\n"
      "buffer_load_dwordx4 v[192:195], v36, s[44:47], s52 offen offset:3072 sc1\n"
      "s_waitcnt vmcnt(3)\n"
      "v_mfma_f32_32x32x16_bf16 v[196:211], v[180:183], v[228:231], 0\n"
      "s_waitcnt vmcnt(2)\n"
      "v_mfma_f32_32x32x16_bf16 v[196:211], v[184:187], v[232:235], v[196:211]\n"
      "s_waitcnt vmcnt(1)\n"
      "v_mfma_f32_32x32x16_bf16 v[196:211], v[188:191], v[236:239], v[196:211]\n"
      "s_waitcnt vmcnt(0)\n"
      "v_mfma_f32_32x32x16_bf16 v[196:211], v[192:195], v[240:243], v[196:211]\n"
      "s_lshl_b32 s52, s57, 2\n"
      "s_add_u32 s52, s52, s50\n"
      "s_lshl_b32 s52, s52, 4\n"
      "s_add_u32 s52, s52, s58\n"
      "s_lshl_b32 s52, s52, 11\n"
      "s_add_u32 s52, s52, 3440640\n"
      "v_cmp_eq_u32 vcc, 0, v11\n"
      "s_nop 4\n"
      "v_mov_b32 v2, v208\n"
      "v_mov_b32 v3, v208\n"
      "v_cndmask_b32 v208, 0, v208, vcc\n"
      "v_cndmask_b32 v209, 0, v209, vcc\n"
      "v_cndmask_b32 v210, 0, v210, vcc\n"
      "v_cndmask_b32 v211, 0, v211, vcc\n"
      "v_permlane32_swap_b32 v2, v3\n"
      "v_add_f32 v3, 0x358637bd, v3\n"
      "v_rcp_f32 v180, v3\n"
      "s_nop 0\n"
      "v_pk_mul_f32 v[196:197], v[196:197], v[180:181] op_sel_hi:[1,0]\n"
      "v_pk_mul_f32 v[198:199], v[198:199], v[180:181] op_sel_hi:[1,0]\n"
      "v_pk_mul_f32 v[200:201], v[200:201], v[180:181] op_sel_hi:[1,0]\n"
      "v_pk_mul_f32 v[202:203], v[202:203], v[180:181] op_sel_hi:[1,0]\n"
      "v_pk_mul_f32 v[204:205], v[204:205], v[180:181] op_sel_hi:[1,0]\n"
      "v_pk_mul_f32 v[206:207], v[206:207], v[180:181] op_sel_hi:[1,0]\n"
      "v_pk_mul_f32 v[208:209], v[208:209], v[180:181] op_sel_hi:[1,0]\n"
      "v_pk_mul_f32 v[210:211], v[210:211], v[180:181] op_sel_hi:[1,0]\n"
      "v_cvt_pk_bf16_f32 v184, v196, v197\n"
      "v_cvt_pk_bf16_f32 v185, v198, v199\n"
      "v_cvt_pk_bf16_f32 v186, v200, v201\n"
      "v_cvt_pk_bf16_f32 v187, v202, v203\n"
      "v_cvt_pk_bf16_f32 v188, v204, v205\n"
      "v_cvt_pk_bf16_f32 v189, v206, v207\n"
      "v_cvt_pk_bf16_f32 v190, v208, v209\n"
      "v_cvt_pk_bf16_f32 v191, v210, v211\n"
      "buffer_store_dwordx4 v[184:187], v36, s[44:47], s52 offen offset:0 sc1\n"
      "buffer_store_dwordx4 v[188:191], v36, s[44:47], s52 offen offset:1024 sc1\n"
      "s_lshl_b32 s52, s57, 1\n"
      "s_lshr_b32 s53, s50, 1\n"
      "s_add_u32 s52, s52, s53\n"
      "s_lshl_b32 s52, s52, 2\n"
      "s_lshr_b32 s53, s58, 2\n"
      "s_add_u32 s52, s52, s53\n"
      "s_lshl_b32 s52, s52, 7\n"
      "s_and_b32 s53, s50, 1\n"
      "s_lshl_b32 s53, s53, 5\n"
      "s_add_u32 s52, s52, s53\n"
      "s_and_b32 s53, s58, 3\n"
      "s_lshl_b32 s53, s53, 3\n"
      "s_add_u32 s52, s52, s53\n"
      "s_add_u32 s52, s52, 9117696\n"
      "s_waitcnt vmcnt(0)\n"
      "buffer_store_dwordx2 v[16:17], v37, s[44:47], s52 offen sc1\n"
      "s_branch .Lk_end\n"
      ".Lk_free2:\n"
      "s_add_u32 s17, s16, 56\n"
      "s_lshr_b32 s18, s17, 3\n"
      "s_min_u32 vcc_lo, s48, 6\n"
      "s_lshl1_add_u32 s18, vcc_lo, s18\n"
      "s_sub_u32 vcc_lo, s18, 14\n"
      "s_cselect_b32 s18, s18, vcc_lo\n"
      "v_add_u32 v8, s18, v9\n"
      "v_mul_u32_u24 v8, 131072, v8\n"
      "v_add_u32 v8, v8, v7\n"
      "s_and_b32 s18, s17, 1\n"
      "s_bfe_u32 s17, s17, 0x20001\n"
      "s_mul_i32 s17, s17, 28\n"
      "s_add_u32 s17, s17, s18\n"
      "s_mul_i32 s17, s17, 65536\n"
      "s_lshl_b32 s18, s48, 9\n"
      "s_add_u32 s17, s17, s18\n"
      "buffer_load_dword v13, v8, s[12:15], s17 offen\n"
      "s_add_u32 s17, s16, 68\n"
      "s_lshr_b32 s18, s17, 3\n"
      "s_min_u32 vcc_lo, s48, 6\n"
      "s_lshl1_add_u32 s18, vcc_lo, s18\n"
      "s_sub_u32 vcc_lo, s18, 14\n"
      "s_cselect_b32 s18, s18, vcc_lo\n"
      "v_add_u32 v8, s18, v9\n"
      "v_mul_u32_u24 v8, 131072, v8\n"
      "v_add_u32 v8, v8, v7\n"
      "s_and_b32 s18, s17, 1\n"
      "s_bfe_u32 s17, s17, 0x20001\n"
      "s_mul_i32 s17, s17, 28\n"
      "s_add_u32 s17, s17, s18\n"
      "s_mul_i32 s17, s17, 65536\n"
      "s_lshl_b32 s18, s48, 9\n"
      "s_add_u32 s17, s17, s18\n"
      "buffer_load_dword v13, v8, s[12:15], s17 offen\n"
      "s_lshl_b32 s63, s60, 17\n"
      "s_mov_b32 m0, s66\n"
      "s_nop 0\n"
      "buffer_load_dwordx4 v60, s[32:35], s63 offen offset:0 lds\n"
      "buffer_load_dwordx4 v61, s[32:35], s63 offen offset:1024 lds\n"
      "buffer_load_dwordx4 v62, s[32:35], s63 offen offset:2048 lds\n"
      "buffer_load_dwordx4 v63, s[32:35], s63 offen offset:3072 lds\n"
      "s_add_u32 s63, s63, 131072\n"
      "s_cmp_eq_u32 s63, 1835008\n"
      "s_cselect_b32 s63, 0, s63\n"
      "s_add_u32 m0, s66, 4096\n"
      "s_nop 0\n"
      "buffer_load_dwordx4 v60, s[32:35], s63 offen offset:0 lds\n"
      "buffer_load_dwordx4 v61, s[32:35], s63 offen offset:1024 lds\n"
      "buffer_load_dwordx4 v62, s[32:35], s63 offen offset:2048 lds\n"
      "buffer_load_dwordx4 v63, s[32:35], s63 offen offset:3072 lds\n"
      "s_add_u32 s63, s63, 131072\n"
      "s_cmp_eq_u32 s63, 1835008\n"
      "s_cselect_b32 s63, 0, s63\n"
      "s_add_u32 m0, s66, 8192\n"
      "s_nop 0\n"
      "buffer_load_dwordx4 v60, s[32:35], s63 offen offset:0 lds\n"
      "buffer_load_dwordx4 v61, s[32:35], s63 offen offset:1024 lds\n"
      "buffer_load_dwordx4 v62, s[32:35], s63 offen offset:2048 lds\n"
      "buffer_load_dwordx4 v63, s[32:35], s63 offen offset:3072 lds\n"
      "s_add_u32 s63, s63, 131072\n"
      "s_cmp_eq_u32 s63, 1835008\n"
      "s_cselect_b32 s63, 0, s63\n"
      "s_add_u32 m0, s61, 8192\n"
      "s_nop 0\n"
      "buffer_load_dwordx4 v60, s[32:35], s63 offen offset:0 lds\n"
      "buffer_load_dwordx4 v61, s[32:35], s63 offen offset:1024 lds\n"
      "buffer_load_dwordx4 v62, s[32:35], s63 offen offset:2048 lds\n"
      "buffer_load_dwordx4 v63, s[32:35], s63 offen offset:3072 lds\n"
      "s_add_u32 s63, s63, 131072\n"
      "s_cmp_eq_u32 s63, 1835008\n"
      "s_cselect_b32 s63, 0, s63\n"
      "s_add_u32 m0, s61, 12288\n"
      "s_nop 0\n"
      "buffer_load_dwordx4 v60, s[32:35], s63 offen offset:0 lds\n"
      "buffer_load_dwordx4 v61, s[32:35], s63 offen offset:1024 lds\n"
      "buffer_load_dwordx4 v62, s[32:35], s63 offen offset:2048 lds\n"
      "buffer_load_dwordx4 v63, s[32:35], s63 offen offset:3072 lds\n"
      "s_add_u32 s63, s63, 131072\n"
      "s_cmp_eq_u32 s63, 1835008\n"
      "s_cselect_b32 s63, 0, s63\n"
      "s_add_u32 m0, s61, 16384\n"
      "s_nop 0\n"
      "buffer_load_dwordx4 v60, s[32:35], s63 offen offset:0 lds\n"
      "buffer_load_dwordx4 v61, s[32:35], s63 offen offset:1024 lds\n"
      "buffer_load_dwordx4 v62, s[32:35], s63 offen offset:2048 lds\n"
      "buffer_load_dwordx4 v63, s[32:35], s63 offen offset:3072 lds\n"
      "s_add_u32 s63, s63, 131072\n"
      "s_cmp_eq_u32 s63, 1835008\n"
      "s_cselect_b32 s63, 0, s63\n"
      "s_add_u32 m0, s61, 20480\n"
      "s_nop 0\n"
      "buffer_load_dwordx4 v60, s[32:35], s63 offen offset:0 lds\n"
      "buffer_load_dwordx4 v61, s[32:35], s63 offen offset:1024 lds\n"
      "buffer_load_dwordx4 v62, s[32:35], s63 offen offset:2048 lds\n"
      "buffer_load_dwordx4 v63, s[32:35], s63 offen offset:3072 lds\n"
      "s_add_u32 s63, s63, 131072\n"
      "s_cmp_eq_u32 s63, 1835008\n"
      "s_cselect_b32 s63, 0, s63\n"
      "v_max3_f32 v14, v212, v213, v214\n"
      "v_min3_f32 v15, v212, v213, v214\n"
      "v_max3_f32 v14, v14, v215, v216\n"
      "v_min3_f32 v15, v15, v215, v216\n"
      "v_max3_f32 v14, v14, v217, v218\n"
      "v_min3_f32 v15, v15, v217, v218\n"
      "v_max3_f32 v14, v14, v219, v220\n"
      "v_min3_f32 v15, v15, v219, v220\n"
      "v_max3_f32 v14, v14, v221, v222\n"
      "v_min3_f32 v15, v15, v221, v222\n"
      "v_max3_f32 v14, v14, v223, v224\n"
      "v_min3_f32 v15, v15, v223, v224\n"
      "v_max3_f32 v14, v14, v225, v226\n"
      "v_min3_f32 v15, v15, v225, v226\n"
      "v_max_f32 v14, v14, v227\n"
      "v_min_f32 v15, v15, v227\n"
      "v_mov_b32 v18, v14\n"
      "v_mov_b32 v19, v15\n"
      "s_nop 0\n"
      "v_permlane32_swap_b32 v14, v18\n"
      "v_permlane32_swap_b32 v15, v19\n"
      "v_max_f32 v14, v14, v18\n"
      "v_min_f32 v15, v15, v19\n"
      "v_pk_mul_f32 v[14:15], v[14:15], v[32:33] op_sel:[0,1] op_sel_hi:[1,0]\n"
      "v_add_f32 v18, v14, v46\n"
      "v_add_f32 v19, v15, v46\n"
      "v_pk_fma_f32 v[180:181], v[212:213], v[32:33], v[14:15] op_sel_hi:[1,0,0]\n"
      "v_pk_fma_f32 v[196:197], v[212:213], v[32:33], v[14:15] op_sel:[0,1,1] op_sel_hi:[1,1,1]\n"
      "v_pk_fma_f32 v[182:183], v[214:215], v[32:33], v[14:15] op_sel_hi:[1,0,0]\n"
      "v_pk_fma_f32 v[198:199], v[214:215], v[32:33], v[14:15] op_sel:[0,1,1] op_sel_hi:[1,1,1]\n"
      "v_pk_fma_f32 v[184:185], v[216:217], v[32:33], v[14:15] op_sel_hi:[1,0,0]\n"
      "v_pk_fma_f32 v[200:201], v[216:217], v[32:33], v[14:15] op_sel:[0,1,1] op_sel_hi:[1,1,1]\n"
      "v_pk_fma_f32 v[186:187], v[218:219], v[32:33], v[14:15] op_sel_hi:[1,0,0]\n"
      "v_pk_fma_f32 v[202:203], v[218:219], v[32:33], v[14:15] op_sel:[0,1,1] op_sel_hi:[1,1,1]\n"
      "v_pk_fma_f32 v[188:189], v[220:221], v[32:33], v[14:15] op_sel_hi:[1,0,0]\n"
      "v_pk_fma_f32 v[204:205], v[220:221], v[32:33], v[14:15] op_sel:[0,1,1] op_sel_hi:[1,1,1]\n"
      "v_pk_fma_f32 v[190:191], v[222:223], v[32:33], v[14:15] op_sel_hi:[1,0,0]\n"
      "v_pk_fma_f32 v[206:207], v[222:223], v[32:33], v[14:15] op_sel:[0,1,1] op_sel_hi:[1,1,1]\n"
      "v_pk_fma_f32 v[192:193], v[224:225], v[32:33], v[18:19] op_sel_hi:[1,0,0]\n"
      "v_pk_fma_f32 v[208:209], v[224:225], v[32:33], v[18:19] op_sel:[0,1,1] op_sel_hi:[1,1,1]\n"
      "v_pk_fma_f32 v[194:195], v[226:227], v[32:33], v[18:19] op_sel_hi:[1,0,0]\n"
      "v_pk_fma_f32 v[210:211], v[226:227], v[32:33], v[18:19] op_sel:[0,1,1] op_sel_hi:[1,1,1]\n"
      "v_exp_f32 v180, v180\n"
      "v_exp_f32 v181, v181\n"
      "v_exp_f32 v196, v196\n"
      "v_exp_f32 v197, v197\n"
      "v_exp_f32 v182, v182\n"
      "v_exp_f32 v183, v183\n"
      "v_exp_f32 v198, v198\n"
      "v_exp_f32 v199, v199\n"
      "v_exp_f32 v184, v184\n"
      "v_exp_f32 v185, v185\n"
      "v_exp_f32 v200, v200\n"
      "v_exp_f32 v201, v201\n"
      "v_pk_add_f32 v[38:39], v[180:181], v[182:183]\n"
      "v_pk_add_f32 v[40:41], v[196:197], v[198:199]\n"
      "v_exp_f32 v186, v186\n"
      "v_exp_f32 v187, v187\n"
      "v_exp_f32 v202, v202\n"
      "v_exp_f32 v203, v203\n"
      "v_pk_add_f32 v[38:39], v[38:39], v[184:185]\n"
      "v_pk_add_f32 v[40:41], v[40:41], v[200:201]\n"
      "v_exp_f32 v188, v188\n"
      "v_exp_f32 v189, v189\n"
      "v_exp_f32 v204, v204\n"
      "v_exp_f32 v205, v205\n"
      "v_pk_add_f32 v[38:39], v[38:39], v[186:187]\n"
      "v_pk_add_f32 v[40:41], v[40:41], v[202:203]\n"
      "v_exp_f32 v190, v190\n"
      "v_exp_f32 v191, v191\n"
      "v_exp_f32 v206, v206\n"
      "v_exp_f32 v207, v207\n"
      "v_pk_add_f32 v[38:39], v[38:39], v[188:189]\n"
      "v_pk_add_f32 v[40:41], v[40:41], v[204:205]\n"
      "v_exp_f32 v192, v192\n"
      "v_exp_f32 v193, v193\n"
      "v_exp_f32 v208, v208\n"
      "v_exp_f32 v209, v209\n"
      "v_pk_add_f32 v[38:39], v[38:39], v[190:191]\n"
      "v_pk_add_f32 v[40:41], v[40:41], v[206:207]\n"
      "v_exp_f32 v194, v194\n"
      "v_exp_f32 v195, v195\n"
      "v_exp_f32 v210, v210\n"
      "v_exp_f32 v211, v211\n"
      "v_pk_add_f32 v[38:39], v[38:39], v[192:193]\n"
      "v_pk_add_f32 v[40:41], v[40:41], v[208:209]\n"
      "v_pk_add_f32 v[38:39], v[38:39], v[194:195]\n"
      "v_pk_add_f32 v[40:41], v[40:41], v[210:211]\n"
      "v_add_f32 v38, v38, v39\n"
      "v_add_f32 v39, v40, v41\n"
      "s_nop 1\n"
      "v_permlane32_swap_b32 v38, v39\n"
      "v_add_f32 v38, v38, v39\n"
      "v_rcp_f32 v38, v38\n"
      "s_nop 0\n"
      "v_mov_b32 v39, v38\n"
      "s_nop 1\n"
      "v_permlane32_swap_b32 v38, v39\n"
      "v_pk_mul_f32 v[180:181], v[180:181], v[38:39] op_sel_hi:[1,0]\n"
      "v_pk_mul_f32 v[196:197], v[196:197], v[38:39] op_sel:[0,1] op_sel_hi:[1,1]\n"
      "v_pk_mul_f32 v[182:183], v[182:183], v[38:39] op_sel_hi:[1,0]\n"
      "v_pk_mul_f32 v[198:199], v[198:199], v[38:39] op_sel:[0,1] op_sel_hi:[1,1]\n"
      "v_pk_mul_f32 v[184:185], v[184:185], v[38:39] op_sel_hi:[1,0]\n"
      "v_pk_mul_f32 v[200:201], v[200:201], v[38:39] op_sel:[0,1] op_sel_hi:[1,1]\n"
      "v_pk_mul_f32 v[186:187], v[186:187], v[38:39] op_sel_hi:[1,0]\n"
      "v_pk_mul_f32 v[202:203], v[202:203], v[38:39] op_sel:[0,1] op_sel_hi:[1,1]\n"
      "v_pk_mul_f32 v[188:189], v[188:189], v[38:39] op_sel_hi:[1,0]\n"
      "v_pk_mul_f32 v[204:205], v[204:205], v[38:39] op_sel:[0,1] op_sel_hi:[1,1]\n"
      "v_pk_mul_f32 v[190:191], v[190:191], v[38:39] op_sel_hi:[1,0]\n"
      "v_pk_mul_f32 v[206:207], v[206:207], v[38:39] op_sel:[0,1] op_sel_hi:[1,1]\n"
      "v_pk_mul_f32 v[192:193], v[192:193], v[38:39] op_sel_hi:[1,0]\n"
      "v_pk_mul_f32 v[208:209], v[208:209], v[38:39] op_sel:[0,1] op_sel_hi:[1,1]\n"
      "v_pk_mul_f32 v[194:195], v[194:195], v[38:39] op_sel_hi:[1,0]\n"
      "v_pk_mul_f32 v[210:211], v[210:211], v[38:39] op_sel:[0,1] op_sel_hi:[1,1]\n"
      "v_cvt_pk_bf16_f32 v228, v180, v181\n"
      "v_cvt_pk_bf16_f32 v236, v196, v197\n"
      "v_cvt_pk_bf16_f32 v229, v182, v183\n"
      "v_cvt_pk_bf16_f32 v237, v198, v199\n"
      "v_cvt_pk_bf16_f32 v230, v184, v185\n"
      "v_cvt_pk_bf16_f32 v238, v200, v201\n"
      "v_cvt_pk_bf16_f32 v231, v186, v187\n"
      "v_cvt_pk_bf16_f32 v239, v202, v203\n"
      "v_cvt_pk_bf16_f32 v232, v188, v189\n"
      "v_cvt_pk_bf16_f32 v240, v204, v205\n"
      "v_cvt_pk_bf16_f32 v233, v190, v191\n"
      "v_cvt_pk_bf16_f32 v241, v206, v207\n"
      "v_cvt_pk_bf16_f32 v234, v192, v193\n"
      "v_cvt_pk_bf16_f32 v242, v208, v209\n"
      "v_cvt_pk_bf16_f32 v235, v194, v195\n"
      "v_cvt_pk_bf16_f32 v243, v210, v211\n"
      "s_waitcnt vmcnt(24)\n"
      "ds_read_b64_tr_b16 a[112:113], v78 offset:0\n"
      "ds_read_b64_tr_b16 a[114:115], v78 offset:1024\n"
      "ds_read_b64_tr_b16 a[116:117], v79 offset:0\n"
      "ds_read_b64_tr_b16 a[118:119], v79 offset:1024\n"
      "ds_read_b64_tr_b16 a[120:121], v78 offset:2048\n"
      "ds_read_b64_tr_b16 a[122:123], v78 offset:3072\n"
      "ds_read_b64_tr_b16 a[124:125], v79 offset:2048\n"
      "ds_read_b64_tr_b16 a[126:127], v79 offset:3072\n"
      "s_mov_b32 m0, s66\n"
      "s_waitcnt lgkmcnt(0)\n"
      "buffer_load_dwordx4 v60, s[32:35], s63 offen offset:0 lds\n"
      "buffer_load_dwordx4 v61, s[32:35], s63 offen offset:1024 lds\n"
      "buffer_load_dwordx4 v62, s[32:35], s63 offen offset:2048 lds\n"
      "buffer_load_dwordx4 v63, s[32:35], s63 offen offset:3072 lds\n"
      "s_add_u32 s63, s63, 131072\n"
      "s_cmp_eq_u32 s63, 1835008\n"
      "s_cselect_b32 s63, 0, s63\n"
      "s_waitcnt vmcnt(24)\n"
      "ds_read_b64_tr_b16 a[128:129], v78 offset:4096\n"
      "ds_read_b64_tr_b16 a[130:131], v78 offset:5120\n"
      "ds_read_b64_tr_b16 a[132:133], v79 offset:4096\n"
      "ds_read_b64_tr_b16 a[134:135], v79 offset:5120\n"
      "ds_read_b64_tr_b16 a[136:137], v78 offset:6144\n"
      "ds_read_b64_tr_b16 a[138:139], v78 offset:7168\n"
      "ds_read_b64_tr_b16 a[140:141], v79 offset:6144\n"
      "ds_read_b64_tr_b16 a[142:143], v79 offset:7168\n"
      "s_add_u32 m0, s66, 4096\n"
      "s_waitcnt lgkmcnt(0)\n"
      "buffer_load_dwordx4 v60, s[32:35], s63 offen offset:0 lds\n"
      "buffer_load_dwordx4 v61, s[32:35], s63 offen offset:1024 lds\n"
      "buffer_load_dwordx4 v62, s[32:35], s63 offen offset:2048 lds\n"
      "buffer_load_dwordx4 v63, s[32:35], s63 offen offset:3072 lds\n"
      "s_add_u32 s63, s63, 131072\n"
      "s_cmp_eq_u32 s63, 1835008\n"
      "s_cselect_b32 s63, 0, s63\n"
      "s_waitcnt vmcnt(24)\n"
      "ds_read_b64_tr_b16 a[144:145], v78 offset:8192\n"
      "ds_read_b64_tr_b16 a[146:147], v78 offset:9216\n"
      "ds_read_b64_tr_b16 a[148:149], v79 offset:8192\n"
      "ds_read_b64_tr_b16 a[150:151], v79 offset:9216\n"
      "ds_read_b64_tr_b16 a[152:153], v78 offset:10240\n"
      "ds_read_b64_tr_b16 a[154:155], v78 offset:11264\n"
      "ds_read_b64_tr_b16 a[156:157], v79 offset:10240\n"
      "ds_read_b64_tr_b16 a[158:159], v79 offset:11264\n"
      "s_add_u32 m0, s66, 8192\n"
      "s_waitcnt lgkmcnt(0)\n"
      "buffer_load_dwordx4 v60, s[32:35], s63 offen offset:0 lds\n"
      "buffer_load_dwordx4 v61, s[32:35], s63 offen offset:1024 lds\n"
      "buffer_load_dwordx4 v62, s[32:35], s63 offen offset:2048 lds\n"
      "buffer_load_dwordx4 v63, s[32:35], s63 offen offset:3072 lds\n"
      "s_add_u32 s63, s63, 131072\n"
      "s_cmp_eq_u32 s63, 1835008\n"
      "s_cselect_b32 s63, 0, s63\n"
      "s_waitcnt vmcnt(24)\n"
      "ds_read_b64_tr_b16 a[160:161], v76 offset:8192\n"
      "ds_read_b64_tr_b16 a[162:163], v76 offset:9216\n"
      "ds_read_b64_tr_b16 a[164:165], v77 offset:8192\n"
      "ds_read_b64_tr_b16 a[166:167], v77 offset:9216\n"
      "ds_read_b64_tr_b16 a[168:169], v76 offset:10240\n"
      "ds_read_b64_tr_b16 a[170:171], v76 offset:11264\n"
      "ds_read_b64_tr_b16 a[172:173], v77 offset:10240\n"
      "ds_read_b64_tr_b16 a[174:175], v77 offset:11264\n"
      "s_add_u32 m0, s61, 8192\n"
      "s_waitcnt lgkmcnt(0)\n"
      "buffer_load_dwordx4 v60, s[32:35], s63 offen offset:0 lds\n"
      "buffer_load_dwordx4 v61, s[32:35], s63 offen offset:1024 lds\n"
      "buffer_load_dwordx4 v62, s[32:35], s63 offen offset:2048 lds\n"
      "buffer_load_dwordx4 v63, s[32:35], s63 offen offset:3072 lds\n"
      "s_add_u32 s63, s63, 131072\n"
      "s_cmp_eq_u32 s63, 1835008\n"
      "s_cselect_b32 s63, 0, s63\n"
      "s_waitcnt vmcnt(24)\n"
      "ds_read_b64_tr_b16 a[176:177], v76 offset:12288\n"
      "ds_read_b64_tr_b16 a[178:179], v76 offset:13312\n"
      "ds_read_b64_tr_b16 a[180:181], v77 offset:12288\n"
      "ds_read_b64_tr_b16 a[182:183], v77 offset:13312\n"
      "ds_read_b64_tr_b16 a[184:185], v76 offset:14336\n"
      "ds_read_b64_tr_b16 a[186:187], v76 offset:15360\n"
      "ds_read_b64_tr_b16 a[188:189], v77 offset:14336\n"
      "ds_read_b64_tr_b16 a[190:191], v77 offset:15360\n"
      "s_add_u32 m0, s61, 12288\n"
      "s_waitcnt lgkmcnt(0)\n"
      "buffer_load_dwordx4 v60, s[32:35], s63 offen offset:0 lds\n"
      "buffer_load_dwordx4 v61, s[32:35], s63 offen offset:1024 lds\n"
      "buffer_load_dwordx4 v62, s[32:35], s63 offen offset:2048 lds\n"
      "buffer_load_dwordx4 v63, s[32:35], s63 offen offset:3072 lds\n"
      "s_add_u32 s63, s63, 131072\n"
      "s_cmp_eq_u32 s63, 1835008\n"
      "s_cselect_b32 s63, 0, s63\n"
      "s_waitcnt vmcnt(24)\n"
      "ds_read_b64_tr_b16 a[192:193], v76 offset:16384\n"
      "ds_read_b64_tr_b16 a[194:195], v76 offset:17408\n"
      "ds_read_b64_tr_b16 a[196:197], v77 offset:16384\n"
      "ds_read_b64_tr_b16 a[198:199], v77 offset:17408\n"
      "ds_read_b64_tr_b16 a[200:201], v76 offset:18432\n"
      "ds_read_b64_tr_b16 a[202:203], v76 offset:19456\n"
      "ds_read_b64_tr_b16 a[204:205], v77 offset:18432\n"
      "ds_read_b64_tr_b16 a[206:207], v77 offset:19456\n"
      "s_add_u32 m0, s61, 16384\n"
      "s_waitcnt lgkmcnt(0)\n"
      "buffer_load_dwordx4 v60, s[32:35], s63 offen offset:0 lds\n"
      "buffer_load_dwordx4 v61, s[32:35], s63 offen offset:1024 lds\n"
      "buffer_load_dwordx4 v62, s[32:35], s63 offen offset:2048 lds\n"
      "buffer_load_dwordx4 v63, s[32:35], s63 offen offset:3072 lds\n"
      "s_add_u32 s63, s63, 131072\n"
      "s_cmp_eq_u32 s63, 1835008\n"
      "s_cselect_b32 s63, 0, s63\n"
      "s_waitcnt vmcnt(24)\n"
      "ds_read_b64_tr_b16 a[208:209], v76 offset:20480\n"
      "ds_read_b64_tr_b16 a[210:211], v76 offset:21504\n"
      "ds_read_b64_tr_b16 a[212:213], v77 offset:20480\n"
      "ds_read_b64_tr_b16 a[214:215], v77 offset:21504\n"
      "ds_read_b64_tr_b16 a[216:217], v76 offset:22528\n"
      "ds_read_b64_tr_b16 a[218:219], v76 offset:23552\n"
      "ds_read_b64_tr_b16 a[220:221], v77 offset:22528\n"
      "ds_read_b64_tr_b16 a[222:223], v77 offset:23552\n"
      "s_add_u32 m0, s61, 20480\n"
      "s_waitcnt lgkmcnt(0)\n"
      "buffer_load_dwordx4 v60, s[32:35], s63 offen offset:0 lds\n"
      "buffer_load_dwordx4 v61, s[32:35], s63 offen offset:1024 lds\n"
      "buffer_load_dwordx4 v62, s[32:35], s63 offen offset:2048 lds\n"
      "buffer_load_dwordx4 v63, s[32:35], s63 offen offset:3072 lds\n"
      "s_add_u32 s64, s76, 14\n"
      "s_and_b32 s64, s64, 3\n"
      "s_mul_i32 s64, s64, 25088\n"
      "s_add_u32 m0, s61, 24576\n"
      "s_nop 0\n"
      "buffer_load_dwordx4 v72, s[20:23], s64 offen offset:0 lds\n"
      "buffer_load_dwordx4 v73, s[20:23], s64 offen offset:1024 lds\n"
      "buffer_load_dwordx4 v74, s[20:23], s64 offen offset:2048 lds\n"
      "buffer_load_dwordx4 v75, s[20:23], s64 offen offset:3072 lds\n"
      "s_add_u32 s64, s76, 15\n"
      "s_and_b32 s64, s64, 3\n"
      "s_mul_i32 s64, s64, 25088\n"
      "s_mov_b32 m0, s61\n"
      "s_nop 0\n"
      "buffer_load_dwordx4 v72, s[20:23], s64 offen offset:0 lds\n"
      "buffer_load_dwordx4 v73, s[20:23], s64 offen offset:1024 lds\n"
      "buffer_load_dwordx4 v74, s[20:23], s64 offen offset:2048 lds\n"
      "buffer_load_dwordx4 v75, s[20:23], s64 offen offset:3072 lds\n"
      "s_lshl_b32 s72, s58, 7\n"
      "s_add_u32 s72, s72, 9109504\n"
      "v_and_b32 v68, 7, v1\n"
      "v_lshlrev_b32 v68, 3, v68\n"
      "buffer_load_dwordx2 v[66:67], v68, s[44:47], s72 offen sc1\n"
      "s_add_u32 s64, s76, 16\n"
      "s_and_b32 s64, s64, 3\n"
      "s_mul_i32 s64, s64, 25088\n"
      "s_add_u32 m0, s61, 4096\n"
      "s_nop 0\n"
      "buffer_load_dwordx4 v72, s[20:23], s64 offen offset:0 lds\n"
      "buffer_load_dwordx4 v73, s[20:23], s64 offen offset:1024 lds\n"
      "buffer_load_dwordx4 v74, s[20:23], s64 offen offset:2048 lds\n"
      "buffer_load_dwordx4 v75, s[20:23], s64 offen offset:3072 lds\n"
      "s_waitcnt vmcnt(37)\n"
      "ds_read_b64_tr_b16 a[224:225], v78 offset:0\n"
      "ds_read_b64_tr_b16 a[226:227], v78 offset:1024\n"
      "ds_read_b64_tr_b16 a[228:229], v79 offset:0\n"
      "ds_read_b64_tr_b16 a[230:231], v79 offset:1024\n"
      "ds_read_b64_tr_b16 a[232:233], v78 offset:2048\n"
      "ds_read_b64_tr_b16 a[234:235], v78 offset:3072\n"
      "ds_read_b64_tr_b16 a[236:237], v79 offset:2048\n"
      "ds_read_b64_tr_b16 a[238:239], v79 offset:3072\n"
      "s_add_u32 s64, s76, 17\n"
      "s_and_b32 s64, s64, 3\n"
      "s_mul_i32 s64, s64, 25088\n"
      "s_mov_b32 m0, s66\n"
      "s_waitcnt lgkmcnt(0)\n"
      "buffer_load_dwordx4 v72, s[20:23], s64 offen offset:0 lds\n"
      "buffer_load_dwordx4 v73, s[20:23], s64 offen offset:1024 lds\n"
      "buffer_load_dwordx4 v74, s[20:23], s64 offen offset:2048 lds\n"
      "buffer_load_dwordx4 v75, s[20:23], s64 offen offset:3072 lds\n"
      "s_waitcnt vmcnt(8)\n"
      "v_cmp_ne_u64 vcc, v[66:67], v[16:17]\n"
      "s_cbranch_vccz .Lk_ok10\n"
      "s_mov_b32 s52, 20000\n"
      "s_nop 2\n"
      ".Lk_spin10:\n"
      "s_sleep 2\n"
      "buffer_load_dwordx2 v[66:67], v68, s[44:47], s72 offen sc1\n"
      "s_waitcnt vmcnt(0)\n"
      "v_cmp_ne_u64 vcc, v[66:67], v[16:17]\n"
      "s_cbranch_vccz .Lk_ok10\n"
      "s_sub_u32 s52, s52, 1\n"
      "s_cbranch_scc0 .Lk_spin10\n"
      "s_nop 3\n"
      ".Lk_ok10:\n"
      "s_lshl_b32 s52, s58, 12\n"
      "s_add_u32 s52, s52, 7634944\n"
      "buffer_load_dwordx4 v[180:183], v36, s[44:47], s52 offen offset:0 sc1\n"
      "buffer_load_dwordx4 v[184:187], v36, s[44:47], s52 offen offset:1024 sc1\n"
      "buffer_load_dwordx4 v[188:191], v36, s[44:47], s52 offen offset:2048 sc1\n"
      "buffer_load_dwordx4 v[192:195], v36, s[44:47], s52 offen offset:3072 sc1\n"
      "s_waitcnt vmcnt(3)\n"
      "v_mfma_f32_32x32x16_bf16 v[196:211], v[180:183], v[228:231], 0\n"
      "s_waitcnt vmcnt(2)\n"
      "v_mfma_f32_32x32x16_bf16 v[196:211], v[184:187], v[232:235], v[196:211]\n"
      "s_waitcnt vmcnt(1)\n"
      "v_mfma_f32_32x32x16_bf16 v[196:211], v[188:191], v[236:239], v[196:211]\n"
      "s_waitcnt vmcnt(0)\n"
      "v_mfma_f32_32x32x16_bf16 v[196:211], v[192:195], v[240:243], v[196:211]\n"
      "s_lshl_b32 s52, s57, 2\n"
      "s_add_u32 s52, s52, s50\n"
      "s_lshl_b32 s52, s52, 4\n"
      "s_add_u32 s52, s52, s58\n"
      "s_lshl_b32 s52, s52, 11\n"
      "s_add_u32 s52, s52, 3440640\n"
      "v_cmp_eq_u32 vcc, 0, v11\n"
      "s_nop 4\n"
      "v_mov_b32 v2, v208\n"
      "v_mov_b32 v3, v208\n"
      "v_cndmask_b32 v208, 0, v208, vcc\n"
      "v_cndmask_b32 v209, 0, v209, vcc\n"
      "v_cndmask_b32 v210, 0, v210, vcc\n"
      "v_cndmask_b32 v211, 0, v211, vcc\n"
      "v_permlane32_swap_b32 v2, v3\n"
      "v_add_f32 v3, 0x358637bd, v3\n"
      "v_rcp_f32 v180, v3\n"
      "s_nop 0\n"
      "v_pk_mul_f32 v[196:197], v[196:197], v[180:181] op_sel_hi:[1,0]\n"
      "v_pk_mul_f32 v[198:199], v[198:199], v[180:181] op_sel_hi:[1,0]\n"
      "v_pk_mul_f32 v[200:201], v[200:201], v[180:181] op_sel_hi:[1,0]\n"
      "v_pk_mul_f32 v[202:203], v[202:203], v[180:181] op_sel_hi:[1,0]\n"
      "v_pk_mul_f32 v[204:205], v[204:205], v[180:181] op_sel_hi:[1,0]\n"
      "v_pk_mul_f32 v[206:207], v[206:207], v[180:181] op_sel_hi:[1,0]\n"
      "v_pk_mul_f32 v[208:209], v[208:209], v[180:181] op_sel_hi:[1,0]\n"
      "v_pk_mul_f32 v[210:211], v[210:211], v[180:181] op_sel_hi:[1,0]\n"
      "v_cvt_pk_bf16_f32 v184, v196, v197\n"
      "v_cvt_pk_bf16_f32 v185, v198, v199\n"
      "v_cvt_pk_bf16_f32 v186, v200, v201\n"
      "v_cvt_pk_bf16_f32 v187, v202, v203\n"
      "v_cvt_pk_bf16_f32 v188, v204, v205\n"
      "v_cvt_pk_bf16_f32 v189, v206, v207\n"
      "v_cvt_pk_bf16_f32 v190, v208, v209\n"
      "v_cvt_pk_bf16_f32 v191, v210, v211\n"
      "buffer_store_dwordx4 v[184:187], v36, s[44:47], s52 offen offset:0 sc1\n"
      "buffer_store_dwordx4 v[188:191], v36, s[44:47], s52 offen offset:1024 sc1\n"
      "s_lshl_b32 s73, s76, 2\n"
      "s_add_u32 s73, s73, s50\n"
      "s_mul_i32 s73, s73, 224\n"
      "s_add_u32 s73, s73, 9134080\n"
      "v_min_u32 v69, 27, v1\n"
      "v_mul_u32_u24 v69, 8, v69\n"
      "buffer_load_dwordx2 v[70:71], v69, s[44:47], s73 offen sc1\n"
      "s_waitcnt vmcnt(0)\n"
      "s_lshl_b32 s52, s57, 1\n"
      "s_lshr_b32 s53, s50, 1\n"
      "s_add_u32 s52, s52, s53\n"
      "s_lshl_b32 s52, s52, 2\n"
      "s_lshr_b32 s53, s58, 2\n"
      "s_add_u32 s52, s52, s53\n"
      "s_lshl_b32 s52, s52, 7\n"
      "s_and_b32 s53, s50, 1\n"
      "s_lshl_b32 s53, s53, 5\n"
      "s_add_u32 s52, s52, s53\n"
      "s_and_b32 s53, s58, 3\n"
      "s_lshl_b32 s53, s53, 3\n"
      "s_add_u32 s52, s52, s53\n"
      "s_add_u32 s52, s52, 9117696\n"
      "buffer_store_dwordx2 v[16:17], v37, s[44:47], s52 offen sc1\n"
      "s_branch .Lk_g2\n"
      ".Lk_last:\n"
      :
      : "s"((unsigned long long)(size_t)__builtin_amdgcn_kernarg_segment_ptr()), "s"(blockIdx.x), "v"(threadIdx.x),
        "s"((unsigned)(size_t)(__attribute__((address_space(3))) char*)smem)
      : "memory", "vcc", "scc", "m0",
        "v1", "v2", "v3", "v4", "v5", "v6", "v7", "v8", "v9", "v10", "v11", "v12", "v13", "v14", "v15", "v16", "v17", "v18",
        "v19", "v20", "v21", "v22", "v23", "v24", "v25", "v26", "v27", "v28", "v29", "v30", "v31", "v32", "v33", "v34", "v35",
        "v36", "v37", "v38", "v39", "v40", "v41", "v42", "v43", "v44", "v45", "v46", "v47", "v48", "v49", "v50", "v51", "v52",
        "v53", "v54", "v55", "v56", "v57", "v58", "v59", "v60", "v61", "v62", "v63", "v64", "v65", "v66", "v67", "v68", "v69",
        "v70", "v71", "v72", "v73", "v74", "v75", "v76", "v77", "v78", "v79", "v80", "v81", "v82", "v83", "v84", "v85", "v86",
        "v87", "v88", "v89", "v90", "v91", "v92", "v93", "v94", "v95", "v96", "v97", "v98", "v99", "v100", "v101", "v102",
        "v103", "v104", "v105", "v106", "v107", "v108", "v109", "v110", "v111", "v112", "v113", "v114", "v115", "v116", "v117",
        "v118", "v119", "v120", "v121", "v122", "v123", "v124", "v125", "v126", "v127", "v128", "v129", "v130", "v131", "v132",
        "v133", "v134", "v135", "v136", "v137", "v138", "v139", "v140", "v141", "v142", "v143", "v144", "v145", "v146", "v147",
        "v148", "v149", "v150", "v151", "v152", "v153", "v154", "v155", "v156", "v157", "v158", "v159", "v160", "v161", "v162",
        "v163", "v164", "v165", "v166", "v167", "v168", "v169", "v170", "v171", "v172", "v173", "v174", "v175", "v176", "v177",
        "v178", "v179", "v180", "v181", "v182", "v183", "v184", "v185", "v186", "v187", "v188", "v189", "v190", "v191", "v192",
        "v193", "v194", "v195", "v196", "v197", "v198", "v199", "v200", "v201", "v202", "v203", "v204", "v205", "v206", "v207",
        "v208", "v209", "v210", "v211", "v212", "v213", "v214", "v215", "v216", "v217", "v218", "v219", "v220", "v221", "v222",
        "v223", "v224", "v225", "v226", "v227", "v228", "v229", "v230", "v231", "v232", "v233", "v234", "v235", "v236", "v237",
        "v238", "v239", "v240", "v241", "v242", "v243", "v244", "v245", "v246", "v247", "v248", "v249", "v250", "v251", "v252",
        "v253", "v254", "v255", "a0", "a1", "a2", "a3", "a4", "a5", "a6", "a7", "a8", "a9", "a10", "a11", "a12", "a13", "a14",
        "a15", "a16", "a17", "a18", "a19", "a20", "a21", "a22", "a23", "a24", "a25", "a26", "a27", "a28", "a29", "a30", "a31",
        "a32", "a33", "a34", "a35", "a36", "a37", "a38", "a39", "a40", "a41", "a42", "a43", "a44", "a45", "a46", "a47", "a48",
        "a49", "a50", "a51", "a52", "a53", "a54", "a55", "a56", "a57", "a58", "a59", "a60", "a61", "a62", "a63", "a64", "a65",
        "a66", "a67", "a68", "a69", "a70", "a71", "a72", "a73", "a74", "a75", "a76", "a77", "a78", "a79", "a80", "a81", "a82",
        "a83", "a84", "a85", "a86", "a87", "a88", "a89", "a90", "a91", "a92", "a93", "a94", "a95", "a96", "a97", "a98", "a99",
        "a100", "a101", "a102", "a103", "a104", "a105", "a106", "a107", "a108", "a109", "a110", "a111", "a112", "a113", "a114",
        "a115", "a116", "a117", "a118", "a119", "a120", "a121", "a122", "a123", "a124", "a125", "a126", "a127", "a128", "a129",
        "a130", "a131", "a132", "a133", "a134", "a135", "a136", "a137", "a138", "a139", "a140", "a141", "a142", "a143", "a144",
        "a145", "a146", "a147", "a148", "a149", "a150", "a151", "a152", "a153", "a154", "a155", "a156", "a157", "a158", "a159",
        "a160", "a161", "a162", "a163", "a164", "a165", "a166", "a167", "a168", "a169", "a170", "a171", "a172", "a173", "a174",
        "a175", "a176", "a177", "a178", "a179", "a180", "a181", "a182", "a183", "a184", "a185", "a186", "a187", "a188", "a189",
        "a190", "a191", "a192", "a193", "a194", "a195", "a196", "a197", "a198", "a199", "a200", "a201", "a202", "a203", "a204",
        "a205", "a206", "a207", "a208", "a209", "a210", "a211", "a212", "a213", "a214", "a215", "a216", "a217", "a218", "a219",
        "a220", "a221", "a222", "a223", "a224", "a225", "a226", "a227", "a228", "a229", "a230", "a231", "a232", "a233", "a234",
        "a235", "a236", "a237", "a238", "a239", "a240", "a241", "a242", "a243", "a244", "a245", "a246", "a247", "a248", "a249",
        "a250", "a251", "a252", "a253", "a254", "a255", "s4", "s5", "s6", "s7", "s8", "s9", "s10", "s11", "s12", "s13", "s14",
        "s15", "s16", "s17", "s18", "s19", "s20", "s21", "s22", "s23", "s24", "s25", "s26", "s27", "s28", "s29", "s30", "s31",
        "s32", "s33", "s34", "s35", "s36", "s37", "s38", "s39", "s40", "s41", "s42", "s43", "s44", "s45", "s46", "s47", "s48",
        "s49", "s50", "s51", "s52", "s53", "s54", "s55", "s56", "s57", "s58", "s59", "s60", "s61", "s62", "s63", "s64", "s65",
        "s66", "s67", "s68", "s69", "s70", "s71", "s72", "s73", "s74", "s75", "s76", "s77", "s78", "s79", "s80", "s81", "s82",
        "s83", "s84", "s85", "s86", "s87", "s88", "s89", "s90", "s91", "s92", "s93", "s94", "s95", "s96", "s97", "s98", "s99");
}

extern "C" void kernel_launch(void* const* d_in, const int* in_sizes, int n_in, void* d_out, int out_size, void* d_ws, size_t ws_size,
                              hipStream_t stream) {
  const unsigned c = 0x00020000u;
  const Args a = {{{d_in[0], 2048u * 448 * 2, c}, {d_in[1], 448u * 1344 * 2, c}, {d_in[2], 16u * 28 * 28 * 2, c}, {d_in[3], 448u * 448 * 2, c},
                   {d_in[5], 2048u * 1792 * 2, c}, {d_in[6], 1792u * 2, c}, {d_in[7], 1792u * 2048 * 2, c}, {d_in[8], 2048u * 2, c},
                   {d_out, 2048u * 448 * 2, c}, {d_ws, 9146368u, c}}};
  fused<<<256, 256, 0, stream>>>(a);
}
```
